```python
import torch

def _softmax(x, axis=-1):
    return torch.softmax(x, dim=axis)
def _gelu(x):
    return torch.nn.functional.gelu(x, approximate="tanh")
def _silu(x):
    return torch.nn.functional.silu(x)
def _sigmoid(x):
    return torch.sigmoid(x)
def _layernorm(x, axis=-1, eps=1e-5):
    m = x.mean(dim=axis, keepdim=True)
    v = x.var(dim=axis, keepdim=True, unbiased=False)
    return (x - m) / torch.sqrt(v + eps)
def _scatter_add(tgt, idx, src, axis=0):
    return tgt.clone().index_add_(axis, idx, src)
def _segment_sum(src, seg, n):
    out = torch.zeros((n,) + src.shape[1:], dtype=src.dtype)
    return out.index_add_(0, seg, src)
def _pad(x, pw):
    flat = tuple(v for pair in reversed(pw) for v in pair)
    return torch.nn.functional.pad(x, flat)

kernel_name = 'gated_mix__ssm_conv_s4__dense_activation_squared_relu__prompt'

def setup_inputs():

    _w = (lambda a: (lambda t: t.to(torch.bfloat16) if t.is_floating_point() else t)(torch.as_tensor(a)))
    def _A_setup():
        B, L, D, N = 1, 2048, 512, 128
        A_re = -(lambda _x: torch.exp(torch.as_tensor(_x)))((lambda *s: torch.randn(*s, dtype=torch.bfloat16) * 6.4)(D, N))
        A_im = torch.pi * (lambda *s: torch.randn(*s, dtype=torch.bfloat16) * 0.1)(D, N)
        B_re = (lambda *s: torch.randn(*s, dtype=torch.bfloat16) * 0.2)(D, N)
        B_im = (lambda *s: torch.randn(*s, dtype=torch.bfloat16) * 0.2)(D, N)
        C_re = (lambda *s: torch.randn(*s, dtype=torch.bfloat16) * 0.2)(D, N)
        C_im = (lambda *s: torch.randn(*s, dtype=torch.bfloat16) * 0.2)(D, N)
        log_dt = (lambda *s: torch.randn(*s, dtype=torch.bfloat16) * 0.1)(D) * 0.5 - 4.0
        Dskip = (lambda *s: torch.randn(*s, dtype=torch.bfloat16) * 0.1)(D)
        return {
            "u": _w((lambda *s: torch.randn(*s, dtype=torch.bfloat16) * 0.05)(B, L, D)),
            "A_re": _w(A_re),
            "A_im": _w(A_im),
            "B_re": _w(B_re),
            "B_im": _w(B_im),
            "C_re": _w(C_re),
            "C_im": _w(C_im),
            "log_dt": _w(log_dt),
            "Dskip": _w(Dskip),
        }
    _A_in = _A_setup()

    def _B_setup():
        B, S, D, F = 1, 2048, 512, 1792
        return {
            "x": _w((lambda *s: torch.randn(*s, dtype=torch.bfloat16) * 0.05)(B, S, D)),
            "w1": _w((lambda *s: torch.randn(*s, dtype=torch.bfloat16) * 0.05)(D, F)),
            "b1": _w((lambda *s: torch.randn(*s, dtype=torch.bfloat16) * 0.05)(F)),
            "w2": _w((lambda *s: torch.randn(*s, dtype=torch.bfloat16) * 0.05)(F, D)),
            "b2": _w((lambda *s: torch.randn(*s, dtype=torch.bfloat16) * 0.05)(D)),
        }
    _B_in = _B_setup()

    return {
        "A_u": _A_in["u"],
        "A_A_re": _A_in["A_re"],
        "A_A_im": _A_in["A_im"],
        "A_B_re": _A_in["B_re"],
        "A_B_im": _A_in["B_im"],
        "A_C_re": _A_in["C_re"],
        "A_C_im": _A_in["C_im"],
        "A_log_dt": _A_in["log_dt"],
        "A_Dskip": _A_in["Dskip"],
        "B_x": _B_in["x"],
        "B_w1": _B_in["w1"],
        "B_b1": _B_in["b1"],
        "B_w2": _B_in["w2"],
        "B_b2": _B_in["b2"],
        "gate_w": _w((lambda *s: torch.randn(*s, dtype=torch.bfloat16) * 0.05)(512) * 16.0),
    }

def reference(A_u, A_A_re, A_A_im, A_B_re, A_B_im, A_C_re, A_C_im, A_log_dt, A_Dskip, B_x, B_w1, B_b1, B_w2, B_b2, gate_w):
    def _A_ref(u, A_re, A_im, B_re, B_im, C_re, C_im, log_dt, Dskip):
        Bsz, L, D = u.shape
        dt = torch.exp(log_dt)[:, None]
        A = A_re + 1j * A_im
        Bc = B_re + 1j * B_im
        Cc = C_re + 1j * C_im
        logdA = dt * A
        dB = dt * Bc
        l = (lambda x, dt: x.to(dt))(torch.arange(L), A_re.dtype)
        P = torch.exp(logdA[:, :, None] * l[None, None, :])
        K = torch.einsum("dn,dnl,dn->dl", Cc, P, dB).real
        L2 = 2 * L
        Kp = _pad(K, ((0, 0), (0, L)))
        up = (lambda x, *d: x.permute(*d))(u, 0, 2, 1)
        up = _pad(up, ((0, 0), (0, 0), (0, L)))
        Kf = (lambda x, axes=(-2,-1): torch.fft.rfft2(x.float(), dim=axes))(Kp[:, None, :], axes=(-2, -1))
        Uf = (lambda x, axes=(-2,-1): torch.fft.rfft2(x.float(), dim=axes))(up[:, :, None, :], axes=(-2, -1))
        Yf = Uf * Kf[None, :, :, :]
        y = (lambda x, s, axes=(-2,-1): torch.fft.irfft2(x, s=s, dim=axes).to(torch.bfloat16))(Yf, (1, L2), axes=(-2, -1))[:, :, 0, :L]
        y = (lambda x, *d: x.permute(*d))(y, 0, 2, 1) + u * Dskip[None, None, :]
        return y

    def _B_ref(x, w1, b1, w2, b2):
        h = torch.einsum("bsd,df->bsf", x, w1) + b1
        zero = torch.zeros_like(h)
        r = torch.where(h > zero, h, zero)
        h = r * r
        out = torch.einsum("bsf,fd->bsd", h, w2) + b2
        return out

    x = A_u
    g = _sigmoid((x * gate_w).sum(-1, keepdims=True))
    a = _A_ref(x, A_A_re, A_A_im, A_B_re, A_B_im, A_C_re, A_C_im, A_log_dt, A_Dskip)
    b = _B_ref(x, B_w1, B_b1, B_w2, B_b2)
    return g * a + (1 - g) * b
```

```cpp
#include <hip/hip_runtime.h>
#include <hip/hip_bf16.h>
#include <stdint.h>

typedef unsigned short u16;
constexpr int L = 2048, D = 512, N = 128, F = 1792;

__device__ __forceinline__ float bf2f(u16 x) { return __uint_as_float(((unsigned)x) << 16); }
__device__ __forceinline__ u16 f2bf(float f) { __bf16 b = (__bf16)f; return __builtin_bit_cast(u16, b); }
__device__ __forceinline__ float rbf(float f) { return bf2f(f2bf(f)); }

__global__ void kgen(const u16* Are, const u16* Aim, const u16* Bre, const u16* Bim, const u16* Cre, const u16* Cim,
                     const u16* logdt, float* K) {
  int d = blockIdx.x;
  float dt = rbf(expf(bf2f(logdt[d])));
  for (int l = threadIdx.x; l < L; l += blockDim.x) {
    float lf = rbf((float)l);
    float acc = 0.f;
    for (int n = 0; n < N; ++n) {
      int i = d * N + n;
      float zr = dt * bf2f(Are[i]), zi = dt * bf2f(Aim[i]);
      float br = dt * bf2f(Bre[i]), bi = dt * bf2f(Bim[i]);
      float cr = bf2f(Cre[i]), ci = bf2f(Cim[i]);
      float wr = cr * br - ci * bi, wi = cr * bi + ci * br;
      float e = expf(zr * lf), s, c;
      sincosf(zi * lf, &s, &c);
      acc += e * (wr * c - wi * s);
    }
    K[d * L + l] = acc;
  }
}

__global__ void conv(const u16* u, const float* K, const u16* Dskip, float* a) {
  int d = blockIdx.x;
  __shared__ float us[L], ks[L];
  for (int t = threadIdx.x; t < L; t += blockDim.x) { us[t] = bf2f(u[t * D + d]); ks[t] = K[d * L + t]; }
  __syncthreads();
  float ds = bf2f(Dskip[d]);
  for (int t = threadIdx.x; t < L; t += blockDim.x) {
    float acc = 0.f;
    for (int j = 0; j <= t; ++j) acc += ks[j] * us[t - j];
    a[t * D + d] = rbf(rbf(acc) + rbf(us[t] * ds));
  }
}

__global__ void gemm1(const u16* x, const u16* w1, const u16* b1, u16* h) {
  int t = blockIdx.x;
  __shared__ float xs[D];
  for (int k = threadIdx.x; k < D; k += blockDim.x) xs[k] = bf2f(x[t * D + k]);
  __syncthreads();
  for (int f = threadIdx.x; f < F; f += blockDim.x) {
    float acc = 0.f;
    for (int k = 0; k < D; ++k) acc += xs[k] * bf2f(w1[k * F + f]);
    float v = rbf(rbf(acc) + bf2f(b1[f]));
    v = v > 0.f ? v : 0.f;
    h[t * F + f] = f2bf(v * v);
  }
}

__global__ void gemm2(const u16* x, const u16* gw, const u16* h, const u16* w2, const u16* b2, const float* a, u16* out) {
  int t = blockIdx.x;
  __shared__ float hs[F];
  __shared__ float red[256];
  for (int k = threadIdx.x; k < F; k += blockDim.x) hs[k] = bf2f(h[t * F + k]);
  float p = 0.f;
  for (int k = threadIdx.x; k < D; k += blockDim.x) p += rbf(bf2f(x[t * D + k]) * bf2f(gw[k]));
  red[threadIdx.x] = p;
  __syncthreads();
  float s = 0.f;
  for (int i = 0; i < 256; ++i) s += red[i];
  s = rbf(s);
  float g = rbf(1.f / (1.f + expf(-s)));
  for (int d = threadIdx.x; d < D; d += blockDim.x) {
    float acc = 0.f;
    for (int k = 0; k < F; ++k) acc += hs[k] * bf2f(w2[k * D + d]);
    float b = rbf(rbf(acc) + bf2f(b2[d]));
    out[t * D + d] = f2bf(rbf(g * a[t * D + d]) + rbf(rbf(1.f - g) * b));
  }
}

extern "C" void kernel_launch(void* const* d_in, const int* in_sizes, int n_in,
                              void* d_out, int out_size, void* d_ws, size_t ws_size,
                              hipStream_t stream) {
  const u16* const* in = (const u16* const*)d_in;
  float* K = (float*)d_ws;
  float* a = K + D * L;
  u16* h = (u16*)(a + D * L);
  kgen<<<D, 256, 0, stream>>>(in[1], in[2], in[3], in[4], in[5], in[6], in[7], K);
  conv<<<D, 256, 0, stream>>>(in[0], K, in[8], a);
  gemm1<<<L, 256, 0, stream>>>(in[0], in[10], in[11], h);
  gemm2<<<L, 256, 0, stream>>>(in[0], in[14], h, in[12], in[13], a, (u16*)d_out);
}
```

```cpp
#include <hip/hip_runtime.h>
#include <stdint.h>
#include <type_traits>
#pragma clang diagnostic ignored "-Winline-asm"

typedef unsigned short u16;
typedef unsigned int u32;
typedef unsigned long long u64;
typedef __attribute__((ext_vector_type(2))) u32 v2u;
typedef __attribute__((ext_vector_type(3))) u32 v3u;
typedef __attribute__((ext_vector_type(4))) u32 v4u;
typedef __attribute__((ext_vector_type(2))) float f2;
typedef __attribute__((ext_vector_type(4))) float f4;
typedef __attribute__((ext_vector_type(16))) float f16v;
typedef __attribute__((ext_vector_type(2))) __bf16 bf2;
typedef __attribute__((ext_vector_type(8))) __bf16 bf8;
typedef __attribute__((ext_vector_type(4))) short s4;
typedef __amdgpu_buffer_rsrc_t rsrc_t;
#define DI __device__ __forceinline__
#define LAS __attribute__((address_space(3)))
#define CAS __attribute__((address_space(4)))

#define MAP1 1
#define PDEPTH 9
#define MAP2 0

constexpr u32 FSP = 256;
constexpr u32 WS_G = 4096, WS_GFLAG = 32768, WS_HFLAG = 65536, WS_UFLAG = WS_HFLAG + 1024 * FSP, WS_AFLAG = WS_UFLAG + 1024 * FSP;
constexpr u32 WS_UT = 1u << 20, WS_A = 3u << 20, WS_H = 5u << 20, WS_END = 13u << 20;
constexpr int SPIN_MAX = 1 << 16;

#define STAMP(i)
#define STAMP_DECL
#define STAMP_OUT

DI rsrc_t mkrsrc(const void* p, u32 bytes) { return __builtin_amdgcn_make_buffer_rsrc((void*)p, (short)0, (int)bytes, 0x00020000); }
DI float bflo(u32 v) { return __uint_as_float(v << 16); }
DI float bfhi(u32 v) { return __uint_as_float(v & 0xffff0000u); }
DI u32 pk2(float a, float b) { bf2 r = __builtin_convertvector((f2){a, b}, bf2); return __builtin_bit_cast(u32, r); }
DI float relu(float v) { return __int_as_float(max(__float_as_int(v), 0)); }
DI void fence_sched() { __builtin_amdgcn_sched_barrier(0); }

template <int N> DI void wait_vm() { asm volatile("s_waitcnt vmcnt(%0)" :: "n"(N) : "memory"); }
template <int N> DI void wait_vm_bar() { asm volatile("s_waitcnt vmcnt(%0) lgkmcnt(0)\n\ts_barrier" :: "n"(N) : "memory"); }
DI void lds_bar() { asm volatile("s_waitcnt lgkmcnt(0)\n\ts_barrier" ::: "memory"); }
DI void wait_lgkm() { asm volatile("s_waitcnt lgkmcnt(0)" ::: "memory"); }

DI void dma2(u32 m0v, u32 v0, u32 v1, rsrc_t rs, u32 so) {
  asm volatile("s_mov_b32 m0, %0\n\ts_nop 0\n\t"
               "buffer_load_dwordx4 %1, %3, %4 offen lds\n\t"
               "buffer_load_dwordx4 %2, %3, %4 offen offset:1024 lds"
               :: "s"(m0v), "v"(v0), "v"(v1), "s"(rs), "s"(so) : "m0", "memory");
}
DI void dma4(u32 m0v, u32 v0, u32 v1, u32 v2, u32 v3, rsrc_t rs, u32 so) {
  asm volatile("s_mov_b32 m0, %0\n\ts_nop 0\n\t"
               "buffer_load_dwordx4 %1, %5, %6 offen lds\n\t"
               "buffer_load_dwordx4 %2, %5, %6 offen offset:1024 lds\n\t"
               "buffer_load_dwordx4 %3, %5, %6 offen offset:2048 lds\n\t"
               "buffer_load_dwordx4 %4, %5, %6 offen offset:3072 lds"
               :: "s"(m0v), "v"(v0), "v"(v1), "v"(v2), "v"(v3), "s"(rs), "s"(so) : "m0", "memory");
}
template <int OFF> DI v4u ld128_sc1(u32 vo, rsrc_t rs, u32 so) {
  v4u r; asm volatile("buffer_load_dwordx4 %0, %1, %2, %3 offen offset:%4 sc1" : "=&v"(r) : "v"(vo), "s"(rs), "s"(so), "n"(OFF) : "memory"); return r;
}
template <int OFF> DI u32 ld32_sc1(u32 vo, rsrc_t rs, u32 so) {
  u32 r; asm volatile("buffer_load_dword %0, %1, %2, %3 offen offset:%4 sc1" : "=&v"(r) : "v"(vo), "s"(rs), "s"(so), "n"(OFF) : "memory"); return r;
}
template <int OFF> DI v2u ld64(u32 vo, rsrc_t rs) {
  v2u r; asm volatile("buffer_load_dwordx2 %0, %1, %2, 0 offen offset:%3" : "=&v"(r) : "v"(vo), "s"(rs), "n"(OFF) : "memory"); return r;
}
template <int OFF> DI void st128_sc1(v4u d, u32 vo, rsrc_t rs, u32 so) {
  asm volatile("buffer_store_dwordx4 %0, %1, %2, %3 offen offset:%4 sc1\n\ts_nop 1" :: "v"(d), "v"(vo), "s"(rs), "s"(so), "n"(OFF) : "memory");
}
DI void st32_sc1(u32 d, u32 vo, rsrc_t rs) {
  asm volatile("buffer_store_dword %0, %1, %2, 0 offen sc1" :: "v"(d), "v"(vo), "s"(rs) : "memory");
}
DI void flag_store(u32 off, rsrc_t rs, u64 tag, int lane) {
  if (lane == 0) {
    v2u d = {(u32)tag, (u32)(tag >> 32)};
    asm volatile("buffer_store_dwordx2 %0, %1, %2, 0 offen sc1\n\ts_nop 1" :: "v"(d), "v"(off), "s"(rs) : "memory");
  }
}
DI u64 epoch_issue(const char* ws) { u64 e; asm volatile("s_load_dwordx2 %0, %1, 0x0" : "=&s"(e) : "s"(ws) : "memory"); return e; }
DI u64 epoch_tag(u64 e) {
  asm volatile("s_waitcnt lgkmcnt(0)" : "+s"(e) :: "memory");
  return e * 0x9E3779B97F4A7C15ull + 0x632BE59BD9B4E019ull;
}
DI void flag_wait(u32 vo, rsrc_t rs, u64 tag) {
#pragma unroll 1
  for (int it = 0; it < SPIN_MAX; ++it) {
    v2u v; asm volatile("buffer_load_dwordx2 %0, %1, %2, 0 offen sc1\n\ts_waitcnt vmcnt(0)" : "=&v"(v) : "v"(vo), "s"(rs) : "memory");
    const bool bad = (v.x != (u32)tag) | (v.y != (u32)(tag >> 32));
    if (__builtin_amdgcn_ballot_w64(bad) == 0) break;
    __builtin_amdgcn_s_sleep(1);
  }
}

DI bf8 lds_rd128(LAS char* p) { return *(LAS bf8*)p; }
DI bf8 lds_tr2(LAS char* p0, LAS char* p1) {
  s4 a = __builtin_amdgcn_ds_read_tr16_b64_v4i16((LAS s4*)p0);
  s4 b = __builtin_amdgcn_ds_read_tr16_b64_v4i16((LAS s4*)p1);
  v2u x = __builtin_bit_cast(v2u, a), y = __builtin_bit_cast(v2u, b);
  return __builtin_bit_cast(bf8, (v4u){x.x, x.y, y.x, y.y});
}
DI f16v mfma32(bf8 a, bf8 b, f16v c) { return __builtin_amdgcn_mfma_f32_32x32x16_bf16(a, b, c, 0, 0, 0); }
DI f4 mfma16(bf8 a, bf8 b, f4 c) { return __builtin_amdgcn_mfma_f32_16x16x32_bf16(a, b, c, 0, 0, 0); }

template <int I, int N, class F> DI void static_for(F&& f) {
  if constexpr (I < N) { f(std::integral_constant<int, I>{}); static_for<I + 1, N>(f); }
}

constexpr u32 XS = 0, WSL = 49152;
constexpr u32 vblk(int i) {
  constexpr u32 t[10] = {XS + 32768, XS + 32768 + 2048, WSL, XS, XS + 2048, WSL + 8192, WSL + 16384, XS + 16384, XS + 16384 + 2048, WSL + 24576};
  return t[i];
}
constexpr bool vblk_x(int i) { return vblk(i) < WSL; }

struct Frag { bf8 a[2][2], b[2][2]; };

DI void gemm_role(int b, LAS char* sm, const u16* x, const u16* w1, const u16* b1, const u16* w2, const u16* b2, const u16* gw,
                  u16* out, char* ws) {
  const int lane = threadIdx.x & 63, wave = __builtin_amdgcn_readfirstlane(threadIdx.x >> 6);
  STAMP_DECL;
  STAMP(0);
  const int xcd = b & 7, r = b >> 3;
  const rsrc_t rws = mkrsrc(ws, WS_END), rx = mkrsrc(x, 2048 * 512 * 2), rw2 = mkrsrc(w2, 1792 * 512 * 2);
  const int g = lane >> 4, q = (lane >> 2) & 3, p = lane & 3, l31 = lane & 31, hh = lane >> 5;
  const u32 smb = (u32)(uintptr_t)sm;
  const int t2 = MAP2 ? 8 * (xcd >> 1) + (r >> 2) : 4 * xcd + (r >> 3);
  const int c2 = MAP2 ? 4 * (xcd & 1) + (r & 3) : (r & 7);

  const u32 vv0 = (u32)(448 * wave + (lane >> 3)) * 1024 + c2 * 128 + (((lane & 7) ^ (((lane >> 4) & 1) << 2)) << 4);
  const u32 vv1 = vv0 + 7168;
  const u32 mX = smb + wave * 4096, mW = smb + wave * 2048;
  auto vload = [&](auto BI, int ks) {
    constexpr int bi = decltype(BI)::value;
    dma2((vblk_x(bi) ? mX : mW) + vblk(bi), vv0, vv1, rw2, (u32)ks * 16384);
  };
#define VLOAD(bi, ks) vload(std::integral_constant<int, bi>{}, ks)
  u64 tag;

  if (r < 28) {
    const int tau = MAP1 ? 4 * (xcd >> 1) + r / 7 : 2 * xcd + r / 14;
    const int j = MAP1 ? 7 * (xcd & 1) + r % 7 : r % 14;
    const int rot = MAP1 ? (r % 7) + 2 * (r / 7) : (r % 14) + 4 * (r / 14);
    const int wf = wave >> 1, wt = wave & 1;
    const rsrc_t rw1 = mkrsrc(w1, 512 * 1792 * 2), rb1 = mkrsrc(b1, 1792 * 2);
    u32 vx[4], vw[2];
#pragma unroll
    for (int m = 0; m < 4; ++m)
      vx[m] = (u32)(tau * 128 + (wave * 4 + m) * 8 + (lane >> 3)) * 1024 + (((lane & 7) ^ ((4 * (m & 1) + g) & 7)) << 4) - 1024 * m;
#pragma unroll
    for (int m = 0; m < 2; ++m)
      vw[m] = (u32)((wave * 2 + m) * 4 + g) * 3584 + j * 256 + (((lane & 15) ^ (g << 2)) << 4) - 1024 * m;
    auto xload = [&](int s) { dma4(mX + XS + (s % 3) * 16384, vx[0], vx[1], vx[2], vx[3], rx, (u32)((s + rot) & 7) * 128); };
    auto wload = [&](int t) { dma2(mW + WSL + (t & 3) * 8192, vw[0], vw[1], rw1, (u32)((t + 2 * rot) & 15) * (32 * 3584)); };
    xload(0); wload(0);
    v2u bv[8];
    const u32 vb = (u32)(j * 128 + wf * 64 + 4 * hh) * 2;
    static_for<0, 8>([&](auto I) { constexpr int i = decltype(I)::value; bv[i] = ld64<i * 16>(vb, rb1); });
    wload(1); xload(1); wload(2); wload(3); xload(2);
    tag = epoch_tag(epoch_issue(ws));
    __builtin_amdgcn_s_setprio(2);

    LAS char* pa[2]; LAS char* pb[4];
#pragma unroll
    for (int mi = 0; mi < 2; ++mi)
      pa[mi] = sm + WSL + (8 * (g >> 1) + q) * 256 + (((8 * wf + 4 * mi + 2 * (g & 1) + (p >> 1)) ^ (q << 2)) << 4) + (p & 1) * 8;
#pragma unroll
    for (int s = 0; s < 4; ++s) pb[s] = sm + XS + (64 * wt + l31) * 128 + (((2 * s + hh) ^ ((l31 >> 1) & 7)) << 4);

    f16v acc[2][2];
    Frag fr[2];
    auto rd = [&](auto T) {
      constexpr int t = decltype(T)::value;
      Frag& f = fr[t & 1];
#pragma unroll
      for (int s = 0; s < 2; ++s) {
#pragma unroll
        for (int mi = 0; mi < 2; ++mi)
          f.a[mi][s] = lds_tr2(pa[mi] + (t & 3) * 8192 + s * 4096, pa[mi] + (t & 3) * 8192 + s * 4096 + 1024);
#pragma unroll
        for (int ni = 0; ni < 2; ++ni) f.b[ni][s] = lds_rd128(pb[2 * (t & 1) + s] + ((t >> 1) % 3) * 16384 + ni * 4096);
      }
    };
    auto mm = [&](auto T) {
      constexpr int t = decltype(T)::value;
      Frag& f = fr[t & 1];
#pragma unroll
      for (int s = 0; s < 2; ++s)
#pragma unroll
        for (int mi = 0; mi < 2; ++mi)
#pragma unroll
          for (int ni = 0; ni < 2; ++ni) acc[mi][ni] = mfma32(f.a[mi][s], f.b[ni][s], acc[mi][ni]);
    };
    STAMP(1);
    static_for<0, 17>([&](auto T) {
      constexpr int t = decltype(T)::value;
      fence_sched();
      if constexpr (t == 0) {
        asm volatile("s_waitcnt vmcnt(14)\n\ts_barrier"
                     : "+v"(bv[0]), "+v"(bv[1]), "+v"(bv[2]), "+v"(bv[3]), "+v"(bv[4]), "+v"(bv[5]), "+v"(bv[6]), "+v"(bv[7]) :: "memory");
      } else if constexpr (t == 16) lds_bar();
      else wait_vm_bar<(t & 1) ? 12 : 8>();
      fence_sched();
      if constexpr (t == 0) { STAMP(2); }
      if constexpr (t == 8) { STAMP(3); }
      if constexpr (t == 15) { STAMP(4); }
      if constexpr (t >= 1 && t + 3 < 16) wload(t + 3);
      if constexpr (t >= 2 && (t & 1) == 0 && t / 2 + 2 < 8) xload(t / 2 + 2);
      if constexpr (t == 12) { VLOAD(0, 0); VLOAD(1, 1); }
      if constexpr (t == 13) { VLOAD(2, 2); }
      if constexpr (t == 14) { VLOAD(3, 3); VLOAD(4, 4); VLOAD(5, 5); }
      if constexpr (t == 15) { VLOAD(6, 6); }
      if constexpr (t == 16) { VLOAD(7, 7); VLOAD(8, 8); VLOAD(9, 9); }
      if constexpr (t < 16) rd(T);
      if constexpr (t == 0) {
#pragma unroll
        for (int mi = 0; mi < 2; ++mi) {
          f16v c;
#pragma unroll
          for (int i = 0; i < 4; ++i) {
            c[4 * i] = bflo(bv[4 * mi + i].x); c[4 * i + 1] = bfhi(bv[4 * mi + i].x);
            c[4 * i + 2] = bflo(bv[4 * mi + i].y); c[4 * i + 3] = bfhi(bv[4 * mi + i].y);
          }
          acc[mi][0] = c; acc[mi][1] = c;
        }
      } else {
        mm(std::integral_constant<int, t - 1>{});
        if constexpr (t < 16) {
#pragma unroll
          for (int k = 0; k < 6; ++k) { __builtin_amdgcn_sched_group_barrier(0x008, 1, 0); __builtin_amdgcn_sched_group_barrier(0x100, 2, 0); }
          __builtin_amdgcn_sched_group_barrier(0x008, 2, 0);
        }
      }
    });
    const u32 vh = WS_H + (u32)((4 * tau + 2 * wt) * 112 + 8 * j + 4 * wf) * 1024 + lane * 16;
    static_for<0, 4>([&](auto I) {
      constexpr int mi = decltype(I)::value >> 1, ni = decltype(I)::value & 1;
#pragma unroll
      for (int s = 0; s < 2; ++s) {
        v4u d;
#pragma unroll
        for (int k = 0; k < 4; ++k) {
          f2 v = {relu(acc[mi][ni][8 * s + 2 * k]), relu(acc[mi][ni][8 * s + 2 * k + 1])};
          v = v * v;
          d[k] = pk2(v.x, v.y);
        }
        if (s == 0) st128_sc1<mi * 2048>(d, vh, rws, ni * 114688); else st128_sc1<mi * 2048 + 1024>(d, vh, rws, ni * 114688);
      }
    });
    STAMP(5);
    __builtin_amdgcn_s_setprio(0);
    wait_vm<8>();
  } else {
    const int tg = xcd * 4 + (r - 28);
    const v4u gv = *(const v4u*)(gw + lane * 8);
    const u16* xr = x + (size_t)(tg * 64 + wave * 16) * 512 + lane * 8;
    v4u xv[16];
#pragma unroll
    for (int i = 0; i < 16; ++i) xv[i] = *(const v4u*)(xr + i * 512);
    tag = epoch_tag(epoch_issue(ws));
    float mine = 0.f;
#pragma unroll
    for (int i = 0; i < 16; ++i) {
      float s = 0.f;
#pragma unroll
      for (int k = 0; k < 4; ++k) { s += bflo(xv[i][k]) * bflo(gv[k]); s += bfhi(xv[i][k]) * bfhi(gv[k]); }
#pragma unroll
      for (int o = 32; o >= 1; o >>= 1) s += __shfl_xor(s, o);
      if ((lane & 15) == i) mine = s;
    }
    const float sl = bflo(pk2(mine, 0.f));
    const float gt = bflo(pk2(1.f / (1.f + __expf(-sl)), 0.f));
    if (lane < 16) st32_sc1(__float_as_uint(gt), WS_G + (u32)(tg * 64 + wave * 16 + lane) * 4, rws);
    wait_vm<0>();
    flag_store(WS_GFLAG + (u32)(tg * 4 + wave) * FSP, rws, tag, lane);
    static_for<0, 10>([&](auto I) { vload(I, decltype(I)::value); });
    wait_vm<0>();
  }

  LAS char* pv[2][2];
#pragma unroll
  for (int mi = 0; mi < 2; ++mi) {
    const u32 o = (4 * (g >> 1) + q) * 128 + (((4 * (mi ^ (wave >> 1)) + 2 * (g & 1) + (p >> 1)) ^ (((q >> 1) & 1) << 2)) << 4) + (p & 1) * 8;
    pv[mi][0] = sm + WSL + wave * 2048 + o; pv[mi][1] = sm + wave * 4096 + o;
  }
  auto vfrag = [&](auto BI, int mi) {
    constexpr int bi = decltype(BI)::value;
    LAS char* a = vblk_x(bi) ? pv[mi][1] + vblk(bi) : pv[mi][0] + (vblk(bi) - WSL);
    return lds_tr2(a, a + 1024);
  };
  bf8 wa[10][2];
  static_for<0, 10>([&](auto I) { constexpr int i = decltype(I)::value; wa[i][0] = vfrag(I, 0); wa[i][1] = vfrag(I, 1); });
  fence_sched();
  wait_lgkm();
  fence_sched();
  static_for<0, 10>([&](auto I) { vload(I, 10 + decltype(I)::value); });
  if (r < 28) {
    const int tau = MAP1 ? 4 * (xcd >> 1) + r / 7 : 2 * xcd + r / 14;
    const int j = MAP1 ? 7 * (xcd & 1) + r % 7 : r % 14;
    wait_vm<20>();
    STAMP(6);
    flag_store(WS_HFLAG + (u32)((2 * tau + (wave & 1)) * 32 + 2 * j + (wave >> 1)) * FSP, rws, tag, lane);
  }
  flag_wait(WS_HFLAG + (u32)(t2 * 32 + 7 * wave + (lane < 7 ? lane : 0)) * FSP, rws, tag);
  STAMP(7);
  __builtin_amdgcn_s_setprio(2);

  constexpr int PD = PDEPTH;
  u32 vhh[2];
#pragma unroll
  for (int ni = 0; ni < 2; ++ni) vhh[ni] = WS_H + (u32)((2 * t2 + (ni ^ (wave & 1))) * 112 + 28 * wave) * 1024 + lane * 16;
  v4u hb[28][2];
  auto ksof = [](int k) constexpr { return k < 10 ? 10 + k : (k < 20 ? k - 10 : k); };
  auto hload = [&](auto K) {
    constexpr int k = decltype(K)::value, ks = ksof(k);
    hb[k][0] = ld128_sc1<(ks & 3) * 1024>(vhh[0], rws, (ks >> 2) * 4096);
    hb[k][1] = ld128_sc1<(ks & 3) * 1024>(vhh[1], rws, (ks >> 2) * 4096);
  };
  static_for<0, PD>(hload);
  f16v o2[2][2];
  bf8 wb[28][2];
  wb[0][0] = vfrag(std::integral_constant<int, 0>{}, 0); wb[0][1] = vfrag(std::integral_constant<int, 0>{}, 1);
  static_for<0, 28>([&](auto K) {
    constexpr int k = decltype(K)::value;
    constexpr auto after_h = [](int kk) constexpr {
      int n = 0;
      if (kk < PD) n += 2 * (PD - 1 - kk);
      for (int i = (kk < PD ? 0 : kk - PD + 1); i < kk; ++i) n += ((i < 8) ? 2 : 0) + ((i + PD < 28) ? 2 : 0);
      return n;
    };
    fence_sched();
    asm volatile("s_waitcnt vmcnt(%2) lgkmcnt(0)" : "+v"(hb[k][0]), "+v"(hb[k][1]) : "n"(after_h(k)) : "memory");
    fence_sched();
    if constexpr (k == 0) { STAMP(8); }
    auto mf = [&](int mi, int ni) {
      const bf8 a = (k >= 10 && k < 20) ? wa[(k >= 10 && k < 20) ? k - 10 : 0][mi] : wb[k][mi];
      if constexpr (k == 0) o2[mi][ni] = mfma32(a, __builtin_bit_cast(bf8, hb[k][ni]), (f16v)(0.f));
      else o2[mi][ni] = mfma32(a, __builtin_bit_cast(bf8, hb[k][ni]), o2[mi][ni]);
      fence_sched();
    };
    mf(0, 0);
    if constexpr (k + 1 < 10) { wb[k + 1][0] = vfrag(std::integral_constant<int, k + 1>{}, 0); wb[k + 1][1] = vfrag(std::integral_constant<int, k + 1>{}, 1); }
    if constexpr (k + 1 >= 20 && k + 1 < 28) { wb[k + 1][0] = vfrag(std::integral_constant<int, k + 1 - 20>{}, 0); wb[k + 1][1] = vfrag(std::integral_constant<int, k + 1 - 20>{}, 1); }
    fence_sched();
    mf(1, 0);
    if constexpr (k < 8) VLOAD(k, 20 + k);
    mf(0, 1);
    mf(1, 1);
    if constexpr (k + PD < 28) hload(std::integral_constant<int, k + PD>{});
  });
  fence_sched();
  STAMP(9);
#pragma unroll
  for (int s = 1; s < 4; ++s)
#pragma unroll
    for (int k = 0; k < 4; ++k) {
      const f16v& a = o2[s >> 1][s & 1];
      *(LAS f4*)(sm + XS + (s - 1) * 16384 + wave * 4096 + k * 1024 + lane * 16) = (f4){a[4 * k], a[4 * k + 1], a[4 * k + 2], a[4 * k + 3]};
    }
  const int mi = wave >> 1, ni = wave & 1;
  flag_wait(lane < 16 ? WS_AFLAG + (u32)((t2 >> 3) * 256 + 32 * c2 + 16 * mi + lane) * FSP
                      : WS_GFLAG + (u32)(t2 * 4 + 2 * ni + (lane & 1)) * FSP, rws, tag);
  STAMP(10);
  u32 av[8];
  const u32 va = WS_A + (u32)t2 * 65536 + (u32)(32 * c2 + 16 * mi + 2 * hh) * 256 + (32 * ni + l31) * 4;
  static_for<0, 8>([&](auto I) { constexpr int i = decltype(I)::value; av[i] = ld32_sc1<(i >> 1) * 1024 + (i & 1) * 256>(va, rws, 0); });
  u32 gq = ld32_sc1<0>(WS_G + (u32)(64 * t2 + 32 * ni + l31) * 4, rws, 0);
  v2u b2v[4];
  const rsrc_t rb2 = mkrsrc(b2, 512 * 2);
  static_for<0, 4>([&](auto I) { constexpr int i = decltype(I)::value; b2v[i] = ld64<i * 16>((u32)(64 * c2 + 32 * mi + 4 * hh) * 2, rb2); });
  lds_bar();
  f16v o = o2[0][0];
#pragma unroll
  for (int s = 1; s < 4; ++s)
#pragma unroll
    for (int k = 0; k < 4; ++k) {
      const f4 v = *(LAS f4*)(sm + XS + (s - 1) * 16384 + (wave ^ s) * 4096 + k * 1024 + lane * 16);
      o[4 * k] += v.x; o[4 * k + 1] += v.y; o[4 * k + 2] += v.z; o[4 * k + 3] += v.w;
    }
  asm volatile("s_waitcnt vmcnt(0)"
               : "+v"(av[0]), "+v"(av[1]), "+v"(av[2]), "+v"(av[3]), "+v"(av[4]), "+v"(av[5]), "+v"(av[6]), "+v"(av[7]), "+v"(gq),
                 "+v"(b2v[0]), "+v"(b2v[1]), "+v"(b2v[2]), "+v"(b2v[3]) :: "memory");
  const float gt = __uint_as_float(gq), gn = 1.f - gt;
  LAS char* tp = sm + WSL + wave * 2048;
#pragma unroll
  for (int i = 0; i < 4; ++i) {
    const float r0 = gt * bflo(av[2 * i]) + gn * (o[4 * i] + bflo(b2v[i].x));
    const float r1 = gt * bfhi(av[2 * i]) + gn * (o[4 * i + 1] + bfhi(b2v[i].x));
    const float r2 = gt * bflo(av[2 * i + 1]) + gn * (o[4 * i + 2] + bflo(b2v[i].y));
    const float r3 = gt * bfhi(av[2 * i + 1]) + gn * (o[4 * i + 3] + bfhi(b2v[i].y));
    *(LAS v2u*)(tp + l31 * 64 + ((i ^ ((l31 >> 1) & 3)) << 4) + hh * 8) = (v2u){pk2(r0, r1), pk2(r2, r3)};
  }
#pragma unroll
  for (int k = 0; k < 2; ++k) {
    const int row = 16 * k + (lane >> 2);
    const v4u v = *(LAS v4u*)(tp + row * 64 + ((p ^ ((row >> 1) & 3)) << 4));
    *(v4u*)(out + (size_t)(64 * t2 + 32 * ni + row) * 512 + 64 * c2 + 32 * mi + p * 8) = v;
  }
  STAMP(11);
  STAMP_OUT;
}

DI f2 cmul(f2 a, f2 m, f2 mr) { return (f2){a.x, a.x} * m + (f2){a.y, a.y} * mr; }
DI f2 crot(f2 m) { return (f2){-m.y, m.x}; }
DI f2 cexp_s(float lr, float rv, float m) {
  const float e = __builtin_amdgcn_exp2f(lr * m), f = __builtin_amdgcn_fractf(rv * m);
  return (f2){e * __builtin_amdgcn_cosf(f), e * __builtin_amdgcn_sinf(f)};
}

constexpr u32 TROW = 544, TCH = 63 * TROW, XT = 69632;
constexpr u32 KCH = 9472, KOO = 4656, UA0 = 18944, UCH = 8768, UBO = 4224, EX = 40960;

DI void conv_role(int ci, LAS char* sm, const u16* x, const u16* are, const u16* aim, const u16* bre, const u16* bim,
                  const u16* cre, const u16* cim, const u16* logdt, const u16* dskip, char* ws) {
  const int tid = threadIdx.x, lane = tid & 63, wave = __builtin_amdgcn_readfirstlane(tid >> 6);
  STAMP_DECL;
  STAMP(0);
  const rsrc_t rws = mkrsrc(ws, WS_END), rx = mkrsrc(x, 2048 * 512 * 2);
  const u32 smb = (u32)(uintptr_t)sm;
  const int cg = ci & 7, tt = ci >> 3, pr = cg * 32 + tt;
  const int g = lane >> 4, q = (lane >> 2) & 3, p = lane & 3, n = lane & 15;
  const int ch = wave >> 1, half = wave & 1;

  {
    const u32 v0 = (u32)(64 * tt + 16 * wave + (lane >> 3)) * 1024 + cg * 128 + (((lane & 7) ^ ((g & 1) << 1)) << 4);
    const u32 v1 = (v0 ^ 64) + 7168;
    dma2(smb + XT + wave * 2048, v0, v1, rx, 0);
  }
  const int pi = pr * 256 + tid;
  u32 r_are = are[pi], r_aim = aim[pi], r_bre = bre[pi], r_bim = bim[pi], r_cre = cre[pi], r_cim = cim[pi];
  u32 r_ldt = logdt[2 * pr + ch];
  const u32 r_dsk = *(const u32*)(dskip + 2 * pr);
  fence_sched();
  const u64 tag = epoch_tag(epoch_issue(ws));
  asm volatile("s_waitcnt vmcnt(0)" : "+v"(r_are), "+v"(r_aim), "+v"(r_bre), "+v"(r_bim), "+v"(r_cre), "+v"(r_cim), "+v"(r_ldt) :: "memory");
  fence_sched();
  STAMP(1);
  {
    LAS char* tb = sm + XT + wave * 2048 + (8 * (p & 1) + q) * 128 + (p >> 1) * 8;
    const int sw = (q & 2) | ((p & 1) << 2);
    const u32 vo = WS_UT + (u32)((16 * cg + 2 * g + (q >> 1)) * 256 + 8 * tt + 2 * wave + (q & 1)) * 64 + p * 16;
    const bf8 d0 = lds_tr2(tb + ((g ^ sw) << 4), tb + ((g ^ sw) << 4) + 512);
    const bf8 d1 = lds_tr2(tb + (((4 + g) ^ sw) << 4), tb + (((4 + g) ^ sw) << 4) + 512);
    st128_sc1<0>(__builtin_bit_cast(v4u, d0), vo, rws, 0);
    st128_sc1<0>(__builtin_bit_cast(v4u, d1), vo, rws, 8 * 256 * 64);
  }
  fence_sched();

  {
    const float dt = bflo(pk2(expf(bflo(r_ldt)), 0.f));
    const float lr = dt * bflo(r_are) * 1.44269504088896341f, rv = dt * bflo(r_aim) * 0.15915494309189535f;
    const f2 db = {dt * bflo(r_bre), -dt * bflo(r_bim)};
    const f2 cb = cmul((f2){bflo(r_cre), -bflo(r_cim)}, db, crot(db));
    const f2 e16 = cexp_s(lr, rv, 16.f), e1 = cexp_s(lr, -rv, 1.f), e256 = cexp_s(lr, -rv, 256.f);
    const f2 e2 = cexp_s(lr, -rv, 2.f), e4 = cexp_s(lr, -rv, 4.f), e8 = cexp_s(lr, -rv, 8.f);
    const f2 q1 = crot(e1), q2 = crot(e2), q4 = crot(e4), q8 = crot(e8), q16 = crot(e16), q256 = crot(e256);
    LAS char* tw = sm + ch * TCH + (tid & 127) * 4;
    auto put = [&](int row, f2 v) { *(volatile LAS u32*)(tw + row * TROW) = pk2(v.x, v.y); };
    f2 a = e16, t0 = cb, tb[8], v[8];
    put(0, (f2){1.f, 0.f}); put(16, t0);
    tb[0] = cb;
#pragma unroll
    for (int i = 1; i < 16; ++i) {
      put(i, a); if (i < 15) a = cmul(a, e16, q16);
      t0 = cmul(t0, e1, q1); put(16 + i, t0);
      if (i < 8) { tb[i] = cmul(tb[i - 1], e256, q256); v[i] = tb[i]; }
#pragma unroll
      for (int tl = 1; tl < 8; ++tl) {
        const int base = tl == 1 ? 32 : (tl < 4 ? 41 + 5 * (tl - 2) : 51 + 3 * (tl - 4));
        const int cnt = tl == 1 ? 9 : (tl < 4 ? 5 : 3), k = i - tl;
        if (k >= 0 && k < cnt) {
          if (k > 0) v[tl] = cmul(v[tl], tl == 1 ? e2 : (tl < 4 ? e4 : e8), tl == 1 ? q2 : (tl < 4 ? q4 : q8));
          put(base + k, v[tl]);
        }
      }
    }
  }
  fence_sched();
  wait_vm<0>();
  flag_store(WS_UFLAG + (u32)(cg * 128 + tt * 4 + wave) * FSP, rws, tag, lane);
  STAMP(2);
  lds_bar();

  f4 kk[4];
  {
    LAS char* tb = sm + ch * TCH + g * 16;
    const int m2 = (n + ((n >> 1) & 1)) >> 1, m4 = (n + 1 + ((n >> 2) & 1)) >> 2, m8 = (n + 3 + ((n >> 3) & 1)) >> 3;
    LAS char* pr_[4] = {tb + (half ? 51 + m8 : 16 + n) * TROW, tb + (half ? 54 + m8 : 32 + m2) * TROW,
                        tb + (half ? 57 + m8 : 41 + m4) * TROW, tb + (half ? 60 + m8 : 46 + m4) * TROW};
    LAS char* pe = tb + n * TROW;
#pragma unroll
    for (int i = 0; i < 4; ++i) kk[i] = (f4)(0.f);
#pragma unroll
    for (int ks = 0; ks < 8; ++ks) {
      const bf8 e = lds_rd128(pe + ks * 64);
#pragma unroll
      for (int i = 0; i < 4; ++i) kk[i] = mfma16(lds_rd128(pr_[i] + ks * 64), e, kk[i]);
    }
  }
  fence_sched();
  STAMP(3);
  flag_wait(WS_UFLAG + (u32)(cg * 128 + wave * 32 + (lane & 31)) * FSP, rws, tag);
  STAMP(4);
  v4u uv[2];
  {
    const u32 vo = WS_UT + (u32)((pr >> 1) * 256 + 64 * wave + (lane >> 1)) * 64 + ((pr & 1) * 2 + (lane & 1)) * 16;
    uv[0] = ld128_sc1<0>(vo, rws, 0); uv[1] = ld128_sc1<2048>(vo, rws, 0);
  }
  lds_bar();
  {
    LAS char* kb = sm + ch * KCH;
#pragma unroll
    for (int i = 0; i < 4; ++i) {
      const int l0 = 256 * (4 * half + i) + 16 * n + 4 * g;
      const u32 a = pk2(kk[i][3], kk[i][2]), bq = pk2(kk[i][1], kk[i][0]), c = pk2(kk[i][2], kk[i][1]);
      *(LAS v2u*)(kb + KOO + 2 * (2044 - l0)) = (v2u){a, bq};
      *(LAS u16*)(kb + 2 * (2051 - l0)) = (u16)a;
      *(LAS u32*)(kb + 2 * (2052 - l0)) = c;
      *(LAS u16*)(kb + 2 * (2054 - l0)) = (u16)(bq >> 16);
    }
    const v4u z = {0, 0, 0, 0};
    if (lane < 29) *(LAS v4u*)(kb + (half ? KOO + 4096 : 4112) + lane * 16) = z;
    else if (lane < 33) *(LAS v4u*)(sm + UA0 + ch * UCH + (half ? UBO : 0) + (lane - 29) * 16 - (half ? 0 : 16)) = z;
    else if (lane == 33 && !half) *(LAS u16*)(kb + 2 * 2055) = 0;
    else if (lane == 34 && !half) *(LAS v3u*)(sm + UA0 + ch * UCH + 48) = (v3u){0, 0, 0};
  }
  asm volatile("s_waitcnt vmcnt(0)" : "+v"(uv[0]), "+v"(uv[1]) :: "memory");
#pragma unroll
  for (int k = 0; k < 2; ++k) {
    LAS char* ub = sm + UA0 + (lane & 1) * UCH + 64 + (64 * wave + 32 * k + (lane >> 1)) * 16;
    *(LAS v4u*)(ub + UBO) = uv[k];
    *(LAS u32*)(ub - 4) = uv[k].x;
    *(LAS v3u*)(ub) = (v3u){uv[k].y, uv[k].z, uv[k].w};
  }
  lds_bar();
  STAMP(5);

  f4 y[8];
  {
    LAS char* pk = sm + ch * KCH + ((n & 1) ? KOO + 4032 : 4048) - 64 * (n >> 1) + 16 * g - 3584 + 256 * half;
    LAS char* pu = sm + UA0 + ch * UCH + (n < 8 ? 8 * n : UBO + 8 * (n - 8) + 8) + 16 * g + 256 * half;
#pragma unroll
    for (int a = 0; a < 8; ++a) y[a] = (f4)(0.f);
#pragma unroll
    for (int rr = 0; rr < 4; ++rr) {
      bf8 kf[8], uf[8];
#pragma unroll
      for (int i = 0; i < 8; ++i) {
        kf[i] = lds_rd128(pk + 512 * (7 - i) + 64 * rr);
        const v2u lo = *(LAS v2u*)(pu + 512 * i + 64 * rr), hi = *(LAS v2u*)(pu + 512 * i + 64 * rr + 8);
        uf[i] = __builtin_bit_cast(bf8, (v4u){lo.x, lo.y, hi.x, hi.y});
      }
#pragma unroll
      for (int s = 0; s < 8; ++s)
#pragma unroll
        for (int m = 0; m <= s; ++m) y[s] = mfma16(kf[s - m], uf[m], y[s]);
    }
  }
  STAMP(6);
#pragma unroll
  for (int a = 0; a < 8; ++a) *(LAS f4*)(sm + EX + (wave * 8 + a) * 1024 + lane * 16) = y[a];
  lds_bar();
  {
    const float d0 = bflo(r_dsk), d1 = bfhi(r_dsk);
    const int rn = n < 8 ? 2 * n : 2 * (n - 8) + 1;
    const bool lo = n < 8;
#pragma unroll
    for (int i = 0; i < 2; ++i) {
      const int al = 2 * wave + i;
      LAS char* ex = sm + EX + al * 1024 + lane * 16;
      const f4 y0 = *(LAS f4*)(ex) + *(LAS f4*)(ex + 8192), y1 = *(LAS f4*)(ex + 16384) + *(LAS f4*)(ex + 24576);
      LAS char* up = sm + UA0 + UBO + 64 + (256 * al + 2 * rn + 64 * g) * 2;
      const u32 u00 = *(LAS u32*)(up), u01 = *(LAS u32*)(up + 64), u10 = *(LAS u32*)(up + UCH), u11 = *(LAS u32*)(up + UCH + 64);
      const u32 x0 = pk2(y0[0] + bflo(u00) * d0, y1[0] + bflo(u10) * d1), x1 = pk2(y0[1] + bfhi(u00) * d0, y1[1] + bfhi(u10) * d1);
      const u32 z0 = pk2(y0[2] + bflo(u01) * d0, y1[2] + bflo(u11) * d1), z1 = pk2(y0[3] + bfhi(u01) * d0, y1[3] + bfhi(u11) * d1);
      const u32 s0 = lo ? z0 : x0, s1 = lo ? z1 : x1;
      const u32 r0 = __builtin_amdgcn_update_dpp(0u, s0, 0x128, 0xf, 0xf, false), r1 = __builtin_amdgcn_update_dpp(0u, s1, 0x128, 0xf, 0xf, false);
      const v4u d = lo ? (v4u){x0, x1, r0, r1} : (v4u){r0, r1, z0, z1};
      st128_sc1<0>(d, WS_A + (u32)(4 * al + g) * 65536 + pr * 256 + n * 16, rws, 0);
    }
  }
  wait_vm<0>();
  flag_store(WS_AFLAG + (u32)(wave * 256 + pr) * FSP, rws, tag, lane);
  STAMP(7);
  if (ci == 0) {
    flag_wait(WS_AFLAG + (u32)tid * FSP, rws, tag);
    flag_wait(tid < 224 ? WS_HFLAG + (u32)((tid / 14) * 64 + (tid % 14) * 2) * FSP : WS_GFLAG + (u32)(tid - 224) * 4 * FSP, rws, tag);
    lds_bar();
    if (wave == 0) flag_store(0, rws, tag, lane);
  }
  STAMP_OUT;
}

__global__ __launch_bounds__(256, 2) void fused_kernel(const u16* __restrict__ x, const u16* __restrict__ are, const u16* __restrict__ aim,
                                                       const u16* __restrict__ bre, const u16* __restrict__ bim, const u16* __restrict__ cre,
                                                       const u16* __restrict__ cim, const u16* __restrict__ logdt, const u16* __restrict__ dskip,
                                                       const u16* __restrict__ w1, const u16* __restrict__ b1, const u16* __restrict__ w2,
                                                       const u16* __restrict__ b2, const u16* __restrict__ gw, u16* __restrict__ out, char* ws) {
  __shared__ __attribute__((aligned(256))) char smem[81920];
  LAS char* sm = (LAS char*)smem;
  asm volatile("" :: "s"(x), "s"(are), "s"(aim), "s"(bre), "s"(bim), "s"(cre), "s"(cim), "s"(logdt), "s"(dskip), "s"(w1), "s"(b1),
               "s"(w2), "s"(b2), "s"(gw), "s"(out), "s"(ws));
  const int b = blockIdx.x;
  if (b >= 256) gemm_role(b - 256, sm, x, w1, b1, w2, b2, gw, out, ws);
  else conv_role(b, sm, x, are, aim, bre, bim, cre, cim, logdt, dskip, ws);
}

extern "C" void kernel_launch(void* const* d_in, const int* in_sizes, int n_in, void* d_out, int out_size, void* d_ws, size_t ws_size,
                              hipStream_t stream) {
  const u16* const* in = (const u16* const*)d_in;
  fused_kernel<<<512, 256, 0, stream>>>(in[0], in[1], in[2], in[3], in[4], in[5], in[6], in[7], in[8], in[10], in[11], in[12], in[13],
                                        in[14], (u16*)d_out, (char*)d_ws);
}
```

```cpp
#define DELAY 4000
#include <hip/hip_runtime.h>
#include <stdint.h>
#include <type_traits>
#pragma clang diagnostic ignored "-Winline-asm"

typedef unsigned short u16;
typedef unsigned int u32;
typedef unsigned long long u64;
typedef __attribute__((ext_vector_type(2))) u32 v2u;
typedef __attribute__((ext_vector_type(3))) u32 v3u;
typedef __attribute__((ext_vector_type(4))) u32 v4u;
typedef __attribute__((ext_vector_type(2))) float f2;
typedef __attribute__((ext_vector_type(4))) float f4;
typedef __attribute__((ext_vector_type(16))) float f16v;
typedef __attribute__((ext_vector_type(2))) __bf16 bf2;
typedef __attribute__((ext_vector_type(8))) __bf16 bf8;
typedef __attribute__((ext_vector_type(4))) short s4;
typedef __amdgpu_buffer_rsrc_t rsrc_t;
#define DI __device__ __forceinline__
#define LAS __attribute__((address_space(3)))
#define CAS __attribute__((address_space(4)))

#define MAP1 1
#define PDEPTH 9
#define MAP2 0

constexpr u32 FSP = 256;
constexpr u32 WS_G = 4096, WS_GFLAG = 32768, WS_HFLAG = 65536, WS_UFLAG = WS_HFLAG + 1024 * FSP, WS_AFLAG = WS_UFLAG + 1024 * FSP;
constexpr u32 WS_UT = 1u << 20, WS_A = 3u << 20, WS_H = 5u << 20, WS_END = 13u << 20;
constexpr int SPIN_MAX = 1 << 16;

#define STAMP(i)
#define STAMP_DECL
#define STAMP_OUT

DI rsrc_t mkrsrc(const void* p, u32 bytes) { return __builtin_amdgcn_make_buffer_rsrc((void*)p, (short)0, (int)bytes, 0x00020000); }
DI float bflo(u32 v) { return __uint_as_float(v << 16); }
DI float bfhi(u32 v) { return __uint_as_float(v & 0xffff0000u); }
DI u32 pk2(float a, float b) { bf2 r = __builtin_convertvector((f2){a, b}, bf2); return __builtin_bit_cast(u32, r); }
DI float relu(float v) { return __int_as_float(max(__float_as_int(v), 0)); }
DI void fence_sched() { __builtin_amdgcn_sched_barrier(0); }

template <int N> DI void wait_vm() { asm volatile("s_waitcnt vmcnt(%0)" :: "n"(N) : "memory"); }
template <int N> DI void wait_vm_bar() { asm volatile("s_waitcnt vmcnt(%0) lgkmcnt(0)\n\ts_barrier" :: "n"(N) : "memory"); }
DI void lds_bar() { asm volatile("s_waitcnt lgkmcnt(0)\n\ts_barrier" ::: "memory"); }
DI void wait_lgkm() { asm volatile("s_waitcnt lgkmcnt(0)" ::: "memory"); }

DI void dma2(u32 m0v, u32 v0, u32 v1, rsrc_t rs, u32 so) {
  asm volatile("s_mov_b32 m0, %0\n\ts_nop 0\n\t"
               "buffer_load_dwordx4 %1, %3, %4 offen lds\n\t"
               "buffer_load_dwordx4 %2, %3, %4 offen offset:1024 lds"
               :: "s"(m0v), "v"(v0), "v"(v1), "s"(rs), "s"(so) : "m0", "memory");
}
DI void dma4(u32 m0v, u32 v0, u32 v1, u32 v2, u32 v3, rsrc_t rs, u32 so) {
  asm volatile("s_mov_b32 m0, %0\n\ts_nop 0\n\t"
               "buffer_load_dwordx4 %1, %5, %6 offen lds\n\t"
               "buffer_load_dwordx4 %2, %5, %6 offen offset:1024 lds\n\t"
               "buffer_load_dwordx4 %3, %5, %6 offen offset:2048 lds\n\t"
               "buffer_load_dwordx4 %4, %5, %6 offen offset:3072 lds"
               :: "s"(m0v), "v"(v0), "v"(v1), "v"(v2), "v"(v3), "s"(rs), "s"(so) : "m0", "memory");
}
template <int OFF> DI v4u ld128_sc1(u32 vo, rsrc_t rs, u32 so) {
  v4u r; asm volatile("buffer_load_dwordx4 %0, %1, %2, %3 offen offset:%4 sc1" : "=&v"(r) : "v"(vo), "s"(rs), "s"(so), "n"(OFF) : "memory"); return r;
}
template <int OFF> DI u32 ld32_sc1(u32 vo, rsrc_t rs, u32 so) {
  u32 r; asm volatile("buffer_load_dword %0, %1, %2, %3 offen offset:%4 sc1" : "=&v"(r) : "v"(vo), "s"(rs), "s"(so), "n"(OFF) : "memory"); return r;
}
template <int OFF> DI v2u ld64(u32 vo, rsrc_t rs) {
  v2u r; asm volatile("buffer_load_dwordx2 %0, %1, %2, 0 offen offset:%3" : "=&v"(r) : "v"(vo), "s"(rs), "n"(OFF) : "memory"); return r;
}
template <int OFF> DI void st128_sc1(v4u d, u32 vo, rsrc_t rs, u32 so) {
  asm volatile("buffer_store_dwordx4 %0, %1, %2, %3 offen offset:%4 sc1\n\ts_nop 1" :: "v"(d), "v"(vo), "s"(rs), "s"(so), "n"(OFF) : "memory");
}
DI void st32_sc1(u32 d, u32 vo, rsrc_t rs) {
  asm volatile("buffer_store_dword %0, %1, %2, 0 offen sc1" :: "v"(d), "v"(vo), "s"(rs) : "memory");
}
DI void flag_store(u32 off, rsrc_t rs, u64 tag, int lane) {
  if (lane == 0) {
    v2u d = {(u32)tag, (u32)(tag >> 32)};
    asm volatile("buffer_store_dwordx2 %0, %1, %2, 0 offen sc1\n\ts_nop 1" :: "v"(d), "v"(off), "s"(rs) : "memory");
  }
}
DI u64 epoch_issue(const char* ws) { u64 e; asm volatile("s_load_dwordx2 %0, %1, 0x0" : "=&s"(e) : "s"(ws) : "memory"); return e; }
DI u64 epoch_tag(u64 e) {
  asm volatile("s_waitcnt lgkmcnt(0)" : "+s"(e) :: "memory");
  return e * 0x9E3779B97F4A7C15ull + 0x632BE59BD9B4E019ull;
}
DI void flag_wait(u32 vo, rsrc_t rs, u64 tag) {
#pragma unroll 1
  for (int it = 0; it < SPIN_MAX; ++it) {
    v2u v; asm volatile("buffer_load_dwordx2 %0, %1, %2, 0 offen sc1\n\ts_waitcnt vmcnt(0)" : "=&v"(v) : "v"(vo), "s"(rs) : "memory");
    const bool bad = (v.x != (u32)tag) | (v.y != (u32)(tag >> 32));
    if (__builtin_amdgcn_ballot_w64(bad) == 0) break;
    __builtin_amdgcn_s_sleep(1);
  }
}

DI bf8 lds_rd128(LAS char* p) { return *(LAS bf8*)p; }
DI bf8 lds_tr2(LAS char* p0, LAS char* p1) {
  s4 a = __builtin_amdgcn_ds_read_tr16_b64_v4i16((LAS s4*)p0);
  s4 b = __builtin_amdgcn_ds_read_tr16_b64_v4i16((LAS s4*)p1);
  v2u x = __builtin_bit_cast(v2u, a), y = __builtin_bit_cast(v2u, b);
  return __builtin_bit_cast(bf8, (v4u){x.x, x.y, y.x, y.y});
}
DI f16v mfma32(bf8 a, bf8 b, f16v c) { return __builtin_amdgcn_mfma_f32_32x32x16_bf16(a, b, c, 0, 0, 0); }
DI f4 mfma16(bf8 a, bf8 b, f4 c) { return __builtin_amdgcn_mfma_f32_16x16x32_bf16(a, b, c, 0, 0, 0); }

template <int I, int N, class F> DI void static_for(F&& f) {
  if constexpr (I < N) { f(std::integral_constant<int, I>{}); static_for<I + 1, N>(f); }
}

constexpr u32 XS = 0, WSL = 49152;
constexpr u32 vblk(int i) {
  constexpr u32 t[10] = {XS + 32768, XS + 32768 + 2048, WSL, XS, XS + 2048, WSL + 8192, WSL + 16384, XS + 16384, XS + 16384 + 2048, WSL + 24576};
  return t[i];
}
constexpr bool vblk_x(int i) { return vblk(i) < WSL; }

struct Frag { bf8 a[2][2], b[2][2]; };

DI void gemm_role(int b, LAS char* sm, const u16* x, const u16* w1, const u16* b1, const u16* w2, const u16* b2, const u16* gw,
                  u16* out, char* ws) {
  const int lane = threadIdx.x & 63, wave = __builtin_amdgcn_readfirstlane(threadIdx.x >> 6);
  STAMP_DECL;
  STAMP(0);
  const int xcd = b & 7, r = b >> 3;
  const rsrc_t rws = mkrsrc(ws, WS_END), rx = mkrsrc(x, 2048 * 512 * 2), rw2 = mkrsrc(w2, 1792 * 512 * 2);
  const int g = lane >> 4, q = (lane >> 2) & 3, p = lane & 3, l31 = lane & 31, hh = lane >> 5;
  const u32 smb = (u32)(uintptr_t)sm;
  const int t2 = MAP2 ? 8 * (xcd >> 1) + (r >> 2) : 4 * xcd + (r >> 3);
  const int c2 = MAP2 ? 4 * (xcd & 1) + (r & 3) : (r & 7);

  const u32 vv0 = (u32)(448 * wave + (lane >> 3)) * 1024 + c2 * 128 + (((lane & 7) ^ (((lane >> 4) & 1) << 2)) << 4);
  const u32 vv1 = vv0 + 7168;
  const u32 mX = smb + wave * 4096, mW = smb + wave * 2048;
  auto vload = [&](auto BI, int ks) {
    constexpr int bi = decltype(BI)::value;
    dma2((vblk_x(bi) ? mX : mW) + vblk(bi), vv0, vv1, rw2, (u32)ks * 16384);
  };
#define VLOAD(bi, ks) vload(std::integral_constant<int, bi>{}, ks)
  u64 tag;

  if (r < 28) {
    const int tau = MAP1 ? 4 * (xcd >> 1) + r / 7 : 2 * xcd + r / 14;
    const int j = MAP1 ? 7 * (xcd & 1) + r % 7 : r % 14;
    const int rot = MAP1 ? (r % 7) + 2 * (r / 7) : (r % 14) + 4 * (r / 14);
    const int wf = wave >> 1, wt = wave & 1;
    const rsrc_t rw1 = mkrsrc(w1, 512 * 1792 * 2), rb1 = mkrsrc(b1, 1792 * 2);
    u32 vx[4], vw[2];
#pragma unroll
    for (int m = 0; m < 4; ++m)
      vx[m] = (u32)(tau * 128 + (wave * 4 + m) * 8 + (lane >> 3)) * 1024 + (((lane & 7) ^ ((4 * (m & 1) + g) & 7)) << 4) - 1024 * m;
#pragma unroll
    for (int m = 0; m < 2; ++m)
      vw[m] = (u32)((wave * 2 + m) * 4 + g) * 3584 + j * 256 + (((lane & 15) ^ (g << 2)) << 4) - 1024 * m;
    auto xload = [&](int s) { dma4(mX + XS + (s % 3) * 16384, vx[0], vx[1], vx[2], vx[3], rx, (u32)((s + rot) & 7) * 128); };
    auto wload = [&](int t) { dma2(mW + WSL + (t & 3) * 8192, vw[0], vw[1], rw1, (u32)((t + 2 * rot) & 15) * (32 * 3584)); };
    xload(0); wload(0);
    v2u bv[8];
    const u32 vb = (u32)(j * 128 + wf * 64 + 4 * hh) * 2;
    static_for<0, 8>([&](auto I) { constexpr int i = decltype(I)::value; bv[i] = ld64<i * 16>(vb, rb1); });
    wload(1); xload(1); wload(2); wload(3); xload(2);
    tag = epoch_tag(epoch_issue(ws));
    __builtin_amdgcn_s_setprio(2);

    LAS char* pa[2]; LAS char* pb[4];
#pragma unroll
    for (int mi = 0; mi < 2; ++mi)
      pa[mi] = sm + WSL + (8 * (g >> 1) + q) * 256 + (((8 * wf + 4 * mi + 2 * (g & 1) + (p >> 1)) ^ (q << 2)) << 4) + (p & 1) * 8;
#pragma unroll
    for (int s = 0; s < 4; ++s) pb[s] = sm + XS + (64 * wt + l31) * 128 + (((2 * s + hh) ^ ((l31 >> 1) & 7)) << 4);

    f16v acc[2][2];
    Frag fr[2];
    auto rd = [&](auto T) {
      constexpr int t = decltype(T)::value;
      Frag& f = fr[t & 1];
#pragma unroll
      for (int s = 0; s < 2; ++s) {
#pragma unroll
        for (int mi = 0; mi < 2; ++mi)
          f.a[mi][s] = lds_tr2(pa[mi] + (t & 3) * 8192 + s * 4096, pa[mi] + (t & 3) * 8192 + s * 4096 + 1024);
#pragma unroll
        for (int ni = 0; ni < 2; ++ni) f.b[ni][s] = lds_rd128(pb[2 * (t & 1) + s] + ((t >> 1) % 3) * 16384 + ni * 4096);
      }
    };
    auto mm = [&](auto T) {
      constexpr int t = decltype(T)::value;
      Frag& f = fr[t & 1];
#pragma unroll
      for (int s = 0; s < 2; ++s)
#pragma unroll
        for (int mi = 0; mi < 2; ++mi)
#pragma unroll
          for (int ni = 0; ni < 2; ++ni) acc[mi][ni] = mfma32(f.a[mi][s], f.b[ni][s], acc[mi][ni]);
    };
    STAMP(1);
    static_for<0, 17>([&](auto T) {
      constexpr int t = decltype(T)::value;
      fence_sched();
      if constexpr (t == 0) {
        asm volatile("s_waitcnt vmcnt(14)\n\ts_barrier"
                     : "+v"(bv[0]), "+v"(bv[1]), "+v"(bv[2]), "+v"(bv[3]), "+v"(bv[4]), "+v"(bv[5]), "+v"(bv[6]), "+v"(bv[7]) :: "memory");
      } else if constexpr (t == 16) lds_bar();
      else wait_vm_bar<(t & 1) ? 12 : 8>();
      fence_sched();
      if constexpr (t == 0) { STAMP(2); }
      if constexpr (t == 8) { STAMP(3); }
      if constexpr (t == 15) { STAMP(4); }
      if constexpr (t >= 1 && t + 3 < 16) wload(t + 3);
      if constexpr (t >= 2 && (t & 1) == 0 && t / 2 + 2 < 8) xload(t / 2 + 2);
      if constexpr (t == 12) { VLOAD(0, 0); VLOAD(1, 1); }
      if constexpr (t == 13) { VLOAD(2, 2); }
      if constexpr (t == 14) { VLOAD(3, 3); VLOAD(4, 4); VLOAD(5, 5); }
      if constexpr (t == 15) { VLOAD(6, 6); }
      if constexpr (t == 16) { VLOAD(7, 7); VLOAD(8, 8); VLOAD(9, 9); }
      if constexpr (t < 16) rd(T);
      if constexpr (t == 0) {
#pragma unroll
        for (int mi = 0; mi < 2; ++mi) {
          f16v c;
#pragma unroll
          for (int i = 0; i < 4; ++i) {
            c[4 * i] = bflo(bv[4 * mi + i].x); c[4 * i + 1] = bfhi(bv[4 * mi + i].x);
            c[4 * i + 2] = bflo(bv[4 * mi + i].y); c[4 * i + 3] = bfhi(bv[4 * mi + i].y);
          }
          acc[mi][0] = c; acc[mi][1] = c;
        }
      } else {
        mm(std::integral_constant<int, t - 1>{});
        if constexpr (t < 16) {
#pragma unroll
          for (int k = 0; k < 6; ++k) { __builtin_amdgcn_sched_group_barrier(0x008, 1, 0); __builtin_amdgcn_sched_group_barrier(0x100, 2, 0); }
          __builtin_amdgcn_sched_group_barrier(0x008, 2, 0);
        }
      }
    });
    const u32 vh = WS_H + (u32)((4 * tau + 2 * wt) * 112 + 8 * j + 4 * wf) * 1024 + lane * 16;
    static_for<0, 4>([&](auto I) {
      constexpr int mi = decltype(I)::value >> 1, ni = decltype(I)::value & 1;
#pragma unroll
      for (int s = 0; s < 2; ++s) {
        v4u d;
#pragma unroll
        for (int k = 0; k < 4; ++k) {
          f2 v = {relu(acc[mi][ni][8 * s + 2 * k]), relu(acc[mi][ni][8 * s + 2 * k + 1])};
          v = v * v;
          d[k] = pk2(v.x, v.y);
        }
        if (s == 0) st128_sc1<mi * 2048>(d, vh, rws, ni * 114688); else st128_sc1<mi * 2048 + 1024>(d, vh, rws, ni * 114688);
      }
    });
    STAMP(5);
    __builtin_amdgcn_s_setprio(0);
    wait_vm<8>();
  } else {
    const int tg = xcd * 4 + (r - 28);
    const v4u gv = *(const v4u*)(gw + lane * 8);
    const u16* xr = x + (size_t)(tg * 64 + wave * 16) * 512 + lane * 8;
    v4u xv[16];
#pragma unroll
    for (int i = 0; i < 16; ++i) xv[i] = *(const v4u*)(xr + i * 512);
    tag = epoch_tag(epoch_issue(ws));
    float mine = 0.f;
#pragma unroll
    for (int i = 0; i < 16; ++i) {
      float s = 0.f;
#pragma unroll
      for (int k = 0; k < 4; ++k) { s += bflo(xv[i][k]) * bflo(gv[k]); s += bfhi(xv[i][k]) * bfhi(gv[k]); }
#pragma unroll
      for (int o = 32; o >= 1; o >>= 1) s += __shfl_xor(s, o);
      if ((lane & 15) == i) mine = s;
    }
    const float sl = bflo(pk2(mine, 0.f));
    const float gt = bflo(pk2(1.f / (1.f + __expf(-sl)), 0.f));
    if (lane < 16) st32_sc1(__float_as_uint(gt), WS_G + (u32)(tg * 64 + wave * 16 + lane) * 4, rws);
    wait_vm<0>();
    flag_store(WS_GFLAG + (u32)(tg * 4 + wave) * FSP, rws, tag, lane);
    static_for<0, 10>([&](auto I) { vload(I, decltype(I)::value); });
    wait_vm<0>();
  }

  LAS char* pv[2][2];
#pragma unroll
  for (int mi = 0; mi < 2; ++mi) {
    const u32 o = (4 * (g >> 1) + q) * 128 + (((4 * (mi ^ (wave >> 1)) + 2 * (g & 1) + (p >> 1)) ^ (((q >> 1) & 1) << 2)) << 4) + (p & 1) * 8;
    pv[mi][0] = sm + WSL + wave * 2048 + o; pv[mi][1] = sm + wave * 4096 + o;
  }
  auto vfrag = [&](auto BI, int mi) {
    constexpr int bi = decltype(BI)::value;
    LAS char* a = vblk_x(bi) ? pv[mi][1] + vblk(bi) : pv[mi][0] + (vblk(bi) - WSL);
    return lds_tr2(a, a + 1024);
  };
  bf8 wa[10][2];
  static_for<0, 10>([&](auto I) { constexpr int i = decltype(I)::value; wa[i][0] = vfrag(I, 0); wa[i][1] = vfrag(I, 1); });
  fence_sched();
  wait_lgkm();
  fence_sched();
  static_for<0, 10>([&](auto I) { vload(I, 10 + decltype(I)::value); });
  if (r < 28) {
    const int tau = MAP1 ? 4 * (xcd >> 1) + r / 7 : 2 * xcd + r / 14;
    const int j = MAP1 ? 7 * (xcd & 1) + r % 7 : r % 14;
    wait_vm<20>();
    STAMP(6);
    flag_store(WS_HFLAG + (u32)((2 * tau + (wave & 1)) * 32 + 2 * j + (wave >> 1)) * FSP, rws, tag, lane);
  }
  flag_wait(WS_HFLAG + (u32)(t2 * 32 + 7 * wave + (lane < 7 ? lane : 0)) * FSP, rws, tag);
  STAMP(7);
  __builtin_amdgcn_s_setprio(2);

  constexpr int PD = PDEPTH;
  u32 vhh[2];
#pragma unroll
  for (int ni = 0; ni < 2; ++ni) vhh[ni] = WS_H + (u32)((2 * t2 + (ni ^ (wave & 1))) * 112 + 28 * wave) * 1024 + lane * 16;
  v4u hb[28][2];
  auto ksof = [](int k) constexpr { return k < 10 ? 10 + k : (k < 20 ? k - 10 : k); };
  auto hload = [&](auto K) {
    constexpr int k = decltype(K)::value, ks = ksof(k);
    hb[k][0] = ld128_sc1<(ks & 3) * 1024>(vhh[0], rws, (ks >> 2) * 4096);
    hb[k][1] = ld128_sc1<(ks & 3) * 1024>(vhh[1], rws, (ks >> 2) * 4096);
  };
  static_for<0, PD>(hload);
  f16v o2[2][2];
  bf8 wb[28][2];
  wb[0][0] = vfrag(std::integral_constant<int, 0>{}, 0); wb[0][1] = vfrag(std::integral_constant<int, 0>{}, 1);
  static_for<0, 28>([&](auto K) {
    constexpr int k = decltype(K)::value;
    constexpr auto after_h = [](int kk) constexpr {
      int n = 0;
      if (kk < PD) n += 2 * (PD - 1 - kk);
      for (int i = (kk < PD ? 0 : kk - PD + 1); i < kk; ++i) n += ((i < 8) ? 2 : 0) + ((i + PD < 28) ? 2 : 0);
      return n;
    };
    fence_sched();
    asm volatile("s_waitcnt vmcnt(%2) lgkmcnt(0)" : "+v"(hb[k][0]), "+v"(hb[k][1]) : "n"(after_h(k)) : "memory");
    fence_sched();
    if constexpr (k == 0) { STAMP(8); }
    auto mf = [&](int mi, int ni) {
      const bf8 a = (k >= 10 && k < 20) ? wa[(k >= 10 && k < 20) ? k - 10 : 0][mi] : wb[k][mi];
      if constexpr (k == 0) o2[mi][ni] = mfma32(a, __builtin_bit_cast(bf8, hb[k][ni]), (f16v)(0.f));
      else o2[mi][ni] = mfma32(a, __builtin_bit_cast(bf8, hb[k][ni]), o2[mi][ni]);
      fence_sched();
    };
    mf(0, 0);
    if constexpr (k + 1 < 10) { wb[k + 1][0] = vfrag(std::integral_constant<int, k + 1>{}, 0); wb[k + 1][1] = vfrag(std::integral_constant<int, k + 1>{}, 1); }
    if constexpr (k + 1 >= 20 && k + 1 < 28) { wb[k + 1][0] = vfrag(std::integral_constant<int, k + 1 - 20>{}, 0); wb[k + 1][1] = vfrag(std::integral_constant<int, k + 1 - 20>{}, 1); }
    fence_sched();
    mf(1, 0);
    if constexpr (k < 8) VLOAD(k, 20 + k);
    mf(0, 1);
    mf(1, 1);
    if constexpr (k + PD < 28) hload(std::integral_constant<int, k + PD>{});
  });
  fence_sched();
  STAMP(9);
#pragma unroll
  for (int s = 1; s < 4; ++s)
#pragma unroll
    for (int k = 0; k < 4; ++k) {
      const f16v& a = o2[s >> 1][s & 1];
      *(LAS f4*)(sm + XS + (s - 1) * 16384 + wave * 4096 + k * 1024 + lane * 16) = (f4){a[4 * k], a[4 * k + 1], a[4 * k + 2], a[4 * k + 3]};
    }
  const int mi = wave >> 1, ni = wave & 1;
  flag_wait(lane < 16 ? WS_AFLAG + (u32)((t2 >> 3) * 256 + 32 * c2 + 16 * mi + lane) * FSP
                      : WS_GFLAG + (u32)(t2 * 4 + 2 * ni + (lane & 1)) * FSP, rws, tag);
  STAMP(10);
  u32 av[8];
  const u32 va = WS_A + (u32)t2 * 65536 + (u32)(32 * c2 + 16 * mi + 2 * hh) * 256 + (32 * ni + l31) * 4;
  static_for<0, 8>([&](auto I) { constexpr int i = decltype(I)::value; av[i] = ld32_sc1<(i >> 1) * 1024 + (i & 1) * 256>(va, rws, 0); });
  u32 gq = ld32_sc1<0>(WS_G + (u32)(64 * t2 + 32 * ni + l31) * 4, rws, 0);
  v2u b2v[4];
  const rsrc_t rb2 = mkrsrc(b2, 512 * 2);
  static_for<0, 4>([&](auto I) { constexpr int i = decltype(I)::value; b2v[i] = ld64<i * 16>((u32)(64 * c2 + 32 * mi + 4 * hh) * 2, rb2); });
  lds_bar();
  f16v o = o2[0][0];
#pragma unroll
  for (int s = 1; s < 4; ++s)
#pragma unroll
    for (int k = 0; k < 4; ++k) {
      const f4 v = *(LAS f4*)(sm + XS + (s - 1) * 16384 + (wave ^ s) * 4096 + k * 1024 + lane * 16);
      o[4 * k] += v.x; o[4 * k + 1] += v.y; o[4 * k + 2] += v.z; o[4 * k + 3] += v.w;
    }
  asm volatile("s_waitcnt vmcnt(0)"
               : "+v"(av[0]), "+v"(av[1]), "+v"(av[2]), "+v"(av[3]), "+v"(av[4]), "+v"(av[5]), "+v"(av[6]), "+v"(av[7]), "+v"(gq),
                 "+v"(b2v[0]), "+v"(b2v[1]), "+v"(b2v[2]), "+v"(b2v[3]) :: "memory");
  const float gt = __uint_as_float(gq), gn = 1.f - gt;
  LAS char* tp = sm + WSL + wave * 2048;
#pragma unroll
  for (int i = 0; i < 4; ++i) {
    const float r0 = gt * bflo(av[2 * i]) + gn * (o[4 * i] + bflo(b2v[i].x));
    const float r1 = gt * bfhi(av[2 * i]) + gn * (o[4 * i + 1] + bfhi(b2v[i].x));
    const float r2 = gt * bflo(av[2 * i + 1]) + gn * (o[4 * i + 2] + bflo(b2v[i].y));
    const float r3 = gt * bfhi(av[2 * i + 1]) + gn * (o[4 * i + 3] + bfhi(b2v[i].y));
    *(LAS v2u*)(tp + l31 * 64 + ((i ^ ((l31 >> 1) & 3)) << 4) + hh * 8) = (v2u){pk2(r0, r1), pk2(r2, r3)};
  }
#pragma unroll
  for (int k = 0; k < 2; ++k) {
    const int row = 16 * k + (lane >> 2);
    const v4u v = *(LAS v4u*)(tp + row * 64 + ((p ^ ((row >> 1) & 3)) << 4));
    *(v4u*)(out + (size_t)(64 * t2 + 32 * ni + row) * 512 + 64 * c2 + 32 * mi + p * 8) = v;
  }
  STAMP(11);
  STAMP_OUT;
}

DI f2 cmul(f2 a, f2 m, f2 mr) { return (f2){a.x, a.x} * m + (f2){a.y, a.y} * mr; }
DI f2 crot(f2 m) { return (f2){-m.y, m.x}; }
DI f2 cexp_s(float lr, float rv, float m) {
  const float e = __builtin_amdgcn_exp2f(lr * m), f = __builtin_amdgcn_fractf(rv * m);
  return (f2){e * __builtin_amdgcn_cosf(f), e * __builtin_amdgcn_sinf(f)};
}

constexpr u32 TROW = 544, TCH = 63 * TROW, XT = 69632;
constexpr u32 KCH = 9472, KOO = 4656, UA0 = 18944, UCH = 8768, UBO = 4224, EX = 40960;

DI void conv_role(int ci, LAS char* sm, const u16* x, const u16* are, const u16* aim, const u16* bre, const u16* bim,
                  const u16* cre, const u16* cim, const u16* logdt, const u16* dskip, char* ws, u64 tb0) {
  const int tid = threadIdx.x, lane = tid & 63, wave = __builtin_amdgcn_readfirstlane(tid >> 6);
  STAMP_DECL;
  STAMP(0);
  const rsrc_t rws = mkrsrc(ws, WS_END), rx = mkrsrc(x, 2048 * 512 * 2);
  const u32 smb = (u32)(uintptr_t)sm;
  const int cg = ci & 7, tt = ci >> 3, pr = cg * 32 + tt;
  const int g = lane >> 4, q = (lane >> 2) & 3, p = lane & 3, n = lane & 15;
  const int ch = wave >> 1, half = wave & 1;

  {
    const u32 v0 = (u32)(64 * tt + 16 * wave + (lane >> 3)) * 1024 + cg * 128 + (((lane & 7) ^ ((g & 1) << 1)) << 4);
    const u32 v1 = (v0 ^ 64) + 7168;
    dma2(smb + XT + wave * 2048, v0, v1, rx, 0);
  }
  const int pi = pr * 256 + tid;
  u32 r_are = are[pi], r_aim = aim[pi], r_bre = bre[pi], r_bim = bim[pi], r_cre = cre[pi], r_cim = cim[pi];
  u32 r_ldt = logdt[2 * pr + ch];
  const u32 r_dsk = *(const u32*)(dskip + 2 * pr);
  fence_sched();
  const u64 tag = epoch_tag(epoch_issue(ws));
  asm volatile("s_waitcnt vmcnt(0)" : "+v"(r_are), "+v"(r_aim), "+v"(r_bre), "+v"(r_bim), "+v"(r_cre), "+v"(r_cim), "+v"(r_ldt) :: "memory");
  fence_sched();
  STAMP(1);
  {
    LAS char* tb = sm + XT + wave * 2048 + (8 * (p & 1) + q) * 128 + (p >> 1) * 8;
    const int sw = (q & 2) | ((p & 1) << 2);
    const u32 vo = WS_UT + (u32)((16 * cg + 2 * g + (q >> 1)) * 256 + 8 * tt + 2 * wave + (q & 1)) * 64 + p * 16;
    const bf8 d0 = lds_tr2(tb + ((g ^ sw) << 4), tb + ((g ^ sw) << 4) + 512);
    const bf8 d1 = lds_tr2(tb + (((4 + g) ^ sw) << 4), tb + (((4 + g) ^ sw) << 4) + 512);
    st128_sc1<0>(__builtin_bit_cast(v4u, d0), vo, rws, 0);
    st128_sc1<0>(__builtin_bit_cast(v4u, d1), vo, rws, 8 * 256 * 64);
  }
  fence_sched();

  {
    const float dt = bflo(pk2(expf(bflo(r_ldt)), 0.f));
    const float lr = dt * bflo(r_are) * 1.44269504088896341f, rv = dt * bflo(r_aim) * 0.15915494309189535f;
    const f2 db = {dt * bflo(r_bre), -dt * bflo(r_bim)};
    const f2 cb = cmul((f2){bflo(r_cre), -bflo(r_cim)}, db, crot(db));
    const f2 e16 = cexp_s(lr, rv, 16.f), e1 = cexp_s(lr, -rv, 1.f), e256 = cexp_s(lr, -rv, 256.f);
    const f2 e2 = cexp_s(lr, -rv, 2.f), e4 = cexp_s(lr, -rv, 4.f), e8 = cexp_s(lr, -rv, 8.f);
    const f2 q1 = crot(e1), q2 = crot(e2), q4 = crot(e4), q8 = crot(e8), q16 = crot(e16), q256 = crot(e256);
    LAS char* tw = sm + ch * TCH + (tid & 127) * 4;
    auto put = [&](int row, f2 v) { *(volatile LAS u32*)(tw + row * TROW) = pk2(v.x, v.y); };
    f2 a = e16, t0 = cb, tb[8], v[8];
    put(0, (f2){1.f, 0.f}); put(16, t0);
    tb[0] = cb;
#pragma unroll
    for (int i = 1; i < 16; ++i) {
      put(i, a); if (i < 15) a = cmul(a, e16, q16);
      t0 = cmul(t0, e1, q1); put(16 + i, t0);
      if (i < 8) { tb[i] = cmul(tb[i - 1], e256, q256); v[i] = tb[i]; }
#pragma unroll
      for (int tl = 1; tl < 8; ++tl) {
        const int base = tl == 1 ? 32 : (tl < 4 ? 41 + 5 * (tl - 2) : 51 + 3 * (tl - 4));
        const int cnt = tl == 1 ? 9 : (tl < 4 ? 5 : 3), k = i - tl;
        if (k >= 0 && k < cnt) {
          if (k > 0) v[tl] = cmul(v[tl], tl == 1 ? e2 : (tl < 4 ? e4 : e8), tl == 1 ? q2 : (tl < 4 ? q4 : q8));
          put(base + k, v[tl]);
        }
      }
    }
  }
  fence_sched();
  wait_vm<0>();
  flag_store(WS_UFLAG + (u32)(cg * 128 + tt * 4 + wave) * FSP, rws, tag, lane);
  STAMP(2);
  lds_bar();

  f4 kk[4];
  {
    LAS char* tb = sm + ch * TCH + g * 16;
    const int m2 = (n + ((n >> 1) & 1)) >> 1, m4 = (n + 1 + ((n >> 2) & 1)) >> 2, m8 = (n + 3 + ((n >> 3) & 1)) >> 3;
    LAS char* pr_[4] = {tb + (half ? 51 + m8 : 16 + n) * TROW, tb + (half ? 54 + m8 : 32 + m2) * TROW,
                        tb + (half ? 57 + m8 : 41 + m4) * TROW, tb + (half ? 60 + m8 : 46 + m4) * TROW};
    LAS char* pe = tb + n * TROW;
#pragma unroll
    for (int i = 0; i < 4; ++i) kk[i] = (f4)(0.f);
#pragma unroll
    for (int ks = 0; ks < 8; ++ks) {
      const bf8 e = lds_rd128(pe + ks * 64);
#pragma unroll
      for (int i = 0; i < 4; ++i) kk[i] = mfma16(lds_rd128(pr_[i] + ks * 64), e, kk[i]);
    }
  }
  fence_sched();
  STAMP(3);
  flag_wait(WS_UFLAG + (u32)(cg * 128 + wave * 32 + (lane & 31)) * FSP, rws, tag);
  STAMP(4);
  v4u uv[2];
  {
    const u32 vo = WS_UT + (u32)((pr >> 1) * 256 + 64 * wave + (lane >> 1)) * 64 + ((pr & 1) * 2 + (lane & 1)) * 16;
    uv[0] = ld128_sc1<0>(vo, rws, 0); uv[1] = ld128_sc1<2048>(vo, rws, 0);
  }
  lds_bar();
  {
    LAS char* kb = sm + ch * KCH;
#pragma unroll
    for (int i = 0; i < 4; ++i) {
      const int l0 = 256 * (4 * half + i) + 16 * n + 4 * g;
      const u32 a = pk2(kk[i][3], kk[i][2]), bq = pk2(kk[i][1], kk[i][0]), c = pk2(kk[i][2], kk[i][1]);
      *(LAS v2u*)(kb + KOO + 2 * (2044 - l0)) = (v2u){a, bq};
      *(LAS u16*)(kb + 2 * (2051 - l0)) = (u16)a;
      *(LAS u32*)(kb + 2 * (2052 - l0)) = c;
      *(LAS u16*)(kb + 2 * (2054 - l0)) = (u16)(bq >> 16);
    }
    const v4u z = {0, 0, 0, 0};
    if (lane < 29) *(LAS v4u*)(kb + (half ? KOO + 4096 : 4112) + lane * 16) = z;
    else if (lane < 33) *(LAS v4u*)(sm + UA0 + ch * UCH + (half ? UBO : 0) + (lane - 29) * 16 - (half ? 0 : 16)) = z;
    else if (lane == 33 && !half) *(LAS u16*)(kb + 2 * 2055) = 0;
    else if (lane == 34 && !half) *(LAS v3u*)(sm + UA0 + ch * UCH + 48) = (v3u){0, 0, 0};
  }
  asm volatile("s_waitcnt vmcnt(0)" : "+v"(uv[0]), "+v"(uv[1]) :: "memory");
#pragma unroll
  for (int k = 0; k < 2; ++k) {
    LAS char* ub = sm + UA0 + (lane & 1) * UCH + 64 + (64 * wave + 32 * k + (lane >> 1)) * 16;
    *(LAS v4u*)(ub + UBO) = uv[k];
    *(LAS u32*)(ub - 4) = uv[k].x;
    *(LAS v3u*)(ub) = (v3u){uv[k].y, uv[k].z, uv[k].w};
  }
  lds_bar();
  STAMP(5);

  f4 y[8];
  {
    LAS char* pk = sm + ch * KCH + ((n & 1) ? KOO + 4032 : 4048) - 64 * (n >> 1) + 16 * g - 3584 + 256 * half;
    LAS char* pu = sm + UA0 + ch * UCH + (n < 8 ? 8 * n : UBO + 8 * (n - 8) + 8) + 16 * g + 256 * half;
#pragma unroll
    for (int a = 0; a < 8; ++a) y[a] = (f4)(0.f);
#pragma unroll
    for (int rr = 0; rr < 4; ++rr) {
      bf8 kf[8], uf[8];
#pragma unroll
      for (int i = 0; i < 8; ++i) {
        kf[i] = lds_rd128(pk + 512 * (7 - i) + 64 * rr);
        const v2u lo = *(LAS v2u*)(pu + 512 * i + 64 * rr), hi = *(LAS v2u*)(pu + 512 * i + 64 * rr + 8);
        uf[i] = __builtin_bit_cast(bf8, (v4u){lo.x, lo.y, hi.x, hi.y});
      }
#pragma unroll
      for (int s = 0; s < 8; ++s)
#pragma unroll
        for (int m = 0; m <= s; ++m) y[s] = mfma16(kf[s - m], uf[m], y[s]);
    }
  }
  STAMP(6);
#pragma unroll
  for (int a = 0; a < 8; ++a) *(LAS f4*)(sm + EX + (wave * 8 + a) * 1024 + lane * 16) = y[a];
  lds_bar();
  {
    const float d0 = bflo(r_dsk), d1 = bfhi(r_dsk);
    const int rn = n < 8 ? 2 * n : 2 * (n - 8) + 1;
    const bool lo = n < 8;
#pragma unroll
    for (int i = 0; i < 2; ++i) {
      const int al = 2 * wave + i;
      LAS char* ex = sm + EX + al * 1024 + lane * 16;
      const f4 y0 = *(LAS f4*)(ex) + *(LAS f4*)(ex + 8192), y1 = *(LAS f4*)(ex + 16384) + *(LAS f4*)(ex + 24576);
      LAS char* up = sm + UA0 + UBO + 64 + (256 * al + 2 * rn + 64 * g) * 2;
      const u32 u00 = *(LAS u32*)(up), u01 = *(LAS u32*)(up + 64), u10 = *(LAS u32*)(up + UCH), u11 = *(LAS u32*)(up + UCH + 64);
      const u32 x0 = pk2(y0[0] + bflo(u00) * d0, y1[0] + bflo(u10) * d1), x1 = pk2(y0[1] + bfhi(u00) * d0, y1[1] + bfhi(u10) * d1);
      const u32 z0 = pk2(y0[2] + bflo(u01) * d0, y1[2] + bflo(u11) * d1), z1 = pk2(y0[3] + bfhi(u01) * d0, y1[3] + bfhi(u11) * d1);
      const u32 s0 = lo ? z0 : x0, s1 = lo ? z1 : x1;
      const u32 r0 = __builtin_amdgcn_update_dpp(0u, s0, 0x128, 0xf, 0xf, false), r1 = __builtin_amdgcn_update_dpp(0u, s1, 0x128, 0xf, 0xf, false);
      const v4u d = lo ? (v4u){x0, x1, r0, r1} : (v4u){r0, r1, z0, z1};
      st128_sc1<0>(d, WS_A + (u32)(4 * al + g) * 65536 + pr * 256 + n * 16, rws, 0);
    }
  }
  wait_vm<0>();
  flag_store(WS_AFLAG + (u32)(wave * 256 + pr) * FSP, rws, tag, lane);
  STAMP(7);
  if (ci == 0) {
    flag_wait(WS_AFLAG + (u32)tid * FSP, rws, tag);
    flag_wait(tid < 224 ? WS_HFLAG + (u32)((tid / 14) * 64 + (tid % 14) * 2) * FSP : WS_GFLAG + (u32)(tid - 224) * 4 * FSP, rws, tag);
    lds_bar();
    if (wave == 0) flag_store(0, rws, tag, lane);
    for (int it = 0; it < 100000; ++it) { u64 t; asm volatile("s_memrealtime %0\n\ts_waitcnt lgkmcnt(0)" : "=s"(t) :: "memory"); if (t - tb0 >= DELAY) break; }
  }
  STAMP_OUT;
}

__global__ __launch_bounds__(256, 2) void fused_kernel(const u16* __restrict__ x, const u16* __restrict__ are, const u16* __restrict__ aim,
                                                       const u16* __restrict__ bre, const u16* __restrict__ bim, const u16* __restrict__ cre,
                                                       const u16* __restrict__ cim, const u16* __restrict__ logdt, const u16* __restrict__ dskip,
                                                       const u16* __restrict__ w1, const u16* __restrict__ b1, const u16* __restrict__ w2,
                                                       const u16* __restrict__ b2, const u16* __restrict__ gw, u16* __restrict__ out, char* ws) {
  __shared__ __attribute__((aligned(256))) char smem[81920];
  LAS char* sm = (LAS char*)smem;
  u64 tb0; asm volatile("s_memrealtime %0" : "=s"(tb0) :: "memory");
  asm volatile("s_waitcnt lgkmcnt(0)" : "+s"(tb0) : "s"(x), "s"(ws) : "memory");
  asm volatile("" :: "s"(x), "s"(are), "s"(aim), "s"(bre), "s"(bim), "s"(cre), "s"(cim), "s"(logdt), "s"(dskip), "s"(w1), "s"(b1),
               "s"(w2), "s"(b2), "s"(gw), "s"(out), "s"(ws));
  const int b = blockIdx.x;
  if (b >= 256) gemm_role(b - 256, sm, x, w1, b1, w2, b2, gw, out, ws);
  else conv_role(b, sm, x, are, aim, bre, bim, cre, cim, logdt, dskip, ws, tb0);
}

extern "C" void kernel_launch(void* const* d_in, const int* in_sizes, int n_in, void* d_out, int out_size, void* d_ws, size_t ws_size,
                              hipStream_t stream) {
  const u16* const* in = (const u16* const*)d_in;
  fused_kernel<<<512, 256, 0, stream>>>(in[0], in[1], in[2], in[3], in[4], in[5], in[6], in[7], in[8], in[10], in[11], in[12], in[13],
                                        in[14], (u16*)d_out, (char*)d_ws);
}
```

```cpp
#include <hip/hip_runtime.h>
#include <stdint.h>
#include <type_traits>
#pragma clang diagnostic ignored "-Winline-asm"

typedef unsigned short u16;
typedef unsigned int u32;
typedef unsigned long long u64;
typedef __attribute__((ext_vector_type(2))) u32 v2u;
typedef __attribute__((ext_vector_type(3))) u32 v3u;
typedef __attribute__((ext_vector_type(4))) u32 v4u;
typedef __attribute__((ext_vector_type(2))) float f2;
typedef __attribute__((ext_vector_type(4))) float f4;
typedef __attribute__((ext_vector_type(16))) float f16v;
typedef __attribute__((ext_vector_type(2))) __bf16 bf2;
typedef __attribute__((ext_vector_type(8))) __bf16 bf8;
typedef __attribute__((ext_vector_type(4))) short s4;
typedef __amdgpu_buffer_rsrc_t rsrc_t;
#define DI __device__ __forceinline__
#define LAS __attribute__((address_space(3)))
#define CAS __attribute__((address_space(4)))

#define GFIRST 0
#define LAM 4
#define UPP 2
#define PA 110
#define PC 127
#define PD 8
#define KP 6
#define KC 9
#define KA 19
#define KB 25
#define NT 2
#define W2PF 0
#define PRG1 2
#define PRG2 1
#define PRC0 3
#define PRC1 1
#define PRCV 2
#define UFP 8

constexpr u32 FSP = 256;
constexpr u32 WS_G = 4096, WS_EFLAG = 65536, WS_UFLAG = WS_EFLAG + 1024 * FSP;
constexpr u32 WS_UT = 1u << 20, WS_A = 3u << 20, WS_H = 7u << 20, WS_END = 15u << 20;
constexpr int SPIN_MAX = 1 << 16;
constexpr int A0 = 0;

#define STAMP(i)
#define STAMPQ(i)
#define STAMPC(i)
#define STAMP_DECL
#define STAMP_OUT

DI rsrc_t mkrsrc(const void* p, u32 bytes) { return __builtin_amdgcn_make_buffer_rsrc((void*)p, (short)0, (int)bytes, 0x00020000); }
DI float bflo(u32 v) { return __uint_as_float(v << 16); }
DI float bfhi(u32 v) { return __uint_as_float(v & 0xffff0000u); }
DI u32 pk2(float a, float b) { bf2 r = __builtin_convertvector((f2){a, b}, bf2); return __builtin_bit_cast(u32, r); }
DI float dot2(u32 a, u32 b, float c) { return __builtin_amdgcn_fdot2_f32_bf16(__builtin_bit_cast(bf2, a), __builtin_bit_cast(bf2, b), c, false); }
DI float relu(float v) { return __int_as_float(max(__float_as_int(v), 0)); }
DI void fence_sched() { __builtin_amdgcn_sched_barrier(0); }

template <int N> DI void wait_vm() { asm volatile("s_waitcnt vmcnt(%0)" :: "n"(N) : "memory"); }
template <int N> DI void wait_vm_bar() { asm volatile("s_waitcnt vmcnt(%0) lgkmcnt(0)\n\ts_barrier" :: "n"(N) : "memory"); }
DI void lds_bar() { asm volatile("s_waitcnt lgkmcnt(0)\n\ts_barrier" ::: "memory"); }
DI void wait_lgkm() { asm volatile("s_waitcnt lgkmcnt(0)" ::: "memory"); }

DI void dma2(u32 m0v, u32 v0, u32 v1, rsrc_t rs, u32 so) {
  asm volatile("s_mov_b32 m0, %0\n\ts_nop 0\n\t"
               "buffer_load_dwordx4 %1, %3, %4 offen lds\n\t"
               "buffer_load_dwordx4 %2, %3, %4 offen offset:1024 lds"
               :: "s"(m0v), "v"(v0), "v"(v1), "s"(rs), "s"(so) : "m0", "memory");
}
template <u32 off> DI void set_m0(u32 base) { asm volatile("s_add_u32 m0, %0, %1" :: "s"(base), "n"(off) : "m0", "scc"); }
template <int OFF> DI void dma1(u32 v, rsrc_t rs, u32 so) {
  asm volatile("buffer_load_dwordx4 %0, %1, %2 offen offset:%3 lds" :: "v"(v), "s"(rs), "s"(so), "n"(OFF) : "memory");
}
template <int OFF> DI void reld128_sc1(v4u& r, u32 vo, rsrc_t rs, u32 so) {
  asm volatile("buffer_load_dwordx4 %0, %1, %2, %3 offen offset:%4 sc1" : "+v"(r) : "v"(vo), "s"(rs), "s"(so), "n"(OFF) : "memory");
}
template <int OFF> DI u32 ld32_sc1(u32 vo, rsrc_t rs, u32 so) {
  u32 r; asm volatile("buffer_load_dword %0, %1, %2, %3 offen offset:%4 sc1" : "=&v"(r) : "v"(vo), "s"(rs), "s"(so), "n"(OFF) : "memory"); return r;
}
template <int OFF> DI v4u ld128_sc1(u32 vo, rsrc_t rs, u32 so) {
  v4u r; asm volatile("buffer_load_dwordx4 %0, %1, %2, %3 offen offset:%4 sc1" : "=&v"(r) : "v"(vo), "s"(rs), "s"(so), "n"(OFF) : "memory"); return r;
}
DI v4u ld128(u32 vo, rsrc_t rs) {
  v4u r; asm volatile("buffer_load_dwordx4 %0, %1, %2, 0 offen" : "=&v"(r) : "v"(vo), "s"(rs) : "memory"); return r;
}
DI v2u ld64_sc1(u32 vo, rsrc_t rs) {
  v2u r; asm volatile("buffer_load_dwordx2 %0, %1, %2, 0 offen sc1" : "=&v"(r) : "v"(vo), "s"(rs) : "memory"); return r;
}
template <int OFF> DI u32 ld16(u32 vo, rsrc_t rs) {
  u32 r; asm volatile("buffer_load_ushort %0, %1, %2, 0 offen offset:%3" : "=&v"(r) : "v"(vo), "s"(rs), "n"(OFF) : "memory"); return r;
}
template <int OFF> DI void st128_sc1(v4u d, u32 vo, rsrc_t rs, u32 so) {
  asm volatile("buffer_store_dwordx4 %0, %1, %2, %3 offen offset:%4 sc1\n\ts_nop 1" :: "v"(d), "v"(vo), "s"(rs), "s"(so), "n"(OFF) : "memory");
}
DI void st64_sc1(v2u d, u32 vo, rsrc_t rs) {
  asm volatile("buffer_store_dwordx2 %0, %1, %2, 0 offen sc1" :: "v"(d), "v"(vo), "s"(rs) : "memory");
}
DI void flag_store(u32 off, rsrc_t rs, u64 tag) {
  const v2u d = {(u32)tag, (u32)(tag >> 32)};
  asm volatile("s_mov_b64 exec, 1\n\tbuffer_store_dwordx2 %0, off, %1, %2 sc1\n\ts_mov_b64 exec, -1" :: "v"(d), "s"(rs), "s"(off) : "memory");
}
DI u64 epoch_tag(v2u e) {
  const u64 v = (u64)(u32)__builtin_amdgcn_readfirstlane(e.x) | ((u64)(u32)__builtin_amdgcn_readfirstlane(e.y) << 32);
  return ((v * 0x9E3779B97F4A7C15ull + 0x632BE59BD9B4E019ull) & ~0xffffull) | (~v & 0xffffull);
}
constexpr u32 SGN = 0x80008000u;
DI u32 sign_of(u64 tag, int t) { return ((((u32)tag >> (t >> 1)) ^ t) & 1) ? SGN : 0u; }
DI u32 strip(v4u& d, u32 s) { d ^= s; return (d.x | d.y) | (d.z | d.w); }
DI bool any(u32 m) { return __builtin_amdgcn_ballot_w64(m != 0) != 0; }
DI bool flag_bad(v2u v, u64 tag) { return __builtin_amdgcn_ballot_w64((v.x != (u32)tag) | (v.y != (u32)(tag >> 32))) != 0; }
DI void flag_wait(u32 vo, rsrc_t rs, u64 tag) {
#pragma unroll 1
  for (int it = 0; it < SPIN_MAX; ++it) {
    v2u v; asm volatile("buffer_load_dwordx2 %0, %1, %2, 0 offen sc1\n\ts_waitcnt vmcnt(0)" : "=&v"(v) : "v"(vo), "s"(rs) : "memory");
    if (!flag_bad(v, tag)) break;
    __builtin_amdgcn_s_sleep(1);
  }
}

DI void granule_wait(u32 vo, rsrc_t rs, u32 tg) {
#pragma unroll 1
  for (int it = 0; it < SPIN_MAX; ++it) {
    v2u v; asm volatile("buffer_load_dwordx2 %0, %1, %2, 0 offen sc1\n\ts_waitcnt vmcnt(0)" : "=&v"(v) : "v"(vo), "s"(rs) : "memory");
    if (__builtin_amdgcn_ballot_w64(v.y != tg) == 0) break;
    __builtin_amdgcn_s_sleep(1);
  }
}
DI float opaque(float v) { asm("" : "+v"(v)); return v; }

DI bf8 lds_rd128(LAS char* p) { return *(LAS bf8*)p; }
DI bf8 lds_tr2(LAS char* p0, LAS char* p1) {
  s4 a = __builtin_amdgcn_ds_read_tr16_b64_v4i16((LAS s4*)p0);
  s4 b = __builtin_amdgcn_ds_read_tr16_b64_v4i16((LAS s4*)p1);
  v2u x = __builtin_bit_cast(v2u, a), y = __builtin_bit_cast(v2u, b);
  return __builtin_bit_cast(bf8, (v4u){x.x, x.y, y.x, y.y});
}
DI f16v mfma32(bf8 a, bf8 b, f16v c) { return __builtin_amdgcn_mfma_f32_32x32x16_bf16(a, b, c, 0, 0, 0); }
DI f4 mfma16(bf8 a, bf8 b, f4 c) { return __builtin_amdgcn_mfma_f32_16x16x32_bf16(a, b, c, 0, 0, 0); }

template <int I, int N, class F> DI void static_for(F&& f) {
  if constexpr (I < N) { f(std::integral_constant<int, I>{}); static_for<I + 1, N>(f); }
}

constexpr u32 REG = 20480;
constexpr int wblk(int t) { constexpr int b[16] = {6, 7, 8, 9, 6, 7, 8, 9, 6, 7, 4, 5, 8, 0, 1, 9}; return b[t]; }
constexpr int rblk(int k) { constexpr int b[10] = {2, 3, 6, 7, 4, 5, 8, 0, 1, 9}; return b[k % 10]; }
constexpr u32 w2row(int k) { return 128 * ((k % 14) >> 1) + 64 * (k / 14) + 8 * (k & 1); }
constexpr int hstep(int k) { return 8 * ((k % 14) >> 1) + 4 * (k / 14) + (k & 1); }

enum { OX, OW, OR, OS, OE, OH, OP, OA };
struct Op { int pos, kind, idx, sub; };
struct OpTab {
  Op op[256]; int n;
  constexpr void add(int pos, int kind, int idx, int sub) { op[n] = Op{pos, kind, idx, sub}; ++n; }
  constexpr int at(int kind, int idx, int sub) const {
    for (int i = 0; i < n; ++i) if (op[i].kind == kind && op[i].idx == idx && op[i].sub == sub) return i;
    return -1;
  }
  constexpr int from(int pos) const { int i = 0; while (i < n && op[i].pos < pos) ++i; return i; }
  constexpr int younger(int kind, int idx, int sub, int pos) const {
    int c = 0, a = at(kind, idx, sub);
    if (a < 0 || op[a].pos >= pos) return -1000;
    for (int i = a + 1; i < n; ++i) if (op[i].pos < pos) ++c;
    return c > 63 ? 63 : c;
  }
  constexpr int lds(int i) const {
    const Op o = op[i];
    return o.kind == OX ? (o.idx % 3) * 4096 + (o.sub >> 1) * 2048 : o.kind == OW ? wblk(o.idx) * 2048 : o.kind == OR ? rblk(o.idx) * 2048 : -1;
  }
  constexpr int m0_at(int pos) const {
    int cur = -1;
    for (int i = 0; i < n; ++i) if (lds(i) >= 0) { if (op[i].pos == pos) return lds(i) != cur ? lds(i) : -1; cur = lds(i); }
    return -1;
  }
  constexpr bool m0_ok() const {
    for (int i = 0; i < n; ++i) for (int j = i + 1; j < n; ++j)
      if (op[i].pos == op[j].pos && op[i].pos >= 0 && lds(i) >= 0 && lds(j) >= 0 && lds(i) != lds(j)) return false;
    return true;
  }
};
constexpr int EP0 = 61, G2P = 200;
constexpr int mn(int a, int b) { return a < b ? a : b; }
template <bool AUX> constexpr OpTab optab() {
  OpTab t{};
  int nh = 0;
  if (!AUX) {
    for (int c = 0; c < 3; ++c) { for (int s = 0; s < 4; ++s) t.add(-1, OX, c, s); t.add(-1, OW, c, 0); t.add(-1, OW, c, 1); }
    t.add(-1, OW, 3, 0); t.add(-1, OW, 3, 1);
    for (int n = 0; n < 128; ++n) {
      const int T = (n + LAM) / 8, i = (n + LAM) % 8;
      if (T >= 1 && T <= 5) { if (i < 4) t.add(n, OX, T + 2, i); else if (i < 6) t.add(n, OW, T + 3, i - 4); }
      if (T >= 6 && T <= 7 && i < 6) t.add(n, OW, 9 + 3 * (T - 6) + i / 2, i & 1);
      if (T == 8 && i < 6) { if (i < 2) t.add(n, OW, 15, i); else t.add(n, OR, i / 2 - 1, i & 1); }
      if (T >= 9 && i < 2) t.add(n, OR, T - 7, i);
      for (int s = 0; s < 4; ++s) if (EP0 + (4 * s + 3) / UPP == n) t.add(n, OS, s, 0);
      if (n == EP0 + 15 / UPP + 1) t.add(n, OE, 0, 0);
      if (n == PA) t.add(n, OP, 0, 0);
      if (n >= PC && nh < 2 * PD) { t.add(n, OH, nh >> 1, nh & 1); ++nh; }
    }
    for (; nh < 2 * PD; ++nh) t.add(128, OH, nh >> 1, nh & 1);
    for (int s = 4; s < 8; ++s) t.add(129 + s, OS, s, 0);
  } else {
    for (; nh < 2 * PD; ++nh) t.add(-1, OH, nh >> 1, nh & 1);
  }
  int na = 0;
  for (int k = 0; k < 28 - NT; ++k)
    for (int g = 0; g < 4; ++g) {
      const int pos = G2P + 4 * k + g;
      const bool ring = (g == 1 || g == 2) && k + 10 < 28;
      if (ring) t.add(pos, OR, k + 10, g - 1);
      if (k == KP && g == 0) t.add(pos, OP, 1, 0);
      const int lim = mn(k < KC ? 28 : 56, 2 * (k + PD) + (g == 3 ? 2 : 1));
      int room = ring ? 0 : 1;
      if (k >= KC && nh + 2 < lim) ++room;
      for (; room > 0 && nh < lim; --room, ++nh) t.add(pos, OH, nh >> 1, nh & 1);
      if (k >= KA && na < 6 && room > 0) { t.add(pos, OA, 0, na); ++na; }
    }
  return t;
}
constexpr OpTab GT[2] = {optab<false>(), optab<true>()};
constexpr int g1wait(int T) {
  const int pos = T ? 8 * T - LAM : 0;
  return mn(GT[0].younger(OW, T, 1, pos), T < 8 ? GT[0].younger(OX, T, 3, pos) : 63);
}
template <bool AUX> constexpr int g2wait(int k) {
  const int pos = G2P + 4 * k, kl = k == 27 - NT ? 27 : k + 1;
  return mn(GT[AUX].younger(OH, kl, 1, pos), (AUX && kl < 10) ? 63 : GT[AUX].younger(OR, kl, 1, pos));
}
template <bool AUX> constexpr bool sched_ok() {
  for (int k = 0; k < 28 - NT; ++k) if (g2wait<AUX>(k) < 0) return false;
  for (int T = 0; T < 16; ++T) if (g1wait(T) < 0) return false;
  return GT[AUX].m0_ok() && GT[AUX].younger(OA, 0, 5, G2P + 4 * KB) >= 0 && GT[AUX].younger(OP, 1, 0, G2P + 4 * KC) >= 0;
}
static_assert(sched_ok<false>() && sched_ok<true>() && GT[0].at(OW, 15, 1) < GT[0].at(OS, 0, 0), "schedule");
static_assert(KP < KC && KC + 1 < 14 && KB < 28 - NT && KB <= 25 && KB >= 19 && NT >= 2 && NT <= 4, "schedule");

template <int N> DI void wait_pin(v4u& a, v4u& b) { asm volatile("s_waitcnt vmcnt(%2)" : "+v"(a), "+v"(b) : "n"(N) : "memory"); }
template <int N> DI void wait_pin(v2u& a) { asm volatile("s_waitcnt vmcnt(%1)" : "+v"(a) : "n"(N) : "memory"); }
template <int N> DI void wait_pin(u32& a) { asm volatile("s_waitcnt vmcnt(%1)" : "+v"(a) : "n"(N) : "memory"); }
template <int N> DI void wait_pin(v4u (&a)[6]) {
  asm volatile("s_waitcnt vmcnt(%6)" : "+v"(a[0]), "+v"(a[1]), "+v"(a[2]), "+v"(a[3]), "+v"(a[4]), "+v"(a[5]) : "n"(N) : "memory");
}
DI void pin(v4u& a, v4u& b) { asm volatile("" : "+v"(a), "+v"(b)); }
template <int I> using IC = std::integral_constant<int, I>;

template <bool AUX>
DI void gemm_role(int b, LAS char* sm, const u16* x, const u16* w1, const u16* b1, const u16* w2, const u16* b2, const u16* gw,
                  u16* out, char* ws) {
  constexpr const OpTab& TB = GT[AUX];
  const int lane = threadIdx.x & 63, wave = __builtin_amdgcn_readfirstlane(threadIdx.x >> 6);
  STAMP_DECL;
  STAMP(0);
  const int xcd = b & 7, r = b >> 3;
  const rsrc_t rws = mkrsrc(ws, WS_END), rx = mkrsrc(x, 2048 * 512 * 2), rw2 = mkrsrc(w2, 1792 * 512 * 2), rb2 = mkrsrc(b2, 512 * 2);
  const rsrc_t rw1 = mkrsrc(w1, 512 * 1792 * 2);
  v2u ep = ld64_sc1(0, rws);
  const int g = lane >> 4, q = (lane >> 2) & 3, p = lane & 3, l31 = lane & 31, hh = lane >> 5, l3 = lane >> 3, l7 = lane & 7;
  const u32 mreg = (u32)(uintptr_t)sm + wave * REG;
  const int t2 = 4 * xcd + (r >> 3), c2 = r & 7;
  const int tau = 4 * (xcd >> 1) + r / 7, j = 7 * (xcd & 1) + r % 7, rot = (r % 7) + 2 * (r / 7);

  const u32 wch = ((l7 ^ ((g & 1) << 2)) << 4);
  const u32 vx0 = (u32)(128 * tau + 32 * wave + l3) * 1024 + ((l7 ^ g) << 4), vx1 = (vx0 ^ 64) + 7168;
  const u32 vw0 = (u32)(16 * wave + l3) * 3584 + j * 256 + wch, vw1 = vw0 + 8 * 3584 - 1024;
  const u32 vv0 = (u32)(896 * (wave >> 1) + 32 * (wave & 1) + l3) * 1024 + c2 * 128 + wch, vv1 = vv0 + 15360;
  const u32 vh = WS_H + (u32)((4 * tau + wave) * 112 + 8 * j) * 1024 + lane * 16;
  const u32 vhh = WS_H + (u32)(2 * t2 * 112 + 56 * (wave >> 1) + 2 * (wave & 1)) * 1024 + lane * 16;
  const u32 vp = vhh + (u32)((l3 & 1) * 112 + 8 * min(l7, 6) + 1) * 1024;
  const u32 va = WS_A + (u32)t2 * 131072 + (u32)(32 * c2 + 4 * l3) * 512 + (u32)(16 * wave + 2 * l7) * 8;
  const u32 vg = WS_G + (u32)(64 * t2 + 16 * wave + 2 * l3) * 8;
  u32 aoff[2];
#pragma unroll
  for (int mi = 0; mi < 2; ++mi) aoff[mi] = (8 * (g >> 1) + q) * 128 + (((4 * mi + 2 * (p & 1) + (g & 1)) ^ ((q >> 1) << 2)) << 4) + (p >> 1) * 8;

  u64 tag;
  u32 sg, pf;
  v4u hd[8], hb[28][2], av[6];
  auto hload = [&](auto K, auto S, auto AGAIN) {
    constexpr int k = decltype(K)::value, s = decltype(S)::value, ks = hstep(k);
    if constexpr (decltype(AGAIN)::value) reld128_sc1<(ks & 3) * 1024>(hb[k][s], vhh, rws, (ks >> 2) * 4096 + s * 114688);
    else hb[k][s] = ld128_sc1<(ks & 3) * 1024>(vhh, rws, (ks >> 2) * 4096 + s * 114688);
  };
  auto aload = [&](auto S) {
    constexpr int s = decltype(S)::value;
    if constexpr (s < 4) av[s] = ld128_sc1<s * 512>(va, rws, 0);
    else if constexpr (s == 4) av[4] = ld128_sc1<0>(vg, rws, 0);
    else av[5] = ld128((u32)(64 * c2 + 8 * l7) * 2, rb2);
  };
  auto issue = [&](auto I) {
    constexpr Op o = TB.op[decltype(I)::value];
    constexpr int lo = (o.sub & 1) * 1024;
    if constexpr (o.pos < 0 && TB.lds(decltype(I)::value) >= 0 && lo == 0) { set_m0<TB.lds(decltype(I)::value)>(mreg); asm volatile("s_nop 0"); }
    if constexpr (o.kind == OX) dma1<lo>(lo ? vx1 : vx0, rx, (u32)((o.idx + rot) & 7) * 128 + (o.sub >> 1) * 16384);
    if constexpr (o.kind == OW) dma1<lo>(lo ? vw1 : vw0, rw1, (u32)((o.idx + rot) & 7) * (64 * 3584) + (o.idx >> 3) * 128);
    if constexpr (o.kind == OR) dma1<lo>(lo ? vv1 : vv0, rw2, w2row(o.idx) * 1024);
    if constexpr (o.kind == OS) st128_sc1<(o.idx & 3) * 1024>(hd[o.idx], vh, rws, (o.idx >> 2) * 4096);
    if constexpr (o.kind == OE) flag_store(WS_EFLAG + (u32)(4 * b + wave) * FSP, rws, tag);
    if constexpr (o.kind == OP) pf = ld32_sc1<0>(vp, rws, o.idx * 4096);
    if constexpr (o.kind == OH) hload(IC<o.idx>{}, IC<o.sub>{}, IC<0>{});
    if constexpr (o.kind == OA) aload(IC<o.sub>{});
    if constexpr (o.pos < 0 && o.kind == OW && o.sub == 1 && o.idx < 2) asm volatile("s_barrier");
  };
  auto issue_at = [&](auto N) {
    static_for<TB.from(decltype(N)::value), TB.from(decltype(N)::value + 1)>(issue);
  };
  auto m0_for = [&](auto N) { if constexpr (TB.m0_at(decltype(N)::value) >= 0) set_m0<TB.m0_at(decltype(N)::value)>(mreg); };
  auto gate = [&](int ph) {
    if (__builtin_expect(any((pf ^ sg) & SGN), 0)) {
#pragma unroll 1
      for (int it = 0; it < SPIN_MAX; ++it) {
        __builtin_amdgcn_s_sleep(1);
        pf = ld32_sc1<0>(vp, rws, ph * 4096);
        wait_pin<0>(pf);
        if (!any((pf ^ sg) & SGN)) break;
      }
    }
  };

  if constexpr (!AUX) {
    const rsrc_t rb1 = mkrsrc(b1, 1792 * 2);
    const u32 vb = (u32)(128 * j + 32 * hh + 16 * ((l31 >> 2) & 1) + 4 * (l31 >> 3) + p) * 2;
    u32 bq0 = ld16<0>(vb, rb1), bq1 = ld16<128>(vb, rb1);
    f16v acc[4];
    issue_at(IC<-1>{});
    __builtin_amdgcn_s_setprio(PRG1);

    LAS char* pa[2][2]; LAS char* pb[4];
#pragma unroll
    for (int mi = 0; mi < 2; ++mi) { pa[mi][0] = sm + aoff[mi]; pa[mi][1] = sm + aoff[mi] + 2 * REG; }
#pragma unroll
    for (int u = 0; u < 4; ++u) pb[u] = sm + wave * REG + l31 * 128 + (((2 * u + hh) ^ ((l31 >> 1) & 7)) << 4);

    asm volatile("s_waitcnt vmcnt(20)" : "+v"(ep), "+v"(bq0), "+v"(bq1) :: "memory");
    tag = epoch_tag(ep);
    sg = sign_of(tag, l31);
    {
      const bf8 o0 = __builtin_bit_cast(bf8, (v4u){hh ? 0u : 0x3f80u, 0, 0, 0}), o1 = __builtin_bit_cast(bf8, (v4u){hh ? 0x3f80u : 0u, 0, 0, 0});
      const bf8 a0 = __builtin_bit_cast(bf8, (v4u){bq0, 0, 0, 0}), a1 = __builtin_bit_cast(bf8, (v4u){bq1, 0, 0, 0});
      acc[0] = mfma32(a0, o0, (f16v)(0.f)); acc[1] = mfma32(a0, o1, (f16v)(0.f));
      acc[2] = mfma32(a1, o0, (f16v)(0.f)); acc[3] = mfma32(a1, o1, (f16v)(0.f));
    }
    constexpr auto ms = [](int n) constexpr { const int T = n >> 3, v = n & 7; return (T & 7) == 7 ? (v & 3) : (v >> 1); };
    constexpr auto mt = [](int n) constexpr { const int T = n >> 3, v = n & 7; return (T & 7) == 7 ? (v >> 2) : (v & 1); };
    bf8 af[128], xf[32];
    auto rda = [&](auto N) {
      constexpr int n = decltype(N)::value, u = ms(n);
      LAS char* a = pa[mt(n)][u >> 1] + (u & 1) * REG + wblk(n >> 3) * 2048;
      af[n] = lds_tr2(a, a + 512);
    };
    auto rdx = [&](auto S) { constexpr int s = decltype(S)::value; xf[s] = lds_rd128(pb[s & 3] + ((s >> 2) % 3) * 4096); };
    auto unit = [&](auto U) {
      constexpr int un = decltype(U)::value, tl = un >> 3, e = (un & 7) * 2;
      const float v0 = relu(acc[tl][e]), v1 = relu(acc[tl][e + 1]);
      hd[un >> 2][un & 3] = (pk2(opaque(v0 * v0), opaque(v1 * v1)) & ~SGN) | sg;
    };
    STAMP(1);
    fence_sched();
    wait_vm_bar<g1wait(0)>();
    fence_sched();
    STAMP(2); STAMPC(60);
    static_for<0, 4>(rdx);
    static_for<0, LAM>(rda);
    static_for<0, 128>([&](auto N) {
      constexpr int n = decltype(N)::value, T = (n + LAM) / 8, i = (n + LAM) % 8;
      fence_sched();
      if constexpr (i == 0) { if constexpr (T < 16) wait_vm_bar<g1wait(T)>(); else lds_bar(); }
      if constexpr (i == 0) { STAMPQ(15 + T); }
      if constexpr (n == PC) { wait_pin<TB.younger(OP, 0, 0, n)>(pf); gate(0); }
      m0_for(N);
      fence_sched();
      acc[2 * (n >> 6) + mt(n)] = mfma32(af[n], xf[(4 * (n >> 3) + ms(n)) & 31], acc[2 * (n >> 6) + mt(n)]);
      fence_sched();
      if constexpr (n + LAM < 128) rda(IC<n + LAM>{});
      if constexpr (T >= 1 && T < 8 && i < 4) rdx(IC<4 * T + i>{});
      if constexpr (n >= EP0 && n < EP0 + 16 / UPP) static_for<UPP * (n - EP0), UPP * (n - EP0 + 1)>(unit);
      if constexpr (n >= 64 + EP0) static_for<16 + UPP * (n - 64 - EP0), 16 + UPP * (n - 64 - EP0 + 1)>(unit);
      fence_sched();
      issue_at(N);
    });
    fence_sched();
    STAMP(5); STAMPC(61);
    issue_at(IC<128>{});
    static_for<16, 32>([&](auto U) {
      constexpr int un = decltype(U)::value;
      if constexpr (64 + EP0 + (un & 15) / UPP > 127) unit(U);
      if constexpr ((un & 3) == 3) { fence_sched(); issue_at(IC<129 + (un >> 2)>{}); fence_sched(); }
    });
    STAMP(6);
  } else {
    const int tg = xcd * 4 + (r - 28);
    const v4u gv = *(const v4u*)(gw + lane * 8);
    const u16* xr = x + (size_t)(tg * 64 + wave * 16) * 512 + lane * 8;
    float s[16];
#pragma unroll
    for (int i = 0; i < 16; ++i) {
      const v4u xv = *(const v4u*)(xr + i * 512);
      s[i] = 0.f;
#pragma unroll
      for (int k = 0; k < 4; ++k) s[i] = dot2(xv[k], gv[k], s[i]);
    }
#pragma unroll
    for (int i = 0; i < 8; ++i) {
      const v2u t = __builtin_amdgcn_permlane32_swap(__float_as_uint(s[i]), __float_as_uint(s[i + 8]), false, false);
      s[i] = opaque(__uint_as_float(t.x) + __uint_as_float(t.y));
    }
#pragma unroll
    for (int i = 0; i < 4; ++i) {
      const v2u t = __builtin_amdgcn_permlane16_swap(__float_as_uint(s[i]), __float_as_uint(s[i + 4]), false, false);
      float v = __uint_as_float(t.x) + __uint_as_float(t.y);
      v += __int_as_float(__builtin_amdgcn_update_dpp(0, __float_as_int(v), 0x128, 0xf, 0xf, false));
      v += __int_as_float(__builtin_amdgcn_update_dpp(0, __float_as_int(v), 0x124, 0xf, 0xf, false));
      v += __int_as_float(__builtin_amdgcn_update_dpp(0, __float_as_int(v), 0x122, 0xf, 0xf, false));
      v += __int_as_float(__builtin_amdgcn_update_dpp(0, __float_as_int(v), 0x121, 0xf, 0xf, false));
      s[i] = opaque(v);
    }
    const float m01 = opaque(p & 1 ? s[1] : s[0]), m23 = opaque(p & 1 ? s[3] : s[2]), mine = p & 2 ? m23 : m01;
    const float sl = bflo(pk2(mine, 0.f));
    const float gt = bflo(pk2(1.f / (1.f + __expf(-sl)), 0.f));
    fence_sched();
    asm volatile("s_waitcnt vmcnt(0)\n\ts_barrier" : "+v"(ep) :: "memory");
    tag = epoch_tag(ep);
    sg = sign_of(tag, l31);
    if (q == 0) st64_sc1((v2u){__float_as_uint(gt), (u32)(tag >> 32)}, WS_G + (u32)(tg * 64 + wave * 16 + 4 * g + p) * 8, rws);
    STAMP(1);
    static_for<0, 10>([&](auto I) { constexpr int i = decltype(I)::value; dma2(mreg + rblk(i) * 2048, vv0, vv1, rw2, w2row(i) * 1024); });
    STAMP(2);
    pf = ld32_sc1<0>(vp, rws, 0);
    wait_pin<0>(pf);
    gate(0);
    STAMP(4);
    issue_at(IC<-1>{});
  }
  __builtin_amdgcn_s_setprio(PRG2);

  const u32 tg32 = (u32)(tag >> 32);
  LAS char* pv[2] = {sm + wave * REG + aoff[0], sm + wave * REG + aoff[1]};
  auto vfrag = [&](int k, int mi) { LAS char* a = pv[mi] + rblk(k) * 2048; return lds_tr2(a, a + 512); };
  auto fresh = [&](auto K0, auto K1, u32 m) {
    constexpr int k0 = decltype(K0)::value, k1 = decltype(K1)::value, ke = mn(k1 | 1, 27);
    if (__builtin_expect(any(m & SGN), 0)) {
#pragma unroll 1
      for (int it = 0; it < SPIN_MAX; ++it) {
        static_for<k0, ke + 1>([&](auto K) { hload(K, IC<0>{}, IC<1>{}); hload(K, IC<1>{}, IC<1>{}); });
        wait_vm<0>();
        u32 z = 0;
        static_for<k0, ke + 1>([&](auto K) { constexpr int k = decltype(K)::value; pin(hb[k][0], hb[k][1]); if constexpr (k <= k1) z |= strip(hb[k][0], sg) | strip(hb[k][1], sg); });
        if (!any(z & SGN)) break;
        __builtin_amdgcn_s_sleep(1);
      }
    }
  };
  auto abad = [&]() {
    u32 m = 0;
#pragma unroll
    for (int i = A0; i < 5; ++i) m |= (av[i].y ^ tg32) | (av[i].w ^ tg32);
    return any(m);
  };
  LAS char* ap = sm + wave * REG + 9 * 2048;
  v4u a4[2];
  float ga[2][8], gs[2];
  auto aput = [&]() {
    *(LAS v4u*)(ap + l7 * 128 + ((l3 ^ l7) << 4)) = (v4u){av[0].x, av[1].x, av[2].x, av[3].x};
    *(LAS v4u*)(ap + l7 * 128 + ((l3 ^ l7) << 4) + 1024) = (v4u){av[0].z, av[1].z, av[2].z, av[3].z};
    a4[0] = *(LAS v4u*)(ap + l3 * 128 + ((l3 ^ l7) << 4));
    a4[1] = *(LAS v4u*)(ap + l3 * 128 + ((l3 ^ l7) << 4) + 1024);
  };
  auto amix = [&](int e, int k) {
    const float gt = __uint_as_float(e ? av[4].z : av[4].x), gn = opaque(1.f - gt);
    ga[e][2 * k] = opaque(__builtin_fmaf(gt, bflo(a4[e][k]), opaque(gn * bflo(av[5][k]))));
    ga[e][2 * k + 1] = opaque(__builtin_fmaf(gt, bfhi(a4[e][k]), opaque(gn * bfhi(av[5][k]))));
    if (k == 0) gs[e] = gn;
  };
  f16v o2[2][2];
  bf8 wb[28][2];
  bool late = false;
  fence_sched();
  wait_pin<mn(TB.younger(OH, 0, 1, G2P), AUX ? 63 : TB.younger(OR, 0, 1, G2P))>(hb[0][0], hb[0][1]);
  wb[0][0] = vfrag(0, 0); wb[0][1] = vfrag(0, 1);
  fresh(IC<0>{}, IC<0>{}, strip(hb[0][0], sg) | strip(hb[0][1], sg));
  STAMP(7);
  static_for<0, 28 - NT>([&](auto K) {
    constexpr int k = decltype(K)::value, kl = k == 27 - NT ? 27 : k + 1;
    fence_sched();
    wait_vm<g2wait<AUX>(k)>();
    static_for<k + 1, kl + 1>([&](auto J) { constexpr int jn = decltype(J)::value; pin(hb[jn][0], hb[jn][1]); });
    if constexpr (k == KC) { wait_pin<TB.younger(OP, 1, 0, G2P + 4 * k)>(pf); gate(1); }
    if constexpr (k == KB) { wait_pin<TB.younger(OA, 0, 5, G2P + 4 * k)>(av); }
    STAMPQ(32 + k);
    u32 m = 0;
    static_for<0, 4>([&](auto G) {
      constexpr int gi = decltype(G)::value, mi = gi & 1, ni = gi >> 1;
      m0_for(IC<G2P + 4 * k + gi>{});
      fence_sched();
      if constexpr (k == 0) o2[mi][ni] = mfma32(wb[k][mi], __builtin_bit_cast(bf8, hb[k][ni]), (f16v)(0.f));
      else o2[mi][ni] = mfma32(wb[k][mi], __builtin_bit_cast(bf8, hb[k][ni]), o2[mi][ni]);
      fence_sched();
      if constexpr (gi < 2) static_for<k + 1, kl + 1>([&](auto J) {
        constexpr int jn = decltype(J)::value;
        wb[jn][gi] = vfrag(jn, gi);
        m |= strip(hb[jn][gi], sg);
      });
      if constexpr (k == KB && gi == 0) aput();
      if constexpr (k == KB && gi == 2) late = abad();
      if constexpr (k > KB && k <= KB + 2) amix(k - KB - 1, gi);
      fence_sched();
      issue_at(IC<G2P + 4 * k + gi>{});
      if constexpr (gi == 2) fresh(IC<k + 1>{}, IC<kl>{}, m);
    });
  });
  fence_sched();
  LAS char* pw = sm + wave * REG + l31 * 256;
  const int kw = (4 * hh) ^ (((l31 >> 1) & 3) | ((l31 & 1) << 2));
  auto pput = [&](int tl, int i) {
    const f16v& a = o2[tl >> 1][tl & 1];
    *(LAS f4*)(pw + ((kw ^ i) << 4) + (tl >> 1) * 128 + (tl & 1) * 8192) = (f4){a[4 * i], a[4 * i + 1], a[4 * i + 2], a[4 * i + 3]};
  };
  static_for<0, 4 * NT>([&](auto N) {
    constexpr int n = decltype(N)::value, tl = n / NT, v = n % NT, k = 28 - NT + v;
    o2[tl >> 1][tl & 1] = mfma32(wb[k][tl >> 1], __builtin_bit_cast(bf8, hb[k][tl & 1]), o2[tl >> 1][tl & 1]);
    fence_sched();
    if constexpr (constexpr int u = 4 * (27 - NT - KB) + n; u < 8) amix(u >> 2, u & 3);
    if constexpr (tl >= 1 && v == NT - 1) { pput(tl - 1, 0); pput(tl - 1, 1); pput(tl - 1, 2); pput(tl - 1, 3); }
    fence_sched();
  });
  STAMP(10); STAMPC(62);
  if (__builtin_expect(late, 0)) {
#pragma unroll 1
    for (int it = 0; it < SPIN_MAX; ++it) {
      static_for<0, 5>(aload);
      wait_pin<0>(av);
      if (!abad()) break;
      __builtin_amdgcn_s_sleep(1);
    }
    aput();
#pragma unroll
    for (int i = 0; i < 8; ++i) amix(i >> 2, i & 3);
  }
#pragma unroll
  for (int i = 0; i < 4; ++i) pput(3, i);
  lds_bar();
  STAMP(11);
  {
    LAS char* pr = sm + (16 * wave + 2 * l3) * 256;
    const int kr = (2 * l7) ^ (l3 & 3);
#pragma unroll
    for (int e = 0; e < 2; ++e) {
      v4u d;
#pragma unroll
      for (int cc = 0; cc < 2; ++cc) {
        LAS char* a = pr + e * 256 + ((kr ^ cc ^ (4 * e)) << 4);
        const f4 s = (*(LAS f4*)(a) + *(LAS f4*)(a + REG)) + (*(LAS f4*)(a + 2 * REG) + *(LAS f4*)(a + 3 * REG));
#pragma unroll
        for (int k = 0; k < 2; ++k)
          d[2 * cc + k] = pk2(opaque(__builtin_fmaf(gs[e], s[2 * k], ga[e][4 * cc + 2 * k])), opaque(__builtin_fmaf(gs[e], s[2 * k + 1], ga[e][4 * cc + 2 * k + 1])));
      }
      *(v4u*)(out + (size_t)(64 * t2 + 16 * wave + 2 * l3 + e) * 512 + 64 * c2 + 8 * l7) = d;
    }
  }
  STAMP(12);
  STAMP_OUT;
}

struct cf { float x, y; };
DI cf cmul(cf a, cf m) { return cf{opaque(__builtin_fmaf(a.x, m.x, -opaque(a.y * m.y))), opaque(__builtin_fmaf(a.x, m.y, opaque(a.y * m.x)))}; }
DI cf cexp_s(float lr, float rv, float m) {
  const float e = __builtin_amdgcn_exp2f(lr * m), f = __builtin_amdgcn_fractf(rv * m);
  return cf{opaque(e * __builtin_amdgcn_cosf(f)), opaque(e * __builtin_amdgcn_sinf(f))};
}

constexpr u32 TROW = 544, TCH = 63 * TROW, XT = 69632;
constexpr u32 KCH = 9472, KOO = 4656, UA0 = 18944, UCH = 8768, UBO = 4224, EX = 40960;

DI void conv_role(int ci, LAS char* sm, const u16* x, const u16* are, const u16* aim, const u16* bre, const u16* bim,
                  const u16* cre, const u16* cim, const u16* logdt, const u16* dskip, char* ws) {
  const int tid = threadIdx.x, lane = tid & 63, wave = __builtin_amdgcn_readfirstlane(tid >> 6);
  STAMP_DECL;
  STAMP(0);
  const rsrc_t rws = mkrsrc(ws, WS_END), rx = mkrsrc(x, 2048 * 512 * 2);
  v2u ep = ld64_sc1(0, rws);
  __builtin_amdgcn_s_setprio(PRC0);
  const u32 smb = (u32)(uintptr_t)sm;
  const int pr = (ci & 7) * 32 + (ci >> 3), cg = (ci >> 3) & 7, tt = 4 * (ci & 7) + (ci >> 6);
  const int g = lane >> 4, q = (lane >> 2) & 3, p = lane & 3, n = lane & 15;
  const int ch = wave >> 1, half = wave & 1;

  {
    const u32 v0 = (u32)(64 * tt + 16 * wave + (lane >> 3)) * 1024 + cg * 128 + (((lane & 7) ^ ((g & 1) << 1)) << 4);
    const u32 v1 = (v0 ^ 64) + 7168;
    dma2(smb + XT + wave * 2048, v0, v1, rx, 0);
  }
  const int pi = pr * 256 + tid;
  u32 r_are = are[pi], r_aim = aim[pi], r_bre = bre[pi], r_bim = bim[pi], r_cre = cre[pi], r_cim = cim[pi];
  u32 r_ldt = logdt[2 * pr + ch];
  u32 r_dsk = *(const u32*)(dskip + 2 * pr);
  fence_sched();
  asm volatile("s_waitcnt vmcnt(0)" : "+v"(r_are), "+v"(r_aim), "+v"(r_bre), "+v"(r_bim), "+v"(r_cre), "+v"(r_cim), "+v"(r_ldt), "+v"(r_dsk), "+v"(ep) :: "memory");
  const u64 tag = epoch_tag(ep);
  const u32 tg32 = (u32)(tag >> 32);
  fence_sched();
  STAMP(1);
  {
    LAS char* tb = sm + XT + wave * 2048 + (8 * (p & 1) + q) * 128 + (p >> 1) * 8;
    const int sw = (q & 2) | ((p & 1) << 2);
    const u32 vo = WS_UT + (u32)((16 * cg + 2 * g + (q >> 1)) * 256 + 8 * tt + 2 * wave + (q & 1)) * 64 + p * 16;
    const bf8 d0 = lds_tr2(tb + ((g ^ sw) << 4), tb + ((g ^ sw) << 4) + 512);
    const bf8 d1 = lds_tr2(tb + (((4 + g) ^ sw) << 4), tb + (((4 + g) ^ sw) << 4) + 512);
    st128_sc1<0>(__builtin_bit_cast(v4u, d0), vo, rws, 0);
    st128_sc1<0>(__builtin_bit_cast(v4u, d1), vo, rws, 8 * 256 * 64);
  }
  fence_sched();

  __builtin_amdgcn_s_setprio(PRC1);
  const u32 uf = WS_UFLAG + (u32)(cg * 128 + tt * 4 + wave) * FSP;
  {
    const float dt = bflo(pk2(expf(bflo(r_ldt)), 0.f));
    const float lr = dt * bflo(r_are) * 1.44269504088896341f, rv = dt * bflo(r_aim) * 0.15915494309189535f;
    const cf db = {opaque(dt * bflo(r_bre)), opaque(-dt * bflo(r_bim))};
    const cf cb = cmul(cf{bflo(r_cre), -bflo(r_cim)}, db);
    const cf e16 = cexp_s(lr, rv, 16.f), e1 = cexp_s(lr, -rv, 1.f), e256 = cexp_s(lr, -rv, 256.f);
    const cf e2 = cexp_s(lr, -rv, 2.f), e4 = cexp_s(lr, -rv, 4.f), e8 = cexp_s(lr, -rv, 8.f);
    LAS char* tw = sm + ch * TCH + (tid & 127) * 4;
    auto put = [&](int row, cf v) { *(volatile LAS u32*)(tw + row * TROW) = pk2(v.x, v.y); };
    cf a = e16, t0 = cb, tb[8], v[8];
    put(0, cf{1.f, 0.f}); put(16, t0);
    tb[0] = cb;
#pragma unroll
    for (int i = 1; i < 16; ++i) {
      if (i == UFP) { wait_vm<0>(); flag_store(uf, rws, tag); }
      put(i, a); if (i < 15) a = cmul(a, e16);
      t0 = cmul(t0, e1); put(16 + i, t0);
      if (i < 8) { tb[i] = cmul(tb[i - 1], e256); v[i] = tb[i]; }
#pragma unroll
      for (int tl = 1; tl < 8; ++tl) {
        const int base = tl == 1 ? 32 : (tl < 4 ? 41 + 5 * (tl - 2) : 51 + 3 * (tl - 4));
        const int cnt = tl == 1 ? 9 : (tl < 4 ? 5 : 3), k = i - tl;
        if (k >= 0 && k < cnt) {
          if (k > 0) v[tl] = cmul(v[tl], tl == 1 ? e2 : (tl < 4 ? e4 : e8));
          put(base + k, v[tl]);
        }
      }
    }
  }
  fence_sched();
  if (UFP >= 16) { wait_vm<0>(); flag_store(uf, rws, tag); }
  const u32 vf = WS_UFLAG + (u32)((ci & 7) * 128 + wave * 32 + (lane & 31)) * FSP;
  v2u pf = ld64_sc1(vf, rws);
  STAMP(2);
  lds_bar();

  f4 kk[4];
  v4u uv[2];
  {
    LAS char* tb = sm + ch * TCH + g * 16;
    const int m2 = (n + ((n >> 1) & 1)) >> 1, m4 = (n + 1 + ((n >> 2) & 1)) >> 2, m8 = (n + 3 + ((n >> 3) & 1)) >> 3;
    LAS char* pr_[4] = {tb + (half ? 51 + m8 : 16 + n) * TROW, tb + (half ? 54 + m8 : 32 + m2) * TROW,
                        tb + (half ? 57 + m8 : 41 + m4) * TROW, tb + (half ? 60 + m8 : 46 + m4) * TROW};
    LAS char* pe = tb + n * TROW;
#pragma unroll
    for (int i = 0; i < 4; ++i) kk[i] = (f4)(0.f);
#pragma unroll
    for (int ks = 0; ks < 8; ++ks) {
      if (ks == 4) {
        fence_sched();
        wait_pin<0>(pf);
        if (__builtin_expect(flag_bad(pf, tag), 0)) flag_wait(vf, rws, tag);
        STAMP(3);
        const u32 vo = WS_UT + (u32)((pr >> 1) * 256 + 64 * wave + (lane >> 1)) * 64 + ((pr & 1) * 2 + (lane & 1)) * 16;
        uv[0] = ld128_sc1<0>(vo, rws, 0); uv[1] = ld128_sc1<2048>(vo, rws, 0);
        fence_sched();
      }
      const bf8 e = lds_rd128(pe + ks * 64);
#pragma unroll
      for (int i = 0; i < 4; ++i) kk[i] = mfma16(lds_rd128(pr_[i] + ks * 64), e, kk[i]);
    }
  }
  fence_sched();
  STAMP(4);
  lds_bar();
  {
    LAS char* kb = sm + ch * KCH;
#pragma unroll
    for (int i = 0; i < 4; ++i) {
      const int l0 = 256 * (4 * half + i) + 16 * n + 4 * g;
      const float k0 = opaque(kk[i][0]), k1 = opaque(kk[i][1]), k2 = opaque(kk[i][2]), k3 = opaque(kk[i][3]);
      const u32 a = pk2(k3, k2), bq = pk2(k1, k0), c = pk2(k2, k1);
      *(LAS v2u*)(kb + KOO + 2 * (2044 - l0)) = (v2u){a, bq};
      *(LAS u16*)(kb + 2 * (2051 - l0)) = (u16)a;
      *(LAS u32*)(kb + 2 * (2052 - l0)) = c;
      *(LAS u16*)(kb + 2 * (2054 - l0)) = (u16)(bq >> 16);
    }
    const v4u z = {0, 0, 0, 0};
    if (lane < 29) *(LAS v4u*)(kb + (half ? KOO + 4096 : 4112) + lane * 16) = z;
    else if (lane < 33) *(LAS v4u*)(sm + UA0 + ch * UCH + (half ? UBO : 0) + (lane - 29) * 16 - (half ? 0 : 16)) = z;
    else if (lane == 33 && !half) *(LAS u16*)(kb + 2 * 2055) = 0;
    else if (lane == 34 && !half) *(LAS v3u*)(sm + UA0 + ch * UCH + 48) = (v3u){0, 0, 0};
  }
  asm volatile("s_waitcnt vmcnt(0)" : "+v"(uv[0]), "+v"(uv[1]) :: "memory");
#pragma unroll
  for (int k = 0; k < 2; ++k) {
    LAS char* ub = sm + UA0 + (lane & 1) * UCH + 64 + (64 * wave + 32 * k + (lane >> 1)) * 16;
    *(LAS v4u*)(ub + UBO) = uv[k];
    *(LAS u32*)(ub - 4) = uv[k].x;
    *(LAS v3u*)(ub) = (v3u){uv[k].y, uv[k].z, uv[k].w};
  }
  lds_bar();
  STAMP(5);
  __builtin_amdgcn_s_setprio(PRCV);

  f4 y[8];
  {
    LAS char* pk = sm + ch * KCH + ((n & 1) ? KOO + 4032 : 4048) - 64 * (n >> 1) + 16 * g - 3584 + 256 * half;
    LAS char* pu = sm + UA0 + ch * UCH + (n < 8 ? 8 * n : UBO + 8 * (n - 8) + 8) + 16 * g + 256 * half;
    bf8 kf[4][8], uf[4][8];
    auto rd = [&](int rr, int i) {
      kf[rr][i] = lds_rd128(pk + 512 * (7 - i) + 64 * rr);
      const v2u lo = *(volatile LAS v2u*)(pu + 512 * i + 64 * rr), hi = *(volatile LAS v2u*)(pu + 512 * i + 64 * rr + 8);
      uf[rr][i] = __builtin_bit_cast(bf8, (v4u){lo.x, lo.y, hi.x, hi.y});
    };
#pragma unroll
    for (int i = 0; i < 8; ++i) { rd(0, i); fence_sched(); }
    static_for<0, 144>([&](auto N) {
      constexpr int nn = decltype(N)::value, rr = nn / 36, v = nn % 36;
      constexpr int s = v < 1 ? 0 : v < 3 ? 1 : v < 6 ? 2 : v < 10 ? 3 : v < 15 ? 4 : v < 21 ? 5 : v < 28 ? 6 : 7, m = v - s * (s + 1) / 2;
      if constexpr (rr == 0 && m == 0) y[s] = mfma16(kf[rr][s - m], uf[rr][m], (f4)(0.f));
      else y[s] = mfma16(kf[rr][s - m], uf[rr][m], y[s]);
      fence_sched();
      if constexpr (rr < 3 && v % 4 == 2 && v / 4 < 8) rd(rr + 1, v / 4);
      fence_sched();
    });
  }
  STAMP(6);
#pragma unroll
  for (int a = 0; a < 8; ++a) *(LAS f4*)(sm + EX + (wave * 8 + a) * 1024 + lane * 16) = y[a];
  lds_bar();
  {
    const float d0 = bflo(r_dsk), d1 = bfhi(r_dsk);
    const int rn = n < 8 ? 2 * n : 2 * (n - 8) + 1;
#pragma unroll
    for (int i = 0; i < 2; ++i) {
      const int al = 2 * wave + i;
      LAS char* ex = sm + EX + al * 1024 + lane * 16;
      const f4 y0 = *(LAS f4*)(ex) + *(LAS f4*)(ex + 8192), y1 = *(LAS f4*)(ex + 16384) + *(LAS f4*)(ex + 24576);
      LAS char* up = sm + UA0 + UBO + 64 + (256 * al + 2 * rn + 64 * g) * 2;
      const u32 u00 = *(LAS u32*)(up), u01 = *(LAS u32*)(up + 64), u10 = *(LAS u32*)(up + UCH), u11 = *(LAS u32*)(up + UCH + 64);
      auto mix = [&](float y, float u, float d) { return opaque(__builtin_fmaf(u, d, opaque(y))); };
      const u32 x0 = pk2(mix(y0[0], bflo(u00), d0), mix(y1[0], bflo(u10), d1)), x1 = pk2(mix(y0[1], bfhi(u00), d0), mix(y1[1], bfhi(u10), d1));
      const u32 z0 = pk2(mix(y0[2], bflo(u01), d0), mix(y1[2], bflo(u11), d1)), z1 = pk2(mix(y0[3], bfhi(u01), d0), mix(y1[3], bfhi(u11), d1));
      const u32 vo = WS_A + (u32)(4 * al + g) * 131072 + pr * 512 + rn * 16;
      st128_sc1<0>((v4u){x0, tg32, x1, tg32}, vo, rws, 0);
      st128_sc1<256>((v4u){z0, tg32, z1, tg32}, vo, rws, 0);
    }
  }
  STAMP(7);
  if (ci == 0) {
    granule_wait(WS_A + (u32)tid * 512, rws, tg32);
    if (tid < 224) flag_wait(WS_EFLAG + (u32)tid * 4 * FSP, rws, tag);
    else granule_wait(WS_G + (u32)(tid - 224) * 512, rws, tg32);
    lds_bar();
    if (wave == 0) flag_store(0, rws, tag);
  }
  STAMP_OUT;
}

__global__ __launch_bounds__(256, 2) void fused_kernel(const u16* __restrict__ x, const u16* __restrict__ are, const u16* __restrict__ aim,
                                                       const u16* __restrict__ bre, const u16* __restrict__ bim, const u16* __restrict__ cre,
                                                       const u16* __restrict__ cim, const u16* __restrict__ logdt, const u16* __restrict__ dskip,
                                                       const u16* __restrict__ w1, const u16* __restrict__ b1, const u16* __restrict__ w2,
                                                       const u16* __restrict__ b2, const u16* __restrict__ gw, u16* __restrict__ out, char* ws) {
  __shared__ __attribute__((aligned(256))) char smem[81920];
  LAS char* sm = (LAS char*)smem;
  asm volatile("" :: "s"(x), "s"(are), "s"(aim), "s"(bre), "s"(bim), "s"(cre), "s"(cim), "s"(logdt), "s"(dskip), "s"(w1), "s"(b1),
               "s"(w2), "s"(b2), "s"(gw), "s"(out), "s"(ws));
  const int b = blockIdx.x;
  const bool mlp = (b < 256) == (GFIRST != 0);
  if (mlp && (b & 255) < 224) gemm_role<false>(b & 255, sm, x, w1, b1, w2, b2, gw, out, ws);
  else if (mlp) gemm_role<true>(b & 255, sm, x, w1, b1, w2, b2, gw, out, ws);
  else conv_role(b & 255, sm, x, are, aim, bre, bim, cre, cim, logdt, dskip, ws);
}

extern "C" void kernel_launch(void* const* d_in, const int* in_sizes, int n_in, void* d_out, int out_size, void* d_ws, size_t ws_size,
                              hipStream_t stream) {
  const u16* const* in = (const u16* const*)d_in;
  fused_kernel<<<512, 256, 0, stream>>>(in[0], in[1], in[2], in[3], in[4], in[5], in[6], in[7], in[8], in[10], in[11], in[12], in[13],
                                        in[14], (u16*)d_out, (char*)d_ws);
}
```

```cpp
#include <hip/hip_runtime.h>
#include <stdint.h>
#include <type_traits>
#pragma clang diagnostic ignored "-Winline-asm"

typedef unsigned short u16;
typedef unsigned int u32;
typedef unsigned long long u64;
typedef __attribute__((ext_vector_type(2))) u32 v2u;
typedef __attribute__((ext_vector_type(3))) u32 v3u;
typedef __attribute__((ext_vector_type(4))) u32 v4u;
typedef __attribute__((ext_vector_type(2))) float f2;
typedef __attribute__((ext_vector_type(4))) float f4;
typedef __attribute__((ext_vector_type(16))) float f16v;
typedef __attribute__((ext_vector_type(2))) __bf16 bf2;
typedef __attribute__((ext_vector_type(8))) __bf16 bf8;
typedef __attribute__((ext_vector_type(4))) short s4;
typedef __amdgpu_buffer_rsrc_t rsrc_t;
#define DI __device__ __forceinline__
#define LAS __attribute__((address_space(3)))
#define CAS __attribute__((address_space(4)))

#define GFIRST 0
#define LAM 4
#define UPP 2
#define PA 110
#define PC 127
#define PD 8
#define KP 6
#define KC 9
#define KA 19
#define KB 25
#define NT 2
#define W2PF 0
#define PRG1 2
#define PRG2 1
#define PRC0 3
#define PRC1 1
#define PRCV 2
#define UFP 8
#define PF 3
#define WSPOS 14
#define PFQ "sc1"

constexpr u32 FSP = 256;
constexpr u32 WS_G = 4096, WS_EFLAG = 65536, WS_UFLAG = WS_EFLAG + 1024 * FSP;
constexpr u32 WS_UT = 1u << 20, WS_A = 3u << 20, WS_H = 7u << 20, WS_END = 15u << 20;
constexpr int SPIN_MAX = 1 << 16;
constexpr int A0 = 0;

#define STAMP(i)
#define STAMPQ(i)
#define STAMPC(i)
#define STAMP_DECL
#define STAMP_OUT

DI rsrc_t mkrsrc(const void* p, u32 bytes) { return __builtin_amdgcn_make_buffer_rsrc((void*)p, (short)0, (int)bytes, 0x00020000); }
DI float bflo(u32 v) { return __uint_as_float(v << 16); }
DI float bfhi(u32 v) { return __uint_as_float(v & 0xffff0000u); }
DI u32 pk2(float a, float b) { bf2 r = __builtin_convertvector((f2){a, b}, bf2); return __builtin_bit_cast(u32, r); }
DI float dot2(u32 a, u32 b, float c) { return __builtin_amdgcn_fdot2_f32_bf16(__builtin_bit_cast(bf2, a), __builtin_bit_cast(bf2, b), c, false); }
DI float relu(float v) { return __int_as_float(max(__float_as_int(v), 0)); }
DI void fence_sched() { __builtin_amdgcn_sched_barrier(0); }

template <int N> DI void wait_vm() { asm volatile("s_waitcnt vmcnt(%0)" :: "n"(N) : "memory"); }
template <int N> DI void wait_vm_bar() { asm volatile("s_waitcnt vmcnt(%0) lgkmcnt(0)\n\ts_barrier" :: "n"(N) : "memory"); }
DI void lds_bar() { asm volatile("s_waitcnt lgkmcnt(0)\n\ts_barrier" ::: "memory"); }
DI void wait_lgkm() { asm volatile("s_waitcnt lgkmcnt(0)" ::: "memory"); }

DI void dma2(u32 m0v, u32 v0, u32 v1, rsrc_t rs, u32 so) {
  asm volatile("s_mov_b32 m0, %0\n\ts_nop 0\n\t"
               "buffer_load_dwordx4 %1, %3, %4 offen lds\n\t"
               "buffer_load_dwordx4 %2, %3, %4 offen offset:1024 lds"
               :: "s"(m0v), "v"(v0), "v"(v1), "s"(rs), "s"(so) : "m0", "memory");
}
template <u32 off> DI void set_m0(u32 base) { asm volatile("s_add_u32 m0, %0, %1" :: "s"(base), "n"(off) : "m0", "scc"); }
template <int OFF> DI void dma1(u32 v, rsrc_t rs, u32 so) {
  asm volatile("buffer_load_dwordx4 %0, %1, %2 offen offset:%3 lds" :: "v"(v), "s"(rs), "s"(so), "n"(OFF) : "memory");
}
template <int OFF> DI void reld128_sc1(v4u& r, u32 vo, rsrc_t rs, u32 so) {
  asm volatile("buffer_load_dwordx4 %0, %1, %2, %3 offen offset:%4 sc1" : "+v"(r) : "v"(vo), "s"(rs), "s"(so), "n"(OFF) : "memory");
}
template <int OFF> DI u32 ld32_sc1(u32 vo, rsrc_t rs, u32 so) {
  u32 r; asm volatile("buffer_load_dword %0, %1, %2, %3 offen offset:%4 sc1" : "=&v"(r) : "v"(vo), "s"(rs), "s"(so), "n"(OFF) : "memory"); return r;
}
template <int OFF> DI v4u ld128_sc1(u32 vo, rsrc_t rs, u32 so) {
  v4u r; asm volatile("buffer_load_dwordx4 %0, %1, %2, %3 offen offset:%4 sc1" : "=&v"(r) : "v"(vo), "s"(rs), "s"(so), "n"(OFF) : "memory"); return r;
}
DI v4u ld128(u32 vo, rsrc_t rs) {
  v4u r; asm volatile("buffer_load_dwordx4 %0, %1, %2, 0 offen" : "=&v"(r) : "v"(vo), "s"(rs) : "memory"); return r;
}
DI v2u ld64_sc1(u32 vo, rsrc_t rs) {
  v2u r; asm volatile("buffer_load_dwordx2 %0, %1, %2, 0 offen sc1" : "=&v"(r) : "v"(vo), "s"(rs) : "memory"); return r;
}
template <int OFF> DI u32 ld16(u32 vo, rsrc_t rs) {
  u32 r; asm volatile("buffer_load_ushort %0, %1, %2, 0 offen offset:%3" : "=&v"(r) : "v"(vo), "s"(rs), "n"(OFF) : "memory"); return r;
}
template <int OFF> DI void st128_sc1(v4u d, u32 vo, rsrc_t rs, u32 so) {
  asm volatile("buffer_store_dwordx4 %0, %1, %2, %3 offen offset:%4 sc1\n\ts_nop 1" :: "v"(d), "v"(vo), "s"(rs), "s"(so), "n"(OFF) : "memory");
}
DI void st64_sc1(v2u d, u32 vo, rsrc_t rs) {
  asm volatile("buffer_store_dwordx2 %0, %1, %2, 0 offen sc1" :: "v"(d), "v"(vo), "s"(rs) : "memory");
}
DI void flag_store(u32 off, rsrc_t rs, u64 tag) {
  const v2u d = {(u32)tag, (u32)(tag >> 32)};
  asm volatile("s_mov_b64 exec, 1\n\tbuffer_store_dwordx2 %0, off, %1, %2 sc1\n\ts_mov_b64 exec, -1" :: "v"(d), "s"(rs), "s"(off) : "memory");
}
DI u64 epoch_tag(v2u e) {
  const u64 v = (u64)(u32)__builtin_amdgcn_readfirstlane(e.x) | ((u64)(u32)__builtin_amdgcn_readfirstlane(e.y) << 32);
  return ((v * 0x9E3779B97F4A7C15ull + 0x632BE59BD9B4E019ull) & ~0xffffull) | (~v & 0xffffull);
}
constexpr u32 SGN = 0x80008000u;
DI u32 sign_of(u64 tag, int t) { return ((((u32)tag >> (t >> 1)) ^ t) & 1) ? SGN : 0u; }
DI u32 strip(v4u& d, u32 s) { d ^= s; return (d.x | d.y) | (d.z | d.w); }
DI bool any(u32 m) { return __builtin_amdgcn_ballot_w64(m != 0) != 0; }
DI bool flag_bad(v2u v, u64 tag) { return __builtin_amdgcn_ballot_w64((v.x != (u32)tag) | (v.y != (u32)(tag >> 32))) != 0; }
DI void flag_wait(u32 vo, rsrc_t rs, u64 tag) {
#pragma unroll 1
  for (int it = 0; it < SPIN_MAX; ++it) {
    v2u v; asm volatile("buffer_load_dwordx2 %0, %1, %2, 0 offen sc1\n\ts_waitcnt vmcnt(0)" : "=&v"(v) : "v"(vo), "s"(rs) : "memory");
    if (!flag_bad(v, tag)) break;
    __builtin_amdgcn_s_sleep(1);
  }
}

DI void granule_wait(u32 vo, rsrc_t rs, u32 tg) {
#pragma unroll 1
  for (int it = 0; it < SPIN_MAX; ++it) {
    v2u v; asm volatile("buffer_load_dwordx2 %0, %1, %2, 0 offen sc1\n\ts_waitcnt vmcnt(0)" : "=&v"(v) : "v"(vo), "s"(rs) : "memory");
    if (__builtin_amdgcn_ballot_w64(v.y != tg) == 0) break;
    __builtin_amdgcn_s_sleep(1);
  }
}
DI float opaque(float v) { asm("" : "+v"(v)); return v; }

DI bf8 lds_rd128(LAS char* p) { return *(LAS bf8*)p; }
DI bf8 lds_tr2(LAS char* p0, LAS char* p1) {
  s4 a = __builtin_amdgcn_ds_read_tr16_b64_v4i16((LAS s4*)p0);
  s4 b = __builtin_amdgcn_ds_read_tr16_b64_v4i16((LAS s4*)p1);
  v2u x = __builtin_bit_cast(v2u, a), y = __builtin_bit_cast(v2u, b);
  return __builtin_bit_cast(bf8, (v4u){x.x, x.y, y.x, y.y});
}
DI f16v mfma32(bf8 a, bf8 b, f16v c) { return __builtin_amdgcn_mfma_f32_32x32x16_bf16(a, b, c, 0, 0, 0); }
DI f4 mfma16(bf8 a, bf8 b, f4 c) { return __builtin_amdgcn_mfma_f32_16x16x32_bf16(a, b, c, 0, 0, 0); }

template <int I, int N, class F> DI void static_for(F&& f) {
  if constexpr (I < N) { f(std::integral_constant<int, I>{}); static_for<I + 1, N>(f); }
}

constexpr u32 REG = 20480;
constexpr int wblk(int t) { constexpr int b[16] = {6, 7, 8, 9, 6, 7, 8, 9, 6, 7, 4, 5, 8, 0, 1, 9}; return b[t]; }
constexpr int rblk(int k) { constexpr int b[10] = {2, 3, 6, 7, 4, 5, 8, 0, 1, 9}; return b[k % 10]; }
constexpr u32 w2row(int k) { return 128 * ((k % 14) >> 1) + 64 * (k / 14) + 8 * (k & 1); }
constexpr int hstep(int k) { return 8 * ((k % 14) >> 1) + 4 * (k / 14) + (k & 1); }

enum { OX, OW, OR, OS, OE, OH, OP, OA };
struct Op { int pos, kind, idx, sub; };
struct OpTab {
  Op op[256]; int n;
  constexpr void add(int pos, int kind, int idx, int sub) { op[n] = Op{pos, kind, idx, sub}; ++n; }
  constexpr int at(int kind, int idx, int sub) const {
    for (int i = 0; i < n; ++i) if (op[i].kind == kind && op[i].idx == idx && op[i].sub == sub) return i;
    return -1;
  }
  constexpr int from(int pos) const { int i = 0; while (i < n && op[i].pos < pos) ++i; return i; }
  constexpr int younger(int kind, int idx, int sub, int pos) const {
    int c = 0, a = at(kind, idx, sub);
    if (a < 0 || op[a].pos >= pos) return -1000;
    for (int i = a + 1; i < n; ++i) if (op[i].pos < pos) ++c;
    return c > 63 ? 63 : c;
  }
  constexpr int lds(int i) const {
    const Op o = op[i];
    return o.kind == OX ? (o.idx % 3) * 4096 + (o.sub >> 1) * 2048 : o.kind == OW ? wblk(o.idx) * 2048 : o.kind == OR ? rblk(o.idx) * 2048 : -1;
  }
  constexpr int m0_at(int pos) const {
    int cur = -1;
    for (int i = 0; i < n; ++i) if (lds(i) >= 0) { if (op[i].pos == pos) return lds(i) != cur ? lds(i) : -1; cur = lds(i); }
    return -1;
  }
  constexpr bool m0_ok() const {
    for (int i = 0; i < n; ++i) for (int j = i + 1; j < n; ++j)
      if (op[i].pos == op[j].pos && op[i].pos >= 0 && lds(i) >= 0 && lds(j) >= 0 && lds(i) != lds(j)) return false;
    return true;
  }
};
constexpr int EP0 = 61, G2P = 200;
constexpr int mn(int a, int b) { return a < b ? a : b; }
template <bool AUX> constexpr OpTab optab() {
  OpTab t{};
  int nh = 0;
  if (!AUX) {
    for (int c = 0; c < 3; ++c) { for (int s = 0; s < 4; ++s) t.add(-1, OX, c, s); t.add(-1, OW, c, 0); t.add(-1, OW, c, 1); }
    t.add(-1, OW, 3, 0); t.add(-1, OW, 3, 1);
    for (int n = 0; n < 128; ++n) {
      const int T = (n + LAM) / 8, i = (n + LAM) % 8;
      if (T >= 1 && T <= 5) { if (i < 4) t.add(n, OX, T + 2, i); else if (i < 6) t.add(n, OW, T + 3, i - 4); }
      if (T >= 6 && T <= 7 && i < 6) t.add(n, OW, 9 + 3 * (T - 6) + i / 2, i & 1);
      if (T == 8 && i < 6) { if (i < 2) t.add(n, OW, 15, i); else t.add(n, OR, i / 2 - 1, i & 1); }
      if (T >= 9 && i < 2) t.add(n, OR, T - 7, i);
      for (int s = 0; s < 4; ++s) if (EP0 + (4 * s + 3) / UPP == n) t.add(n, OS, s, 0);
      if (n == EP0 + 15 / UPP + 1) t.add(n, OE, 0, 0);
      if (n == PA) t.add(n, OP, 0, 0);
      if (n >= PC && nh < 2 * PD) { t.add(n, OH, nh >> 1, nh & 1); ++nh; }
    }
    for (; nh < 2 * PD; ++nh) t.add(128, OH, nh >> 1, nh & 1);
    for (int s = 4; s < 8; ++s) t.add(129 + s, OS, s, 0);
  } else {
    for (; nh < 2 * PD; ++nh) t.add(-1, OH, nh >> 1, nh & 1);
  }
  int na = 0;
  for (int k = 0; k < 28 - NT; ++k)
    for (int g = 0; g < 4; ++g) {
      const int pos = G2P + 4 * k + g;
      const bool ring = (g == 1 || g == 2) && k + 10 < 28;
      if (ring) t.add(pos, OR, k + 10, g - 1);
      if (k == KP && g == 0) t.add(pos, OP, 1, 0);
      const int lim = mn(k < KC ? 28 : 56, 2 * (k + PD) + (g == 3 ? 2 : 1));
      int room = ring ? 0 : 1;
      if (k >= KC && nh + 2 < lim) ++room;
      for (; room > 0 && nh < lim; --room, ++nh) t.add(pos, OH, nh >> 1, nh & 1);
      if (k >= KA && na < 6 && room > 0) { t.add(pos, OA, 0, na); ++na; }
    }
  return t;
}
constexpr OpTab GT[2] = {optab<false>(), optab<true>()};
constexpr int g1wait(int T) {
  const int pos = T ? 8 * T - LAM : 0;
  return mn(GT[0].younger(OW, T, 1, pos), T < 8 ? GT[0].younger(OX, T, 3, pos) : 63);
}
template <bool AUX> constexpr int g2wait(int k) {
  const int pos = G2P + 4 * k, kl = k == 27 - NT ? 27 : k + 1;
  return mn(GT[AUX].younger(OH, kl, 1, pos), (AUX && kl < 10) ? 63 : GT[AUX].younger(OR, kl, 1, pos));
}
template <bool AUX> constexpr bool sched_ok() {
  for (int k = 0; k < 28 - NT; ++k) if (g2wait<AUX>(k) < 0) return false;
  for (int T = 0; T < 16; ++T) if (g1wait(T) < 0) return false;
  return GT[AUX].m0_ok() && GT[AUX].younger(OA, 0, 5, G2P + 4 * KB) >= 0 && GT[AUX].younger(OP, 1, 0, G2P + 4 * KC) >= 0;
}
static_assert(sched_ok<false>() && sched_ok<true>() && GT[0].at(OW, 15, 1) < GT[0].at(OS, 0, 0), "schedule");
static_assert(KP < KC && KC + 1 < 14 && KB < 28 - NT && KB <= 25 && KB >= 19 && NT >= 2 && NT <= 4, "schedule");

template <int N> DI void wait_pin(v4u& a, v4u& b) { asm volatile("s_waitcnt vmcnt(%2)" : "+v"(a), "+v"(b) : "n"(N) : "memory"); }
template <int N> DI void wait_pin(v2u& a) { asm volatile("s_waitcnt vmcnt(%1)" : "+v"(a) : "n"(N) : "memory"); }
template <int N> DI void wait_pin(u32& a) { asm volatile("s_waitcnt vmcnt(%1)" : "+v"(a) : "n"(N) : "memory"); }
template <int N> DI void wait_pin(v4u (&a)[6]) {
  asm volatile("s_waitcnt vmcnt(%6)" : "+v"(a[0]), "+v"(a[1]), "+v"(a[2]), "+v"(a[3]), "+v"(a[4]), "+v"(a[5]) : "n"(N) : "memory");
}
DI void pin(v4u& a, v4u& b) { asm volatile("" : "+v"(a), "+v"(b)); }
template <int I> using IC = std::integral_constant<int, I>;

template <bool AUX>
DI void gemm_role(int b, LAS char* sm, const u16* x, const u16* w1, const u16* b1, const u16* w2, const u16* b2, const u16* gw,
                  u16* out, char* ws) {
  constexpr const OpTab& TB = GT[AUX];
  const int lane = threadIdx.x & 63, wave = __builtin_amdgcn_readfirstlane(threadIdx.x >> 6);
  STAMP_DECL;
  STAMP(0);
  const int xcd = b & 7, r = b >> 3;
  const rsrc_t rws = mkrsrc(ws, WS_END), rx = mkrsrc(x, 2048 * 512 * 2), rw2 = mkrsrc(w2, 1792 * 512 * 2), rb2 = mkrsrc(b2, 512 * 2);
  const rsrc_t rw1 = mkrsrc(w1, 512 * 1792 * 2);
  v2u ep = ld64_sc1(0, rws);
  const int g = lane >> 4, q = (lane >> 2) & 3, p = lane & 3, l31 = lane & 31, hh = lane >> 5, l3 = lane >> 3, l7 = lane & 7;
  const u32 mreg = (u32)(uintptr_t)sm + wave * REG;
  const int t2 = 4 * xcd + (r >> 3), c2 = r & 7;
  const int tau = 4 * (xcd >> 1) + r / 7, j = 7 * (xcd & 1) + r % 7, rot = (r % 7) + 2 * (r / 7);

  const u32 wch = ((l7 ^ ((g & 1) << 2)) << 4);
  const u32 vx0 = (u32)(128 * tau + 32 * wave + l3) * 1024 + ((l7 ^ g) << 4), vx1 = (vx0 ^ 64) + 7168;
  const u32 vw0 = (u32)(16 * wave + l3) * 3584 + j * 256 + wch, vw1 = vw0 + 8 * 3584 - 1024;
  const u32 vv0 = (u32)(896 * (wave >> 1) + 32 * (wave & 1) + l3) * 1024 + c2 * 128 + wch, vv1 = vv0 + 15360;
  const u32 vh = WS_H + (u32)((4 * tau + wave) * 112 + 8 * j) * 1024 + lane * 16;
  const u32 vhh = WS_H + (u32)(2 * t2 * 112 + 56 * (wave >> 1) + 2 * (wave & 1)) * 1024 + lane * 16;
  const u32 vp = vhh + (u32)((l3 & 1) * 112 + 8 * min(l7, 6) + 1) * 1024;
  const u32 va = WS_A + (u32)t2 * 131072 + (u32)(32 * c2 + 4 * l3) * 512 + (u32)(16 * wave + 2 * l7) * 8;
  const u32 vg = WS_G + (u32)(64 * t2 + 16 * wave + 2 * l3) * 8;
  u32 aoff[2];
#pragma unroll
  for (int mi = 0; mi < 2; ++mi) aoff[mi] = (8 * (g >> 1) + q) * 128 + (((4 * mi + 2 * (p & 1) + (g & 1)) ^ ((q >> 1) << 2)) << 4) + (p >> 1) * 8;

  u64 tag;
  u32 sg, pf;
  v4u hd[8], hb[28][2], av[6];
  auto hload = [&](auto K, auto S, auto AGAIN) {
    constexpr int k = decltype(K)::value, s = decltype(S)::value, ks = hstep(k);
    if constexpr (decltype(AGAIN)::value) reld128_sc1<(ks & 3) * 1024>(hb[k][s], vhh, rws, (ks >> 2) * 4096 + s * 114688);
    else hb[k][s] = ld128_sc1<(ks & 3) * 1024>(vhh, rws, (ks >> 2) * 4096 + s * 114688);
  };
  auto aload = [&](auto S) {
    constexpr int s = decltype(S)::value;
    if constexpr (s < 4) av[s] = ld128_sc1<s * 512>(va, rws, 0);
    else if constexpr (s == 4) av[4] = ld128_sc1<0>(vg, rws, 0);
    else av[5] = ld128((u32)(64 * c2 + 8 * l7) * 2, rb2);
  };
  auto issue = [&](auto I) {
    constexpr Op o = TB.op[decltype(I)::value];
    constexpr int lo = (o.sub & 1) * 1024;
    if constexpr (o.pos < 0 && TB.lds(decltype(I)::value) >= 0 && lo == 0) { set_m0<TB.lds(decltype(I)::value)>(mreg); asm volatile("s_nop 0"); }
    if constexpr (o.kind == OX) dma1<lo>(lo ? vx1 : vx0, rx, (u32)((o.idx + rot) & 7) * 128 + (o.sub >> 1) * 16384);
    if constexpr (o.kind == OW) dma1<lo>(lo ? vw1 : vw0, rw1, (u32)((o.idx + rot) & 7) * (64 * 3584) + (o.idx >> 3) * 128);
    if constexpr (o.kind == OR) dma1<lo>(lo ? vv1 : vv0, rw2, w2row(o.idx) * 1024);
    if constexpr (o.kind == OS) st128_sc1<(o.idx & 3) * 1024>(hd[o.idx], vh, rws, (o.idx >> 2) * 4096);
    if constexpr (o.kind == OE) flag_store(WS_EFLAG + (u32)(4 * b + wave) * FSP, rws, tag);
    if constexpr (o.kind == OP) pf = ld32_sc1<0>(vp, rws, o.idx * 4096);
    if constexpr (o.kind == OH) hload(IC<o.idx>{}, IC<o.sub>{}, IC<0>{});
    if constexpr (o.kind == OA) aload(IC<o.sub>{});
    if constexpr (o.pos < 0 && o.kind == OW && o.sub == 1 && o.idx < 2) asm volatile("s_barrier");
  };
  auto issue_at = [&](auto N) {
    static_for<TB.from(decltype(N)::value), TB.from(decltype(N)::value + 1)>(issue);
  };
  auto m0_for = [&](auto N) { if constexpr (TB.m0_at(decltype(N)::value) >= 0) set_m0<TB.m0_at(decltype(N)::value)>(mreg); };
  auto gate = [&](int ph) {
    if (__builtin_expect(any((pf ^ sg) & SGN), 0)) {
#pragma unroll 1
      for (int it = 0; it < SPIN_MAX; ++it) {
        __builtin_amdgcn_s_sleep(1);
        pf = ld32_sc1<0>(vp, rws, ph * 4096);
        wait_pin<0>(pf);
        if (!any((pf ^ sg) & SGN)) break;
      }
    }
  };

  if constexpr (!AUX) {
    const rsrc_t rb1 = mkrsrc(b1, 1792 * 2);
    const u32 vb = (u32)(128 * j + 32 * hh + 16 * ((l31 >> 2) & 1) + 4 * (l31 >> 3) + p) * 2;
    u32 bq0 = ld16<0>(vb, rb1), bq1 = ld16<128>(vb, rb1);
    f16v acc[4];
    issue_at(IC<-1>{});
    __builtin_amdgcn_s_setprio(PRG1);

    LAS char* pa[2][2]; LAS char* pb[4];
#pragma unroll
    for (int mi = 0; mi < 2; ++mi) { pa[mi][0] = sm + aoff[mi]; pa[mi][1] = sm + aoff[mi] + 2 * REG; }
#pragma unroll
    for (int u = 0; u < 4; ++u) pb[u] = sm + wave * REG + l31 * 128 + (((2 * u + hh) ^ ((l31 >> 1) & 7)) << 4);

    asm volatile("s_waitcnt vmcnt(20)" : "+v"(ep), "+v"(bq0), "+v"(bq1) :: "memory");
    tag = epoch_tag(ep);
    sg = sign_of(tag, l31);
    {
      const bf8 o0 = __builtin_bit_cast(bf8, (v4u){hh ? 0u : 0x3f80u, 0, 0, 0}), o1 = __builtin_bit_cast(bf8, (v4u){hh ? 0x3f80u : 0u, 0, 0, 0});
      const bf8 a0 = __builtin_bit_cast(bf8, (v4u){bq0, 0, 0, 0}), a1 = __builtin_bit_cast(bf8, (v4u){bq1, 0, 0, 0});
      acc[0] = mfma32(a0, o0, (f16v)(0.f)); acc[1] = mfma32(a0, o1, (f16v)(0.f));
      acc[2] = mfma32(a1, o0, (f16v)(0.f)); acc[3] = mfma32(a1, o1, (f16v)(0.f));
    }
    constexpr auto ms = [](int n) constexpr { const int T = n >> 3, v = n & 7; return (T & 7) == 7 ? (v & 3) : (v >> 1); };
    constexpr auto mt = [](int n) constexpr { const int T = n >> 3, v = n & 7; return (T & 7) == 7 ? (v >> 2) : (v & 1); };
    bf8 af[128], xf[32];
    auto rda = [&](auto N) {
      constexpr int n = decltype(N)::value, u = ms(n);
      LAS char* a = pa[mt(n)][u >> 1] + (u & 1) * REG + wblk(n >> 3) * 2048;
      af[n] = lds_tr2(a, a + 512);
    };
    auto rdx = [&](auto S) { constexpr int s = decltype(S)::value; xf[s] = lds_rd128(pb[s & 3] + ((s >> 2) % 3) * 4096); };
    auto unit = [&](auto U) {
      constexpr int un = decltype(U)::value, tl = un >> 3, e = (un & 7) * 2;
      const float v0 = relu(acc[tl][e]), v1 = relu(acc[tl][e + 1]);
      hd[un >> 2][un & 3] = (pk2(opaque(v0 * v0), opaque(v1 * v1)) & ~SGN) | sg;
    };
    STAMP(1);
    fence_sched();
    wait_vm_bar<g1wait(0)>();
    fence_sched();
    STAMP(2); STAMPC(60);
    static_for<0, 4>(rdx);
    static_for<0, LAM>(rda);
    static_for<0, 128>([&](auto N) {
      constexpr int n = decltype(N)::value, T = (n + LAM) / 8, i = (n + LAM) % 8;
      fence_sched();
      if constexpr (i == 0) { if constexpr (T < 16) wait_vm_bar<g1wait(T)>(); else lds_bar(); }
      if constexpr (i == 0) { STAMPQ(15 + T); }
      if constexpr (n == PC) { wait_pin<TB.younger(OP, 0, 0, n)>(pf); gate(0); }
      m0_for(N);
      fence_sched();
      acc[2 * (n >> 6) + mt(n)] = mfma32(af[n], xf[(4 * (n >> 3) + ms(n)) & 31], acc[2 * (n >> 6) + mt(n)]);
      fence_sched();
      if constexpr (n + LAM < 128) rda(IC<n + LAM>{});
      if constexpr (T >= 1 && T < 8 && i < 4) rdx(IC<4 * T + i>{});
      if constexpr (n >= EP0 && n < EP0 + 16 / UPP) static_for<UPP * (n - EP0), UPP * (n - EP0 + 1)>(unit);
      if constexpr (n >= 64 + EP0) static_for<16 + UPP * (n - 64 - EP0), 16 + UPP * (n - 64 - EP0 + 1)>(unit);
      fence_sched();
      issue_at(N);
    });
    fence_sched();
    STAMP(5); STAMPC(61);
    issue_at(IC<128>{});
    static_for<16, 32>([&](auto U) {
      constexpr int un = decltype(U)::value;
      if constexpr (64 + EP0 + (un & 15) / UPP > 127) unit(U);
      if constexpr ((un & 3) == 3) { fence_sched(); issue_at(IC<129 + (un >> 2)>{}); fence_sched(); }
    });
    STAMP(6);
  } else {
    const int tg = xcd * 4 + (r - 28);
    const v4u gv = *(const v4u*)(gw + lane * 8);
    const u16* xr = x + (size_t)(tg * 64 + wave * 16) * 512 + lane * 8;
    float s[16];
#pragma unroll
    for (int i = 0; i < 16; ++i) {
      const v4u xv = *(const v4u*)(xr + i * 512);
      s[i] = 0.f;
#pragma unroll
      for (int k = 0; k < 4; ++k) s[i] = dot2(xv[k], gv[k], s[i]);
    }
#pragma unroll
    for (int i = 0; i < 8; ++i) {
      const v2u t = __builtin_amdgcn_permlane32_swap(__float_as_uint(s[i]), __float_as_uint(s[i + 8]), false, false);
      s[i] = opaque(__uint_as_float(t.x) + __uint_as_float(t.y));
    }
#pragma unroll
    for (int i = 0; i < 4; ++i) {
      const v2u t = __builtin_amdgcn_permlane16_swap(__float_as_uint(s[i]), __float_as_uint(s[i + 4]), false, false);
      float v = __uint_as_float(t.x) + __uint_as_float(t.y);
      v += __int_as_float(__builtin_amdgcn_update_dpp(0, __float_as_int(v), 0x128, 0xf, 0xf, false));
      v += __int_as_float(__builtin_amdgcn_update_dpp(0, __float_as_int(v), 0x124, 0xf, 0xf, false));
      v += __int_as_float(__builtin_amdgcn_update_dpp(0, __float_as_int(v), 0x122, 0xf, 0xf, false));
      v += __int_as_float(__builtin_amdgcn_update_dpp(0, __float_as_int(v), 0x121, 0xf, 0xf, false));
      s[i] = opaque(v);
    }
    const float m01 = opaque(p & 1 ? s[1] : s[0]), m23 = opaque(p & 1 ? s[3] : s[2]), mine = p & 2 ? m23 : m01;
    const float sl = bflo(pk2(mine, 0.f));
    const float gt = bflo(pk2(1.f / (1.f + __expf(-sl)), 0.f));
    fence_sched();
    asm volatile("s_waitcnt vmcnt(0)\n\ts_barrier" : "+v"(ep) :: "memory");
    tag = epoch_tag(ep);
    sg = sign_of(tag, l31);
    if (q == 0) st64_sc1((v2u){__float_as_uint(gt), (u32)(tag >> 32)}, WS_G + (u32)(tg * 64 + wave * 16 + 4 * g + p) * 8, rws);
    STAMP(1);
    static_for<0, 10>([&](auto I) { constexpr int i = decltype(I)::value; dma2(mreg + rblk(i) * 2048, vv0, vv1, rw2, w2row(i) * 1024); });
    STAMP(2);
    pf = ld32_sc1<0>(vp, rws, 0);
    wait_pin<0>(pf);
    gate(0);
    STAMP(4);
    issue_at(IC<-1>{});
  }
  __builtin_amdgcn_s_setprio(PRG2);

  const u32 tg32 = (u32)(tag >> 32);
  LAS char* pv[2] = {sm + wave * REG + aoff[0], sm + wave * REG + aoff[1]};
  auto vfrag = [&](int k, int mi) { LAS char* a = pv[mi] + rblk(k) * 2048; return lds_tr2(a, a + 512); };
  auto fresh = [&](auto K0, auto K1, u32 m) {
    constexpr int k0 = decltype(K0)::value, k1 = decltype(K1)::value, ke = mn(k1 | 1, 27);
    if (__builtin_expect(any(m & SGN), 0)) {
#pragma unroll 1
      for (int it = 0; it < SPIN_MAX; ++it) {
        static_for<k0, ke + 1>([&](auto K) { hload(K, IC<0>{}, IC<1>{}); hload(K, IC<1>{}, IC<1>{}); });
        wait_vm<0>();
        u32 z = 0;
        static_for<k0, ke + 1>([&](auto K) { constexpr int k = decltype(K)::value; pin(hb[k][0], hb[k][1]); if constexpr (k <= k1) z |= strip(hb[k][0], sg) | strip(hb[k][1], sg); });
        if (!any(z & SGN)) break;
        __builtin_amdgcn_s_sleep(1);
      }
    }
  };
  auto abad = [&]() {
    u32 m = 0;
#pragma unroll
    for (int i = A0; i < 5; ++i) m |= (av[i].y ^ tg32) | (av[i].w ^ tg32);
    return any(m);
  };
  LAS char* ap = sm + wave * REG + 9 * 2048;
  v4u a4[2];
  float ga[2][8], gs[2];
  auto aput = [&]() {
    *(LAS v4u*)(ap + l7 * 128 + ((l3 ^ l7) << 4)) = (v4u){av[0].x, av[1].x, av[2].x, av[3].x};
    *(LAS v4u*)(ap + l7 * 128 + ((l3 ^ l7) << 4) + 1024) = (v4u){av[0].z, av[1].z, av[2].z, av[3].z};
    a4[0] = *(LAS v4u*)(ap + l3 * 128 + ((l3 ^ l7) << 4));
    a4[1] = *(LAS v4u*)(ap + l3 * 128 + ((l3 ^ l7) << 4) + 1024);
  };
  auto amix = [&](int e, int k) {
    const float gt = __uint_as_float(e ? av[4].z : av[4].x), gn = opaque(1.f - gt);
    ga[e][2 * k] = opaque(__builtin_fmaf(gt, bflo(a4[e][k]), opaque(gn * bflo(av[5][k]))));
    ga[e][2 * k + 1] = opaque(__builtin_fmaf(gt, bfhi(a4[e][k]), opaque(gn * bfhi(av[5][k]))));
    if (k == 0) gs[e] = gn;
  };
  f16v o2[2][2];
  bf8 wb[28][2];
  bool late = false;
  fence_sched();
  wait_pin<mn(TB.younger(OH, 0, 1, G2P), AUX ? 63 : TB.younger(OR, 0, 1, G2P))>(hb[0][0], hb[0][1]);
  wb[0][0] = vfrag(0, 0); wb[0][1] = vfrag(0, 1);
  fresh(IC<0>{}, IC<0>{}, strip(hb[0][0], sg) | strip(hb[0][1], sg));
  STAMP(7);
  static_for<0, 28 - NT>([&](auto K) {
    constexpr int k = decltype(K)::value, kl = k == 27 - NT ? 27 : k + 1;
    fence_sched();
    wait_vm<g2wait<AUX>(k)>();
    static_for<k + 1, kl + 1>([&](auto J) { constexpr int jn = decltype(J)::value; pin(hb[jn][0], hb[jn][1]); });
    if constexpr (k == KC) { wait_pin<TB.younger(OP, 1, 0, G2P + 4 * k)>(pf); gate(1); }
    if constexpr (k == KB) { wait_pin<TB.younger(OA, 0, 5, G2P + 4 * k)>(av); }
    STAMPQ(32 + k);
    u32 m = 0;
    static_for<0, 4>([&](auto G) {
      constexpr int gi = decltype(G)::value, mi = gi & 1, ni = gi >> 1;
      m0_for(IC<G2P + 4 * k + gi>{});
      fence_sched();
      if constexpr (k == 0) o2[mi][ni] = mfma32(wb[k][mi], __builtin_bit_cast(bf8, hb[k][ni]), (f16v)(0.f));
      else o2[mi][ni] = mfma32(wb[k][mi], __builtin_bit_cast(bf8, hb[k][ni]), o2[mi][ni]);
      fence_sched();
      if constexpr (gi < 2) static_for<k + 1, kl + 1>([&](auto J) {
        constexpr int jn = decltype(J)::value;
        wb[jn][gi] = vfrag(jn, gi);
        m |= strip(hb[jn][gi], sg);
      });
      if constexpr (k == KB && gi == 0) aput();
      if constexpr (k == KB && gi == 2) late = abad();
      if constexpr (k > KB && k <= KB + 2) amix(k - KB - 1, gi);
      fence_sched();
      issue_at(IC<G2P + 4 * k + gi>{});
      if constexpr (gi == 2) fresh(IC<k + 1>{}, IC<kl>{}, m);
    });
  });
  fence_sched();
  LAS char* pw = sm + wave * REG + l31 * 256;
  const int kw = (4 * hh) ^ (((l31 >> 1) & 3) | ((l31 & 1) << 2));
  auto pput = [&](int tl, int i) {
    const f16v& a = o2[tl >> 1][tl & 1];
    *(LAS f4*)(pw + ((kw ^ i) << 4) + (tl >> 1) * 128 + (tl & 1) * 8192) = (f4){a[4 * i], a[4 * i + 1], a[4 * i + 2], a[4 * i + 3]};
  };
  static_for<0, 4 * NT>([&](auto N) {
    constexpr int n = decltype(N)::value, tl = n / NT, v = n % NT, k = 28 - NT + v;
    o2[tl >> 1][tl & 1] = mfma32(wb[k][tl >> 1], __builtin_bit_cast(bf8, hb[k][tl & 1]), o2[tl >> 1][tl & 1]);
    fence_sched();
    if constexpr (constexpr int u = 4 * (27 - NT - KB) + n; u < 8) amix(u >> 2, u & 3);
    if constexpr (tl >= 1 && v == NT - 1) { pput(tl - 1, 0); pput(tl - 1, 1); pput(tl - 1, 2); pput(tl - 1, 3); }
    fence_sched();
  });
  STAMP(10); STAMPC(62);
  if (__builtin_expect(late, 0)) {
#pragma unroll 1
    for (int it = 0; it < SPIN_MAX; ++it) {
      static_for<0, 5>(aload);
      wait_pin<0>(av);
      if (!abad()) break;
      __builtin_amdgcn_s_sleep(1);
    }
    aput();
#pragma unroll
    for (int i = 0; i < 8; ++i) amix(i >> 2, i & 3);
  }
#pragma unroll
  for (int i = 0; i < 4; ++i) pput(3, i);
  lds_bar();
  STAMP(11);
  {
    LAS char* pr = sm + (16 * wave + 2 * l3) * 256;
    const int kr = (2 * l7) ^ (l3 & 3);
#pragma unroll
    for (int e = 0; e < 2; ++e) {
      v4u d;
#pragma unroll
      for (int cc = 0; cc < 2; ++cc) {
        LAS char* a = pr + e * 256 + ((kr ^ cc ^ (4 * e)) << 4);
        const f4 s = (*(LAS f4*)(a) + *(LAS f4*)(a + REG)) + (*(LAS f4*)(a + 2 * REG) + *(LAS f4*)(a + 3 * REG));
#pragma unroll
        for (int k = 0; k < 2; ++k)
          d[2 * cc + k] = pk2(opaque(__builtin_fmaf(gs[e], s[2 * k], ga[e][4 * cc + 2 * k])), opaque(__builtin_fmaf(gs[e], s[2 * k + 1], ga[e][4 * cc + 2 * k + 1])));
      }
      *(v4u*)(out + (size_t)(64 * t2 + 16 * wave + 2 * l3 + e) * 512 + 64 * c2 + 8 * l7) = d;
    }
  }
  STAMP(12);
  STAMP_OUT;
}

struct cf { float x, y; };
DI cf cmul(cf a, cf m) { return cf{opaque(__builtin_fmaf(a.x, m.x, -opaque(a.y * m.y))), opaque(__builtin_fmaf(a.x, m.y, opaque(a.y * m.x)))}; }
DI cf cexp_s(float lr, float rv, float m) {
  const float e = __builtin_amdgcn_exp2f(lr * m), f = __builtin_amdgcn_fractf(rv * m);
  return cf{opaque(e * __builtin_amdgcn_cosf(f)), opaque(e * __builtin_amdgcn_sinf(f))};
}

constexpr u32 TROW = 544, TCH = 63 * TROW, XT = 69632;
constexpr u32 KCH = 9472, KOO = 4656, UA0 = 18944, UCH = 8768, UBO = 4224, EX = 40960;

DI void touch(u32& d, u32 vo, rsrc_t rs) { asm volatile("buffer_load_dword %0, %1, %2, 0 offen" : "+v"(d) : "v"(vo), "s"(rs) : "memory"); }
DI void touch_ws(u32& d, u32 vo, rsrc_t rs) { asm volatile("buffer_load_dword %0, %1, %2, 0 offen " PFQ : "+v"(d) : "v"(vo), "s"(rs) : "memory"); }
constexpr int NPF = !!(PF & 1) + !!(PF & 2) + 2 * !!(PF & 4) + !!(PF & 8) + !!(PF & 16);

DI void conv_role(int ci, LAS char* sm, const u16* x, const u16* are, const u16* aim, const u16* bre, const u16* bim,
                  const u16* cre, const u16* cim, const u16* logdt, const u16* dskip, const u16* w1, const u16* w2, char* ws) {
  const int tid = threadIdx.x, lane = tid & 63, wave = __builtin_amdgcn_readfirstlane(tid >> 6);
  STAMP_DECL;
  STAMP(0);
  const rsrc_t rws = mkrsrc(ws, WS_END), rx = mkrsrc(x, 2048 * 512 * 2);
  v2u ep = ld64_sc1(0, rws);
  __builtin_amdgcn_s_setprio(PRC0);
  const u32 smb = (u32)(uintptr_t)sm;
  const int pr = (ci & 7) * 32 + (ci >> 3), cg = (ci >> 3) & 7, tt = 4 * (ci & 7) + (ci >> 6);
  const int g = lane >> 4, q = (lane >> 2) & 3, p = lane & 3, n = lane & 15;
  const int ch = wave >> 1, half = wave & 1;

  {
    const u32 v0 = (u32)(64 * tt + 16 * wave + (lane >> 3)) * 1024 + cg * 128 + (((lane & 7) ^ ((g & 1) << 1)) << 4);
    const u32 v1 = (v0 ^ 64) + 7168;
    dma2(smb + XT + wave * 2048, v0, v1, rx, 0);
  }
  const int pi = pr * 256 + tid;
  u32 r_are = are[pi], r_aim = aim[pi], r_bre = bre[pi], r_bim = bim[pi], r_cre = cre[pi], r_cim = cim[pi];
  u32 r_ldt = logdt[2 * pr + ch];
  u32 r_dsk = *(const u32*)(dskip + 2 * pr);
  fence_sched();
  asm volatile("s_waitcnt vmcnt(0)" : "+v"(r_are), "+v"(r_aim), "+v"(r_bre), "+v"(r_bim), "+v"(r_cre), "+v"(r_cim), "+v"(r_ldt), "+v"(r_dsk), "+v"(ep) :: "memory");
  const u64 tag = epoch_tag(ep);
  const u32 tg32 = (u32)(tag >> 32);
  fence_sched();
  STAMP(1);
  {
    LAS char* tb = sm + XT + wave * 2048 + (8 * (p & 1) + q) * 128 + (p >> 1) * 8;
    const int sw = (q & 2) | ((p & 1) << 2);
    const u32 vo = WS_UT + (u32)((16 * cg + 2 * g + (q >> 1)) * 256 + 8 * tt + 2 * wave + (q & 1)) * 64 + p * 16;
    const bf8 d0 = lds_tr2(tb + ((g ^ sw) << 4), tb + ((g ^ sw) << 4) + 512);
    const bf8 d1 = lds_tr2(tb + (((4 + g) ^ sw) << 4), tb + (((4 + g) ^ sw) << 4) + 512);
    st128_sc1<0>(__builtin_bit_cast(v4u, d0), vo, rws, 0);
    st128_sc1<0>(__builtin_bit_cast(v4u, d1), vo, rws, 8 * 256 * 64);
  }
  fence_sched();
  u32 tch = 0;
  {
    const u32 wv = 4 * ci + wave, big = 1u << 30;
    if (PF & 1) touch(tch, lane < 7 ? (wv >> 1) * 3584 + ((wv & 1) * 7 + lane) * 256 + 128 : big, mkrsrc(w1, 512 * 1792 * 2));
    if (PF & 2) touch(tch, lane < 14 ? (wv * 14 + lane) * 128 : big, mkrsrc(w2, 512 * 1792 * 2));
    if (PF & 4) {
      const u32 wx = 4 * (ci >> 3) + wave;
      touch(tch, (wx * 112 + lane) * 128, mkrsrc(w2, 512 * 1792 * 2));
      touch(tch, lane < 48 ? (wx * 112 + 64 + lane) * 128 : big, mkrsrc(w2, 512 * 1792 * 2));
    }
    if (PF & 8) touch_ws(tch, lane < 56 ? WS_H + (wv * 56 + lane) * 128 : big, rws);
    if (PF & 16) touch_ws(tch, lane < 32 ? WS_A + (wv * 32 + lane) * 128 : big, rws);
  }
  fence_sched();

  __builtin_amdgcn_s_setprio(PRC1);
  const u32 uf = WS_UFLAG + (u32)(cg * 128 + tt * 4 + wave) * FSP;
  {
    const float dt = bflo(pk2(expf(bflo(r_ldt)), 0.f));
    const float lr = dt * bflo(r_are) * 1.44269504088896341f, rv = dt * bflo(r_aim) * 0.15915494309189535f;
    const cf db = {opaque(dt * bflo(r_bre)), opaque(-dt * bflo(r_bim))};
    const cf cb = cmul(cf{bflo(r_cre), -bflo(r_cim)}, db);
    const cf e16 = cexp_s(lr, rv, 16.f), e1 = cexp_s(lr, -rv, 1.f), e256 = cexp_s(lr, -rv, 256.f);
    const cf e2 = cexp_s(lr, -rv, 2.f), e4 = cexp_s(lr, -rv, 4.f), e8 = cexp_s(lr, -rv, 8.f);
    LAS char* tw = sm + ch * TCH + (tid & 127) * 4;
    auto put = [&](int row, cf v) { *(volatile LAS u32*)(tw + row * TROW) = pk2(v.x, v.y); };
    cf a = e16, t0 = cb, tb[8], v[8];
    put(0, cf{1.f, 0.f}); put(16, t0);
    tb[0] = cb;
#pragma unroll
    for (int i = 1; i < 16; ++i) {
      if (i == UFP) { wait_vm<NPF>(); flag_store(uf, rws, tag); }
      put(i, a); if (i < 15) a = cmul(a, e16);
      t0 = cmul(t0, e1); put(16 + i, t0);
      if (i < 8) { tb[i] = cmul(tb[i - 1], e256); v[i] = tb[i]; }
#pragma unroll
      for (int tl = 1; tl < 8; ++tl) {
        const int base = tl == 1 ? 32 : (tl < 4 ? 41 + 5 * (tl - 2) : 51 + 3 * (tl - 4));
        const int cnt = tl == 1 ? 9 : (tl < 4 ? 5 : 3), k = i - tl;
        if (k >= 0 && k < cnt) {
          if (k > 0) v[tl] = cmul(v[tl], tl == 1 ? e2 : (tl < 4 ? e4 : e8));
          put(base + k, v[tl]);
        }
      }
    }
  }
  fence_sched();
  if (UFP >= 16) { wait_vm<NPF>(); flag_store(uf, rws, tag); }
  const u32 vf = WS_UFLAG + (u32)((ci & 7) * 128 + wave * 32 + (lane & 31)) * FSP;
  v2u pf = ld64_sc1(vf, rws);
  STAMP(2);
  lds_bar();

  f4 kk[4];
  v4u uv[2];
  {
    LAS char* tb = sm + ch * TCH + g * 16;
    const int m2 = (n + ((n >> 1) & 1)) >> 1, m4 = (n + 1 + ((n >> 2) & 1)) >> 2, m8 = (n + 3 + ((n >> 3) & 1)) >> 3;
    LAS char* pr_[4] = {tb + (half ? 51 + m8 : 16 + n) * TROW, tb + (half ? 54 + m8 : 32 + m2) * TROW,
                        tb + (half ? 57 + m8 : 41 + m4) * TROW, tb + (half ? 60 + m8 : 46 + m4) * TROW};
    LAS char* pe = tb + n * TROW;
#pragma unroll
    for (int i = 0; i < 4; ++i) kk[i] = (f4)(0.f);
#pragma unroll
    for (int ks = 0; ks < 8; ++ks) {
      if (ks == 4) {
        fence_sched();
        wait_pin<0>(pf);
        asm volatile("" :: "v"(tch));
        if (__builtin_expect(flag_bad(pf, tag), 0)) flag_wait(vf, rws, tag);
        STAMP(3);
        const u32 vo = WS_UT + (u32)((pr >> 1) * 256 + 64 * wave + (lane >> 1)) * 64 + ((pr & 1) * 2 + (lane & 1)) * 16;
        uv[0] = ld128_sc1<0>(vo, rws, 0); uv[1] = ld128_sc1<2048>(vo, rws, 0);
        fence_sched();
      }
      const bf8 e = lds_rd128(pe + ks * 64);
#pragma unroll
      for (int i = 0; i < 4; ++i) kk[i] = mfma16(lds_rd128(pr_[i] + ks * 64), e, kk[i]);
    }
  }
  fence_sched();
  STAMP(4);
  lds_bar();
  {
    LAS char* kb = sm + ch * KCH;
#pragma unroll
    for (int i = 0; i < 4; ++i) {
      const int l0 = 256 * (4 * half + i) + 16 * n + 4 * g;
      const float k0 = opaque(kk[i][0]), k1 = opaque(kk[i][1]), k2 = opaque(kk[i][2]), k3 = opaque(kk[i][3]);
      const u32 a = pk2(k3, k2), bq = pk2(k1, k0), c = pk2(k2, k1);
      *(LAS v2u*)(kb + KOO + 2 * (2044 - l0)) = (v2u){a, bq};
      *(LAS u16*)(kb + 2 * (2051 - l0)) = (u16)a;
      *(LAS u32*)(kb + 2 * (2052 - l0)) = c;
      *(LAS u16*)(kb + 2 * (2054 - l0)) = (u16)(bq >> 16);
    }
    const v4u z = {0, 0, 0, 0};
    if (lane < 29) *(LAS v4u*)(kb + (half ? KOO + 4096 : 4112) + lane * 16) = z;
    else if (lane < 33) *(LAS v4u*)(sm + UA0 + ch * UCH + (half ? UBO : 0) + (lane - 29) * 16 - (half ? 0 : 16)) = z;
    else if (lane == 33 && !half) *(LAS u16*)(kb + 2 * 2055) = 0;
    else if (lane == 34 && !half) *(LAS v3u*)(sm + UA0 + ch * UCH + 48) = (v3u){0, 0, 0};
  }
  asm volatile("s_waitcnt vmcnt(0)" : "+v"(uv[0]), "+v"(uv[1]) :: "memory");
#pragma unroll
  for (int k = 0; k < 2; ++k) {
    LAS char* ub = sm + UA0 + (lane & 1) * UCH + 64 + (64 * wave + 32 * k + (lane >> 1)) * 16;
    *(LAS v4u*)(ub + UBO) = uv[k];
    *(LAS u32*)(ub - 4) = uv[k].x;
    *(LAS v3u*)(ub) = (v3u){uv[k].y, uv[k].z, uv[k].w};
  }
  lds_bar();
  STAMP(5);
  __builtin_amdgcn_s_setprio(PRCV);

  f4 y[8];
  {
    LAS char* pk = sm + ch * KCH + ((n & 1) ? KOO + 4032 : 4048) - 64 * (n >> 1) + 16 * g - 3584 + 256 * half;
    LAS char* pu = sm + UA0 + ch * UCH + (n < 8 ? 8 * n : UBO + 8 * (n - 8) + 8) + 16 * g + 256 * half;
    bf8 kf[4][8], uf[4][8];
    auto rd = [&](int rr, int i) {
      kf[rr][i] = lds_rd128(pk + 512 * (7 - i) + 64 * rr);
      const v2u lo = *(volatile LAS v2u*)(pu + 512 * i + 64 * rr), hi = *(volatile LAS v2u*)(pu + 512 * i + 64 * rr + 8);
      uf[rr][i] = __builtin_bit_cast(bf8, (v4u){lo.x, lo.y, hi.x, hi.y});
    };
#pragma unroll
    for (int i = 0; i < 8; ++i) { rd(0, i); fence_sched(); }
    static_for<0, 144>([&](auto N) {
      constexpr int nn = decltype(N)::value, rr = nn / 36, v = nn % 36;
      constexpr int s = v < 1 ? 0 : v < 3 ? 1 : v < 6 ? 2 : v < 10 ? 3 : v < 15 ? 4 : v < 21 ? 5 : v < 28 ? 6 : 7, m = v - s * (s + 1) / 2;
      if constexpr (rr == 0 && m == 0) y[s] = mfma16(kf[rr][s - m], uf[rr][m], (f4)(0.f));
      else y[s] = mfma16(kf[rr][s - m], uf[rr][m], y[s]);
      fence_sched();
      if constexpr (rr < 3 && v % 4 == 2 && v / 4 < 8) rd(rr + 1, v / 4);
      fence_sched();
    });
  }
  STAMP(6);
#pragma unroll
  for (int a = 0; a < 8; ++a) *(LAS f4*)(sm + EX + (wave * 8 + a) * 1024 + lane * 16) = y[a];
  lds_bar();
  {
    const float d0 = bflo(r_dsk), d1 = bfhi(r_dsk);
    const int rn = n < 8 ? 2 * n : 2 * (n - 8) + 1;
#pragma unroll
    for (int i = 0; i < 2; ++i) {
      const int al = 2 * wave + i;
      LAS char* ex = sm + EX + al * 1024 + lane * 16;
      const f4 y0 = *(LAS f4*)(ex) + *(LAS f4*)(ex + 8192), y1 = *(LAS f4*)(ex + 16384) + *(LAS f4*)(ex + 24576);
      LAS char* up = sm + UA0 + UBO + 64 + (256 * al + 2 * rn + 64 * g) * 2;
      const u32 u00 = *(LAS u32*)(up), u01 = *(LAS u32*)(up + 64), u10 = *(LAS u32*)(up + UCH), u11 = *(LAS u32*)(up + UCH + 64);
      auto mix = [&](float y, float u, float d) { return opaque(__builtin_fmaf(u, d, opaque(y))); };
      const u32 x0 = pk2(mix(y0[0], bflo(u00), d0), mix(y1[0], bflo(u10), d1)), x1 = pk2(mix(y0[1], bfhi(u00), d0), mix(y1[1], bfhi(u10), d1));
      const u32 z0 = pk2(mix(y0[2], bflo(u01), d0), mix(y1[2], bflo(u11), d1)), z1 = pk2(mix(y0[3], bfhi(u01), d0), mix(y1[3], bfhi(u11), d1));
      const u32 vo = WS_A + (u32)(4 * al + g) * 131072 + pr * 512 + rn * 16;
      st128_sc1<0>((v4u){x0, tg32, x1, tg32}, vo, rws, 0);
      st128_sc1<256>((v4u){z0, tg32, z1, tg32}, vo, rws, 0);
    }
  }
  STAMP(7);
  if (ci == 0) {
    granule_wait(WS_A + (u32)tid * 512, rws, tg32);
    if (tid < 224) flag_wait(WS_EFLAG + (u32)tid * 4 * FSP, rws, tag);
    else granule_wait(WS_G + (u32)(tid - 224) * 512, rws, tg32);
    lds_bar();
    if (wave == 0) flag_store(0, rws, tag);
  }
  STAMP_OUT;
}

__global__ __launch_bounds__(256, 2) void fused_kernel(const u16* __restrict__ x, const u16* __restrict__ are, const u16* __restrict__ aim,
                                                       const u16* __restrict__ bre, const u16* __restrict__ bim, const u16* __restrict__ cre,
                                                       const u16* __restrict__ cim, const u16* __restrict__ logdt, const u16* __restrict__ dskip,
                                                       const u16* __restrict__ w1, const u16* __restrict__ b1, const u16* __restrict__ w2,
                                                       const u16* __restrict__ b2, const u16* __restrict__ gw, u16* __restrict__ out, char* ws) {
  __shared__ __attribute__((aligned(256))) char smem[81920];
  LAS char* sm = (LAS char*)smem;
  asm volatile("" :: "s"(x), "s"(are), "s"(aim), "s"(bre), "s"(bim), "s"(cre), "s"(cim), "s"(logdt), "s"(dskip), "s"(w1), "s"(b1),
               "s"(w2), "s"(b2), "s"(gw), "s"(out), "s"(ws));
  const int b = blockIdx.x;
  const bool mlp = (b < 256) == (GFIRST != 0);
  if (mlp && (b & 255) < 224) gemm_role<false>(b & 255, sm, x, w1, b1, w2, b2, gw, out, ws);
  else if (mlp) gemm_role<true>(b & 255, sm, x, w1, b1, w2, b2, gw, out, ws);
  else conv_role(b & 255, sm, x, are, aim, bre, bim, cre, cim, logdt, dskip, w1, w2, ws);
}

extern "C" void kernel_launch(void* const* d_in, const int* in_sizes, int n_in, void* d_out, int out_size, void* d_ws, size_t ws_size,
                              hipStream_t stream) {
  const u16* const* in = (const u16* const*)d_in;
  fused_kernel<<<512, 256, 0, stream>>>(in[0], in[1], in[2], in[3], in[4], in[5], in[6], in[7], in[8], in[10], in[11], in[12], in[13],
                                        in[14], (u16*)d_out, (char*)d_ws + (((ws_size - WS_END) / 16 * WSPOS) & ~(size_t)0xfffff));
}
```

```cpp
#define PROBE 4
#include <hip/hip_runtime.h>
#include <stdint.h>
#include <type_traits>
#pragma clang diagnostic ignored "-Winline-asm"

typedef unsigned short u16;
typedef unsigned int u32;
typedef unsigned long long u64;
typedef __attribute__((ext_vector_type(2))) u32 v2u;
typedef __attribute__((ext_vector_type(3))) u32 v3u;
typedef __attribute__((ext_vector_type(4))) u32 v4u;
typedef __attribute__((ext_vector_type(2))) float f2;
typedef __attribute__((ext_vector_type(4))) float f4;
typedef __attribute__((ext_vector_type(16))) float f16v;
typedef __attribute__((ext_vector_type(2))) __bf16 bf2;
typedef __attribute__((ext_vector_type(8))) __bf16 bf8;
typedef __attribute__((ext_vector_type(4))) short s4;
typedef __amdgpu_buffer_rsrc_t rsrc_t;
#define DI __device__ __forceinline__
#define LAS __attribute__((address_space(3)))
#define CAS __attribute__((address_space(4)))

#define GFIRST 0
#define LAM 4
#define UPP 2
#define PA 110
#define PC 127
#define PD 8
#define KP 6
#define KC 9
#define KA 19
#define KB 25
#define NT 2
#define W2PF 0
#define PRG1 2
#define PRG2 1
#define PRC0 3
#define PRC1 1
#define PRCV 2
#define UFP 8
#define PF 3
#define WSPOS 0
#define PFQ "sc1"

constexpr u32 FSP = 256;
constexpr u32 WS_G = 4096, WS_EFLAG = 65536, WS_UFLAG = WS_EFLAG + 1024 * FSP;
constexpr u32 WS_UT = 1u << 20, WS_A = 3u << 20, WS_H = 7u << 20, WS_END = 15u << 20;
constexpr int SPIN_MAX = 1 << 16;
constexpr int A0 = 0;

#define STAMP(i)
#define STAMPQ(i)
#define STAMPC(i)
#define STAMP_DECL
#define STAMP_OUT

DI rsrc_t mkrsrc(const void* p, u32 bytes) { return __builtin_amdgcn_make_buffer_rsrc((void*)p, (short)0, (int)bytes, 0x00020000); }
DI float bflo(u32 v) { return __uint_as_float(v << 16); }
DI float bfhi(u32 v) { return __uint_as_float(v & 0xffff0000u); }
DI u32 pk2(float a, float b) { bf2 r = __builtin_convertvector((f2){a, b}, bf2); return __builtin_bit_cast(u32, r); }
DI float dot2(u32 a, u32 b, float c) { return __builtin_amdgcn_fdot2_f32_bf16(__builtin_bit_cast(bf2, a), __builtin_bit_cast(bf2, b), c, false); }
DI float relu(float v) { return __int_as_float(max(__float_as_int(v), 0)); }
DI void fence_sched() { __builtin_amdgcn_sched_barrier(0); }

template <int N> DI void wait_vm() { asm volatile("s_waitcnt vmcnt(%0)" :: "n"(N) : "memory"); }
template <int N> DI void wait_vm_bar() { asm volatile("s_waitcnt vmcnt(%0) lgkmcnt(0)\n\ts_barrier" :: "n"(N) : "memory"); }
DI void lds_bar() { asm volatile("s_waitcnt lgkmcnt(0)\n\ts_barrier" ::: "memory"); }
DI void wait_lgkm() { asm volatile("s_waitcnt lgkmcnt(0)" ::: "memory"); }

DI void dma2(u32 m0v, u32 v0, u32 v1, rsrc_t rs, u32 so) {
  asm volatile("s_mov_b32 m0, %0\n\ts_nop 0\n\t"
               "buffer_load_dwordx4 %1, %3, %4 offen lds\n\t"
               "buffer_load_dwordx4 %2, %3, %4 offen offset:1024 lds"
               :: "s"(m0v), "v"(v0), "v"(v1), "s"(rs), "s"(so) : "m0", "memory");
}
template <u32 off> DI void set_m0(u32 base) { asm volatile("s_add_u32 m0, %0, %1" :: "s"(base), "n"(off) : "m0", "scc"); }
template <int OFF> DI void dma1(u32 v, rsrc_t rs, u32 so) {
  asm volatile("buffer_load_dwordx4 %0, %1, %2 offen offset:%3 lds" :: "v"(v), "s"(rs), "s"(so), "n"(OFF) : "memory");
}
template <int OFF> DI void reld128_sc1(v4u& r, u32 vo, rsrc_t rs, u32 so) {
  asm volatile("buffer_load_dwordx4 %0, %1, %2, %3 offen offset:%4 sc1" : "+v"(r) : "v"(vo), "s"(rs), "s"(so), "n"(OFF) : "memory");
}
template <int OFF> DI u32 ld32_sc1(u32 vo, rsrc_t rs, u32 so) {
  u32 r; asm volatile("buffer_load_dword %0, %1, %2, %3 offen offset:%4 sc1" : "=&v"(r) : "v"(vo), "s"(rs), "s"(so), "n"(OFF) : "memory"); return r;
}
template <int OFF> DI v4u ld128_sc1(u32 vo, rsrc_t rs, u32 so) {
  v4u r; asm volatile("buffer_load_dwordx4 %0, %1, %2, %3 offen offset:%4 sc1" : "=&v"(r) : "v"(vo), "s"(rs), "s"(so), "n"(OFF) : "memory"); return r;
}
DI v4u ld128(u32 vo, rsrc_t rs) {
  v4u r; asm volatile("buffer_load_dwordx4 %0, %1, %2, 0 offen" : "=&v"(r) : "v"(vo), "s"(rs) : "memory"); return r;
}
DI v2u ld64_sc1(u32 vo, rsrc_t rs) {
  v2u r; asm volatile("buffer_load_dwordx2 %0, %1, %2, 0 offen sc1" : "=&v"(r) : "v"(vo), "s"(rs) : "memory"); return r;
}
template <int OFF> DI u32 ld16(u32 vo, rsrc_t rs) {
  u32 r; asm volatile("buffer_load_ushort %0, %1, %2, 0 offen offset:%3" : "=&v"(r) : "v"(vo), "s"(rs), "n"(OFF) : "memory"); return r;
}
template <int OFF> DI void st128_sc1(v4u d, u32 vo, rsrc_t rs, u32 so) {
  asm volatile("buffer_store_dwordx4 %0, %1, %2, %3 offen offset:%4 sc1\n\ts_nop 1" :: "v"(d), "v"(vo), "s"(rs), "s"(so), "n"(OFF) : "memory");
}
DI void st64_sc1(v2u d, u32 vo, rsrc_t rs) {
  asm volatile("buffer_store_dwordx2 %0, %1, %2, 0 offen sc1" :: "v"(d), "v"(vo), "s"(rs) : "memory");
}
DI void flag_store(u32 off, rsrc_t rs, u64 tag) {
  const v2u d = {(u32)tag, (u32)(tag >> 32)};
  asm volatile("s_mov_b64 exec, 1\n\tbuffer_store_dwordx2 %0, off, %1, %2 sc1\n\ts_mov_b64 exec, -1" :: "v"(d), "s"(rs), "s"(off) : "memory");
}
DI u64 epoch_tag(v2u e) {
  const u64 v = (u64)(u32)__builtin_amdgcn_readfirstlane(e.x) | ((u64)(u32)__builtin_amdgcn_readfirstlane(e.y) << 32);
  return ((v * 0x9E3779B97F4A7C15ull + 0x632BE59BD9B4E019ull) & ~0xffffull) | (~v & 0xffffull);
}
constexpr u32 SGN = 0x80008000u;
DI u32 sign_of(u64 tag, int t) { return ((((u32)tag >> (t >> 1)) ^ t) & 1) ? SGN : 0u; }
DI u32 strip(v4u& d, u32 s) { d ^= s; return (d.x | d.y) | (d.z | d.w); }
DI bool any(u32 m) { return __builtin_amdgcn_ballot_w64(m != 0) != 0; }
DI bool flag_bad(v2u v, u64 tag) { return __builtin_amdgcn_ballot_w64((v.x != (u32)tag) | (v.y != (u32)(tag >> 32))) != 0; }
DI void flag_wait(u32 vo, rsrc_t rs, u64 tag) {
#pragma unroll 1
  for (int it = 0; it < SPIN_MAX; ++it) {
    v2u v; asm volatile("buffer_load_dwordx2 %0, %1, %2, 0 offen sc1\n\ts_waitcnt vmcnt(0)" : "=&v"(v) : "v"(vo), "s"(rs) : "memory");
    if (!flag_bad(v, tag)) break;
    __builtin_amdgcn_s_sleep(1);
  }
}

DI void granule_wait(u32 vo, rsrc_t rs, u32 tg) {
#pragma unroll 1
  for (int it = 0; it < SPIN_MAX; ++it) {
    v2u v; asm volatile("buffer_load_dwordx2 %0, %1, %2, 0 offen sc1\n\ts_waitcnt vmcnt(0)" : "=&v"(v) : "v"(vo), "s"(rs) : "memory");
    if (__builtin_amdgcn_ballot_w64(v.y != tg) == 0) break;
    __builtin_amdgcn_s_sleep(1);
  }
}
DI float opaque(float v) { asm("" : "+v"(v)); return v; }

DI bf8 lds_rd128(LAS char* p) { return *(LAS bf8*)p; }
DI bf8 lds_tr2(LAS char* p0, LAS char* p1) {
  s4 a = __builtin_amdgcn_ds_read_tr16_b64_v4i16((LAS s4*)p0);
  s4 b = __builtin_amdgcn_ds_read_tr16_b64_v4i16((LAS s4*)p1);
  v2u x = __builtin_bit_cast(v2u, a), y = __builtin_bit_cast(v2u, b);
  return __builtin_bit_cast(bf8, (v4u){x.x, x.y, y.x, y.y});
}
DI f16v mfma32(bf8 a, bf8 b, f16v c) { return __builtin_amdgcn_mfma_f32_32x32x16_bf16(a, b, c, 0, 0, 0); }
DI f4 mfma16(bf8 a, bf8 b, f4 c) { return __builtin_amdgcn_mfma_f32_16x16x32_bf16(a, b, c, 0, 0, 0); }

template <int I, int N, class F> DI void static_for(F&& f) {
  if constexpr (I < N) { f(std::integral_constant<int, I>{}); static_for<I + 1, N>(f); }
}

constexpr u32 REG = 20480;
constexpr int wblk(int t) { constexpr int b[16] = {6, 7, 8, 9, 6, 7, 8, 9, 6, 7, 4, 5, 8, 0, 1, 9}; return b[t]; }
constexpr int rblk(int k) { constexpr int b[10] = {2, 3, 6, 7, 4, 5, 8, 0, 1, 9}; return b[k % 10]; }
constexpr u32 w2row(int k) { return 128 * ((k % 14) >> 1) + 64 * (k / 14) + 8 * (k & 1); }
constexpr int hstep(int k) { return 8 * ((k % 14) >> 1) + 4 * (k / 14) + (k & 1); }

enum { OX, OW, OR, OS, OE, OH, OP, OA };
struct Op { int pos, kind, idx, sub; };
struct OpTab {
  Op op[256]; int n;
  constexpr void add(int pos, int kind, int idx, int sub) { op[n] = Op{pos, kind, idx, sub}; ++n; }
  constexpr int at(int kind, int idx, int sub) const {
    for (int i = 0; i < n; ++i) if (op[i].kind == kind && op[i].idx == idx && op[i].sub == sub) return i;
    return -1;
  }
  constexpr int from(int pos) const { int i = 0; while (i < n && op[i].pos < pos) ++i; return i; }
  constexpr int younger(int kind, int idx, int sub, int pos) const {
    int c = 0, a = at(kind, idx, sub);
    if (a < 0 || op[a].pos >= pos) return -1000;
    for (int i = a + 1; i < n; ++i) if (op[i].pos < pos) ++c;
    return c > 63 ? 63 : c;
  }
  constexpr int lds(int i) const {
    const Op o = op[i];
    return o.kind == OX ? (o.idx % 3) * 4096 + (o.sub >> 1) * 2048 : o.kind == OW ? wblk(o.idx) * 2048 : o.kind == OR ? rblk(o.idx) * 2048 : -1;
  }
  constexpr int m0_at(int pos) const {
    int cur = -1;
    for (int i = 0; i < n; ++i) if (lds(i) >= 0) { if (op[i].pos == pos) return lds(i) != cur ? lds(i) : -1; cur = lds(i); }
    return -1;
  }
  constexpr bool m0_ok() const {
    for (int i = 0; i < n; ++i) for (int j = i + 1; j < n; ++j)
      if (op[i].pos == op[j].pos && op[i].pos >= 0 && lds(i) >= 0 && lds(j) >= 0 && lds(i) != lds(j)) return false;
    return true;
  }
};
constexpr int EP0 = 61, G2P = 200;
constexpr int mn(int a, int b) { return a < b ? a : b; }
template <bool AUX> constexpr OpTab optab() {
  OpTab t{};
  int nh = 0;
  if (!AUX) {
    for (int c = 0; c < 3; ++c) { for (int s = 0; s < 4; ++s) t.add(-1, OX, c, s); t.add(-1, OW, c, 0); t.add(-1, OW, c, 1); }
    t.add(-1, OW, 3, 0); t.add(-1, OW, 3, 1);
    for (int n = 0; n < 128; ++n) {
      const int T = (n + LAM) / 8, i = (n + LAM) % 8;
      if (T >= 1 && T <= 5) { if (i < 4) t.add(n, OX, T + 2, i); else if (i < 6) t.add(n, OW, T + 3, i - 4); }
      if (T >= 6 && T <= 7 && i < 6) t.add(n, OW, 9 + 3 * (T - 6) + i / 2, i & 1);
      if (T == 8 && i < 6) { if (i < 2) t.add(n, OW, 15, i); else t.add(n, OR, i / 2 - 1, i & 1); }
      if (T >= 9 && i < 2) t.add(n, OR, T - 7, i);
      for (int s = 0; s < 4; ++s) if (EP0 + (4 * s + 3) / UPP == n) t.add(n, OS, s, 0);
      if (n == EP0 + 15 / UPP + 1) t.add(n, OE, 0, 0);
      if (n == PA) t.add(n, OP, 0, 0);
      if (n >= PC && nh < 2 * PD) { t.add(n, OH, nh >> 1, nh & 1); ++nh; }
    }
    for (; nh < 2 * PD; ++nh) t.add(128, OH, nh >> 1, nh & 1);
    for (int s = 4; s < 8; ++s) t.add(129 + s, OS, s, 0);
  } else {
    for (; nh < 2 * PD; ++nh) t.add(-1, OH, nh >> 1, nh & 1);
  }
  int na = 0;
  for (int k = 0; k < 28 - NT; ++k)
    for (int g = 0; g < 4; ++g) {
      const int pos = G2P + 4 * k + g;
      const bool ring = (g == 1 || g == 2) && k + 10 < 28;
      if (ring) t.add(pos, OR, k + 10, g - 1);
      if (k == KP && g == 0) t.add(pos, OP, 1, 0);
      const int lim = mn(k < KC ? 28 : 56, 2 * (k + PD) + (g == 3 ? 2 : 1));
      int room = ring ? 0 : 1;
      if (k >= KC && nh + 2 < lim) ++room;
      for (; room > 0 && nh < lim; --room, ++nh) t.add(pos, OH, nh >> 1, nh & 1);
      if (k >= KA && na < 6 && room > 0) { t.add(pos, OA, 0, na); ++na; }
    }
  return t;
}
constexpr OpTab GT[2] = {optab<false>(), optab<true>()};
constexpr int g1wait(int T) {
  const int pos = T ? 8 * T - LAM : 0;
  return mn(GT[0].younger(OW, T, 1, pos), T < 8 ? GT[0].younger(OX, T, 3, pos) : 63);
}
template <bool AUX> constexpr int g2wait(int k) {
  const int pos = G2P + 4 * k, kl = k == 27 - NT ? 27 : k + 1;
  return mn(GT[AUX].younger(OH, kl, 1, pos), (AUX && kl < 10) ? 63 : GT[AUX].younger(OR, kl, 1, pos));
}
template <bool AUX> constexpr bool sched_ok() {
  for (int k = 0; k < 28 - NT; ++k) if (g2wait<AUX>(k) < 0) return false;
  for (int T = 0; T < 16; ++T) if (g1wait(T) < 0) return false;
  return GT[AUX].m0_ok() && GT[AUX].younger(OA, 0, 5, G2P + 4 * KB) >= 0 && GT[AUX].younger(OP, 1, 0, G2P + 4 * KC) >= 0;
}
static_assert(sched_ok<false>() && sched_ok<true>() && GT[0].at(OW, 15, 1) < GT[0].at(OS, 0, 0), "schedule");
static_assert(KP < KC && KC + 1 < 14 && KB < 28 - NT && KB <= 25 && KB >= 19 && NT >= 2 && NT <= 4, "schedule");

template <int N> DI void wait_pin(v4u& a, v4u& b) { asm volatile("s_waitcnt vmcnt(%2)" : "+v"(a), "+v"(b) : "n"(N) : "memory"); }
template <int N> DI void wait_pin(v2u& a) { asm volatile("s_waitcnt vmcnt(%1)" : "+v"(a) : "n"(N) : "memory"); }
template <int N> DI void wait_pin(u32& a) { asm volatile("s_waitcnt vmcnt(%1)" : "+v"(a) : "n"(N) : "memory"); }
template <int N> DI void wait_pin(v4u (&a)[6]) {
  asm volatile("s_waitcnt vmcnt(%6)" : "+v"(a[0]), "+v"(a[1]), "+v"(a[2]), "+v"(a[3]), "+v"(a[4]), "+v"(a[5]) : "n"(N) : "memory");
}
DI void pin(v4u& a, v4u& b) { asm volatile("" : "+v"(a), "+v"(b)); }
template <int I> using IC = std::integral_constant<int, I>;

template <bool AUX>
DI void gemm_role(int b, LAS char* sm, const u16* x, const u16* w1, const u16* b1, const u16* w2, const u16* b2, const u16* gw,
                  u16* out, char* ws) {
  constexpr const OpTab& TB = GT[AUX];
  const int lane = threadIdx.x & 63, wave = __builtin_amdgcn_readfirstlane(threadIdx.x >> 6);
  STAMP_DECL;
  STAMP(0);
  const int xcd = b & 7, r = b >> 3;
  const rsrc_t rws = mkrsrc(ws, WS_END), rx = mkrsrc(x, 2048 * 512 * 2), rw2 = mkrsrc(w2, 1792 * 512 * 2), rb2 = mkrsrc(b2, 512 * 2);
  const rsrc_t rw1 = mkrsrc(w1, 512 * 1792 * 2);
  v2u ep = ld64_sc1(0, rws);
  const int g = lane >> 4, q = (lane >> 2) & 3, p = lane & 3, l31 = lane & 31, hh = lane >> 5, l3 = lane >> 3, l7 = lane & 7;
  const u32 mreg = (u32)(uintptr_t)sm + wave * REG;
  const int t2 = 4 * xcd + (r >> 3), c2 = r & 7;
  const int tau = 4 * (xcd >> 1) + r / 7, j = 7 * (xcd & 1) + r % 7, rot = (r % 7) + 2 * (r / 7);

  const u32 wch = ((l7 ^ ((g & 1) << 2)) << 4);
  const u32 vx0 = (u32)(128 * tau + 32 * wave + l3) * 1024 + ((l7 ^ g) << 4), vx1 = (vx0 ^ 64) + 7168;
  const u32 vw0 = (u32)(16 * wave + l3) * 3584 + j * 256 + wch, vw1 = vw0 + 8 * 3584 - 1024;
  const u32 vv0 = (u32)(896 * (wave >> 1) + 32 * (wave & 1) + l3) * 1024 + c2 * 128 + wch, vv1 = vv0 + 15360;
  const u32 vh = WS_H + (u32)((4 * tau + wave) * 112 + 8 * j) * 1024 + lane * 16;
  const u32 vhh = WS_H + (u32)(2 * t2 * 112 + 56 * (wave >> 1) + 2 * (wave & 1)) * 1024 + lane * 16;
  const u32 vp = vhh + (u32)((l3 & 1) * 112 + 8 * min(l7, 6) + 1) * 1024;
  const u32 va = WS_A + (u32)t2 * 131072 + (u32)(32 * c2 + 4 * l3) * 512 + (u32)(16 * wave + 2 * l7) * 8;
  const u32 vg = WS_G + (u32)(64 * t2 + 16 * wave + 2 * l3) * 8;
  u32 aoff[2];
#pragma unroll
  for (int mi = 0; mi < 2; ++mi) aoff[mi] = (8 * (g >> 1) + q) * 128 + (((4 * mi + 2 * (p & 1) + (g & 1)) ^ ((q >> 1) << 2)) << 4) + (p >> 1) * 8;

  u64 tag;
  u32 sg, pf;
  v4u hd[8], hb[28][2], av[6];
  auto hload = [&](auto K, auto S, auto AGAIN) {
    constexpr int k = decltype(K)::value, s = decltype(S)::value, ks = hstep(k);
    if constexpr (decltype(AGAIN)::value) reld128_sc1<(ks & 3) * 1024>(hb[k][s], vhh, rws, (ks >> 2) * 4096 + s * 114688);
    else hb[k][s] = ld128_sc1<(ks & 3) * 1024>(vhh, rws, (ks >> 2) * 4096 + s * 114688);
  };
  auto aload = [&](auto S) {
    constexpr int s = decltype(S)::value;
    if constexpr (s < 4) av[s] = ld128_sc1<s * 512>(va, rws, 0);
    else if constexpr (s == 4) av[4] = ld128_sc1<0>(vg, rws, 0);
    else av[5] = ld128((u32)(64 * c2 + 8 * l7) * 2, rb2);
  };
  auto issue = [&](auto I) {
    constexpr Op o = TB.op[decltype(I)::value];
    constexpr int lo = (o.sub & 1) * 1024;
    if constexpr (o.pos < 0 && TB.lds(decltype(I)::value) >= 0 && lo == 0) { set_m0<TB.lds(decltype(I)::value)>(mreg); asm volatile("s_nop 0"); }
    if constexpr (o.kind == OX) dma1<lo>(lo ? vx1 : vx0, rx, (u32)((o.idx + rot) & 7) * 128 + (o.sub >> 1) * 16384);
    if constexpr (o.kind == OW) dma1<lo>(lo ? vw1 : vw0, rw1, (u32)((o.idx + rot) & 7) * (64 * 3584) + (o.idx >> 3) * 128);
    if constexpr (o.kind == OR) dma1<lo>(lo ? vv1 : vv0, rw2, w2row(o.idx) * 1024);
    if constexpr (o.kind == OS) st128_sc1<(o.idx & 3) * 1024>(hd[o.idx], vh, rws, (o.idx >> 2) * 4096);
    if constexpr (o.kind == OE) flag_store(WS_EFLAG + (u32)(4 * b + wave) * FSP, rws, tag);
    if constexpr (o.kind == OP) pf = ld32_sc1<0>(vp, rws, o.idx * 4096);
    if constexpr (o.kind == OH) hload(IC<o.idx>{}, IC<o.sub>{}, IC<0>{});
    if constexpr (o.kind == OA) aload(IC<o.sub>{});
    if constexpr (o.pos < 0 && o.kind == OW && o.sub == 1 && o.idx < 2) asm volatile("s_barrier");
  };
  auto issue_at = [&](auto N) {
    static_for<TB.from(decltype(N)::value), TB.from(decltype(N)::value + 1)>(issue);
  };
  auto m0_for = [&](auto N) { if constexpr (TB.m0_at(decltype(N)::value) >= 0) set_m0<TB.m0_at(decltype(N)::value)>(mreg); };
  auto gate = [&](int ph) {
    if (__builtin_expect(any((pf ^ sg) & SGN), 0)) {
#pragma unroll 1
      for (int it = 0; it < SPIN_MAX; ++it) {
        __builtin_amdgcn_s_sleep(1);
        pf = ld32_sc1<0>(vp, rws, ph * 4096);
        wait_pin<0>(pf);
        if (!any((pf ^ sg) & SGN)) break;
      }
    }
  };

  if constexpr (!AUX) {
    const rsrc_t rb1 = mkrsrc(b1, 1792 * 2);
    const u32 vb = (u32)(128 * j + 32 * hh + 16 * ((l31 >> 2) & 1) + 4 * (l31 >> 3) + p) * 2;
    u32 bq0 = ld16<0>(vb, rb1), bq1 = ld16<128>(vb, rb1);
    f16v acc[4];
    issue_at(IC<-1>{});
    __builtin_amdgcn_s_setprio(PRG1);

    LAS char* pa[2][2]; LAS char* pb[4];
#pragma unroll
    for (int mi = 0; mi < 2; ++mi) { pa[mi][0] = sm + aoff[mi]; pa[mi][1] = sm + aoff[mi] + 2 * REG; }
#pragma unroll
    for (int u = 0; u < 4; ++u) pb[u] = sm + wave * REG + l31 * 128 + (((2 * u + hh) ^ ((l31 >> 1) & 7)) << 4);

    asm volatile("s_waitcnt vmcnt(20)" : "+v"(ep), "+v"(bq0), "+v"(bq1) :: "memory");
    tag = epoch_tag(ep);
    sg = sign_of(tag, l31);
    {
      const bf8 o0 = __builtin_bit_cast(bf8, (v4u){hh ? 0u : 0x3f80u, 0, 0, 0}), o1 = __builtin_bit_cast(bf8, (v4u){hh ? 0x3f80u : 0u, 0, 0, 0});
      const bf8 a0 = __builtin_bit_cast(bf8, (v4u){bq0, 0, 0, 0}), a1 = __builtin_bit_cast(bf8, (v4u){bq1, 0, 0, 0});
      acc[0] = mfma32(a0, o0, (f16v)(0.f)); acc[1] = mfma32(a0, o1, (f16v)(0.f));
      acc[2] = mfma32(a1, o0, (f16v)(0.f)); acc[3] = mfma32(a1, o1, (f16v)(0.f));
    }
    constexpr auto ms = [](int n) constexpr { const int T = n >> 3, v = n & 7; return (T & 7) == 7 ? (v & 3) : (v >> 1); };
    constexpr auto mt = [](int n) constexpr { const int T = n >> 3, v = n & 7; return (T & 7) == 7 ? (v >> 2) : (v & 1); };
    bf8 af[128], xf[32];
    auto rda = [&](auto N) {
      constexpr int n = decltype(N)::value, u = ms(n);
      LAS char* a = pa[mt(n)][u >> 1] + (u & 1) * REG + wblk(n >> 3) * 2048;
      af[n] = lds_tr2(a, a + 512);
    };
    auto rdx = [&](auto S) { constexpr int s = decltype(S)::value; xf[s] = lds_rd128(pb[s & 3] + ((s >> 2) % 3) * 4096); };
    auto unit = [&](auto U) {
      constexpr int un = decltype(U)::value, tl = un >> 3, e = (un & 7) * 2;
      const float v0 = relu(acc[tl][e]), v1 = relu(acc[tl][e + 1]);
      hd[un >> 2][un & 3] = (pk2(opaque(v0 * v0), opaque(v1 * v1)) & ~SGN) | sg;
    };
    STAMP(1);
    fence_sched();
    wait_vm_bar<g1wait(0)>();
    fence_sched();
    STAMP(2); STAMPC(60);
    static_for<0, 4>(rdx);
    static_for<0, LAM>(rda);
    static_for<0, 128>([&](auto N) {
      constexpr int n = decltype(N)::value, T = (n + LAM) / 8, i = (n + LAM) % 8;
      fence_sched();
      if constexpr (i == 0) { if constexpr (T < 16) wait_vm_bar<g1wait(T)>(); else lds_bar(); }
      if constexpr (i == 0) { STAMPQ(15 + T); }
      if constexpr (n == PC) { wait_pin<TB.younger(OP, 0, 0, n)>(pf); gate(0); }
      m0_for(N);
      fence_sched();
      acc[2 * (n >> 6) + mt(n)] = mfma32(af[n], xf[(4 * (n >> 3) + ms(n)) & 31], acc[2 * (n >> 6) + mt(n)]);
      fence_sched();
      if constexpr (n + LAM < 128) rda(IC<n + LAM>{});
      if constexpr (T >= 1 && T < 8 && i < 4) rdx(IC<4 * T + i>{});
      if constexpr (n >= EP0 && n < EP0 + 16 / UPP) static_for<UPP * (n - EP0), UPP * (n - EP0 + 1)>(unit);
      if constexpr (n >= 64 + EP0) static_for<16 + UPP * (n - 64 - EP0), 16 + UPP * (n - 64 - EP0 + 1)>(unit);
      fence_sched();
      issue_at(N);
    });
    fence_sched();
    STAMP(5); STAMPC(61);
    issue_at(IC<128>{});
    static_for<16, 32>([&](auto U) {
      constexpr int un = decltype(U)::value;
      if constexpr (64 + EP0 + (un & 15) / UPP > 127) unit(U);
      if constexpr ((un & 3) == 3) { fence_sched(); issue_at(IC<129 + (un >> 2)>{}); fence_sched(); }
    });
    STAMP(6);
  } else {
    const int tg = xcd * 4 + (r - 28);
    const v4u gv = *(const v4u*)(gw + lane * 8);
    const u16* xr = x + (size_t)(tg * 64 + wave * 16) * 512 + lane * 8;
    float s[16];
#pragma unroll
    for (int i = 0; i < 16; ++i) {
      const v4u xv = *(const v4u*)(xr + i * 512);
      s[i] = 0.f;
#pragma unroll
      for (int k = 0; k < 4; ++k) s[i] = dot2(xv[k], gv[k], s[i]);
    }
#pragma unroll
    for (int i = 0; i < 8; ++i) {
      const v2u t = __builtin_amdgcn_permlane32_swap(__float_as_uint(s[i]), __float_as_uint(s[i + 8]), false, false);
      s[i] = opaque(__uint_as_float(t.x) + __uint_as_float(t.y));
    }
#pragma unroll
    for (int i = 0; i < 4; ++i) {
      const v2u t = __builtin_amdgcn_permlane16_swap(__float_as_uint(s[i]), __float_as_uint(s[i + 4]), false, false);
      float v = __uint_as_float(t.x) + __uint_as_float(t.y);
      v += __int_as_float(__builtin_amdgcn_update_dpp(0, __float_as_int(v), 0x128, 0xf, 0xf, false));
      v += __int_as_float(__builtin_amdgcn_update_dpp(0, __float_as_int(v), 0x124, 0xf, 0xf, false));
      v += __int_as_float(__builtin_amdgcn_update_dpp(0, __float_as_int(v), 0x122, 0xf, 0xf, false));
      v += __int_as_float(__builtin_amdgcn_update_dpp(0, __float_as_int(v), 0x121, 0xf, 0xf, false));
      s[i] = opaque(v);
    }
    const float m01 = opaque(p & 1 ? s[1] : s[0]), m23 = opaque(p & 1 ? s[3] : s[2]), mine = p & 2 ? m23 : m01;
    const float sl = bflo(pk2(mine, 0.f));
    const float gt = bflo(pk2(1.f / (1.f + __expf(-sl)), 0.f));
    fence_sched();
    asm volatile("s_waitcnt vmcnt(0)\n\ts_barrier" : "+v"(ep) :: "memory");
    tag = epoch_tag(ep);
    sg = sign_of(tag, l31);
    if (q == 0) st64_sc1((v2u){__float_as_uint(gt), (u32)(tag >> 32)}, WS_G + (u32)(tg * 64 + wave * 16 + 4 * g + p) * 8, rws);
    STAMP(1);
    static_for<0, 10>([&](auto I) { constexpr int i = decltype(I)::value; dma2(mreg + rblk(i) * 2048, vv0, vv1, rw2, w2row(i) * 1024); });
    STAMP(2);
    pf = ld32_sc1<0>(vp, rws, 0);
    wait_pin<0>(pf);
    gate(0);
    STAMP(4);
    issue_at(IC<-1>{});
  }
  __builtin_amdgcn_s_setprio(PRG2);

  const u32 tg32 = (u32)(tag >> 32);
  LAS char* pv[2] = {sm + wave * REG + aoff[0], sm + wave * REG + aoff[1]};
  auto vfrag = [&](int k, int mi) { LAS char* a = pv[mi] + rblk(k) * 2048; return lds_tr2(a, a + 512); };
  auto fresh = [&](auto K0, auto K1, u32 m) {
    constexpr int k0 = decltype(K0)::value, k1 = decltype(K1)::value, ke = mn(k1 | 1, 27);
    if (__builtin_expect(any(m & SGN), 0)) {
#pragma unroll 1
      for (int it = 0; it < SPIN_MAX; ++it) {
        static_for<k0, ke + 1>([&](auto K) { hload(K, IC<0>{}, IC<1>{}); hload(K, IC<1>{}, IC<1>{}); });
        wait_vm<0>();
        u32 z = 0;
        static_for<k0, ke + 1>([&](auto K) { constexpr int k = decltype(K)::value; pin(hb[k][0], hb[k][1]); if constexpr (k <= k1) z |= strip(hb[k][0], sg) | strip(hb[k][1], sg); });
        if (!any(z & SGN)) break;
        __builtin_amdgcn_s_sleep(1);
      }
    }
  };
  auto abad = [&]() {
    u32 m = 0;
#pragma unroll
    for (int i = A0; i < 5; ++i) m |= (av[i].y ^ tg32) | (av[i].w ^ tg32);
    return any(m);
  };
  LAS char* ap = sm + wave * REG + 9 * 2048;
  v4u a4[2];
  float ga[2][8], gs[2];
  auto aput = [&]() {
    *(LAS v4u*)(ap + l7 * 128 + ((l3 ^ l7) << 4)) = (v4u){av[0].x, av[1].x, av[2].x, av[3].x};
    *(LAS v4u*)(ap + l7 * 128 + ((l3 ^ l7) << 4) + 1024) = (v4u){av[0].z, av[1].z, av[2].z, av[3].z};
    a4[0] = *(LAS v4u*)(ap + l3 * 128 + ((l3 ^ l7) << 4));
    a4[1] = *(LAS v4u*)(ap + l3 * 128 + ((l3 ^ l7) << 4) + 1024);
  };
  auto amix = [&](int e, int k) {
    const float gt = __uint_as_float(e ? av[4].z : av[4].x), gn = opaque(1.f - gt);
    ga[e][2 * k] = opaque(__builtin_fmaf(gt, bflo(a4[e][k]), opaque(gn * bflo(av[5][k]))));
    ga[e][2 * k + 1] = opaque(__builtin_fmaf(gt, bfhi(a4[e][k]), opaque(gn * bfhi(av[5][k]))));
    if (k == 0) gs[e] = gn;
  };
  f16v o2[2][2];
  bf8 wb[28][2];
  bool late = false;
  fence_sched();
  wait_pin<mn(TB.younger(OH, 0, 1, G2P), AUX ? 63 : TB.younger(OR, 0, 1, G2P))>(hb[0][0], hb[0][1]);
  wb[0][0] = vfrag(0, 0); wb[0][1] = vfrag(0, 1);
  fresh(IC<0>{}, IC<0>{}, strip(hb[0][0], sg) | strip(hb[0][1], sg));
  STAMP(7);
  static_for<0, 28 - NT>([&](auto K) {
    constexpr int k = decltype(K)::value, kl = k == 27 - NT ? 27 : k + 1;
    fence_sched();
    wait_vm<g2wait<AUX>(k)>();
    static_for<k + 1, kl + 1>([&](auto J) { constexpr int jn = decltype(J)::value; pin(hb[jn][0], hb[jn][1]); });
    if constexpr (k == KC) { wait_pin<TB.younger(OP, 1, 0, G2P + 4 * k)>(pf); gate(1); }
    if constexpr (k == KB) { wait_pin<TB.younger(OA, 0, 5, G2P + 4 * k)>(av); }
    STAMPQ(32 + k);
    u32 m = 0;
    static_for<0, 4>([&](auto G) {
      constexpr int gi = decltype(G)::value, mi = gi & 1, ni = gi >> 1;
      m0_for(IC<G2P + 4 * k + gi>{});
      fence_sched();
      if constexpr (k == 0) o2[mi][ni] = mfma32(wb[k][mi], __builtin_bit_cast(bf8, hb[k][ni]), (f16v)(0.f));
      else o2[mi][ni] = mfma32(wb[k][mi], __builtin_bit_cast(bf8, hb[k][ni]), o2[mi][ni]);
      fence_sched();
      if constexpr (gi < 2) static_for<k + 1, kl + 1>([&](auto J) {
        constexpr int jn = decltype(J)::value;
        wb[jn][gi] = vfrag(jn, gi);
        m |= strip(hb[jn][gi], sg);
      });
      if constexpr (k == KB && gi == 0) aput();
      if constexpr (k == KB && gi == 2) late = abad();
      if constexpr (k > KB && k <= KB + 2) amix(k - KB - 1, gi);
      fence_sched();
      issue_at(IC<G2P + 4 * k + gi>{});
      if constexpr (gi == 2) fresh(IC<k + 1>{}, IC<kl>{}, m);
    });
  });
  fence_sched();
  LAS char* pw = sm + wave * REG + l31 * 256;
  const int kw = (4 * hh) ^ (((l31 >> 1) & 3) | ((l31 & 1) << 2));
  auto pput = [&](int tl, int i) {
    const f16v& a = o2[tl >> 1][tl & 1];
    *(LAS f4*)(pw + ((kw ^ i) << 4) + (tl >> 1) * 128 + (tl & 1) * 8192) = (f4){a[4 * i], a[4 * i + 1], a[4 * i + 2], a[4 * i + 3]};
  };
  static_for<0, 4 * NT>([&](auto N) {
    constexpr int n = decltype(N)::value, tl = n / NT, v = n % NT, k = 28 - NT + v;
    o2[tl >> 1][tl & 1] = mfma32(wb[k][tl >> 1], __builtin_bit_cast(bf8, hb[k][tl & 1]), o2[tl >> 1][tl & 1]);
    fence_sched();
    if constexpr (constexpr int u = 4 * (27 - NT - KB) + n; u < 8) amix(u >> 2, u & 3);
    if constexpr (tl >= 1 && v == NT - 1) { pput(tl - 1, 0); pput(tl - 1, 1); pput(tl - 1, 2); pput(tl - 1, 3); }
    fence_sched();
  });
  STAMP(10); STAMPC(62);
  if (__builtin_expect(late, 0)) {
#pragma unroll 1
    for (int it = 0; it < SPIN_MAX; ++it) {
      static_for<0, 5>(aload);
      wait_pin<0>(av);
      if (!abad()) break;
      __builtin_amdgcn_s_sleep(1);
    }
    aput();
#pragma unroll
    for (int i = 0; i < 8; ++i) amix(i >> 2, i & 3);
  }
#pragma unroll
  for (int i = 0; i < 4; ++i) pput(3, i);
  lds_bar();
  STAMP(11);
  {
    LAS char* pr = sm + (16 * wave + 2 * l3) * 256;
    const int kr = (2 * l7) ^ (l3 & 3);
#pragma unroll
    for (int e = 0; e < 2; ++e) {
      v4u d;
#pragma unroll
      for (int cc = 0; cc < 2; ++cc) {
        LAS char* a = pr + e * 256 + ((kr ^ cc ^ (4 * e)) << 4);
        const f4 s = (*(LAS f4*)(a) + *(LAS f4*)(a + REG)) + (*(LAS f4*)(a + 2 * REG) + *(LAS f4*)(a + 3 * REG));
#pragma unroll
        for (int k = 0; k < 2; ++k)
          d[2 * cc + k] = pk2(opaque(__builtin_fmaf(gs[e], s[2 * k], ga[e][4 * cc + 2 * k])), opaque(__builtin_fmaf(gs[e], s[2 * k + 1], ga[e][4 * cc + 2 * k + 1])));
      }
      *(v4u*)(out + (size_t)(64 * t2 + 16 * wave + 2 * l3 + e) * 512 + 64 * c2 + 8 * l7) = d;
    }
  }
  STAMP(12);
  STAMP_OUT;
}

struct cf { float x, y; };
DI cf cmul(cf a, cf m) { return cf{opaque(__builtin_fmaf(a.x, m.x, -opaque(a.y * m.y))), opaque(__builtin_fmaf(a.x, m.y, opaque(a.y * m.x)))}; }
DI cf cexp_s(float lr, float rv, float m) {
  const float e = __builtin_amdgcn_exp2f(lr * m), f = __builtin_amdgcn_fractf(rv * m);
  return cf{opaque(e * __builtin_amdgcn_cosf(f)), opaque(e * __builtin_amdgcn_sinf(f))};
}

constexpr u32 TROW = 544, TCH = 63 * TROW, XT = 69632;
constexpr u32 KCH = 9472, KOO = 4656, UA0 = 18944, UCH = 8768, UBO = 4224, EX = 40960;

DI void touch(u32& d, u32 vo, rsrc_t rs) { asm volatile("buffer_load_dword %0, %1, %2, 0 offen" : "+v"(d) : "v"(vo), "s"(rs) : "memory"); }
DI void touch_ws(u32& d, u32 vo, rsrc_t rs) { asm volatile("buffer_load_dword %0, %1, %2, 0 offen " PFQ : "+v"(d) : "v"(vo), "s"(rs) : "memory"); }
constexpr int NPF = !!(PF & 1) + !!(PF & 2) + 2 * !!(PF & 4) + !!(PF & 8) + !!(PF & 16);

DI void conv_role(int ci, LAS char* sm, const u16* x, const u16* are, const u16* aim, const u16* bre, const u16* bim,
                  const u16* cre, const u16* cim, const u16* logdt, const u16* dskip, const u16* w1, const u16* w2, char* ws) {
  const int tid = threadIdx.x, lane = tid & 63, wave = __builtin_amdgcn_readfirstlane(tid >> 6);
  STAMP_DECL;
  STAMP(0);
  const rsrc_t rws = mkrsrc(ws, WS_END), rx = mkrsrc(x, 2048 * 512 * 2);
  v2u ep = ld64_sc1(0, rws);
  __builtin_amdgcn_s_setprio(PRC0);
  const u32 smb = (u32)(uintptr_t)sm;
  const int pr = (ci & 7) * 32 + (ci >> 3), cg = (ci >> 3) & 7, tt = 4 * (ci & 7) + (ci >> 6);
  const int g = lane >> 4, q = (lane >> 2) & 3, p = lane & 3, n = lane & 15;
  const int ch = wave >> 1, half = wave & 1;

  {
    const u32 v0 = (u32)(64 * tt + 16 * wave + (lane >> 3)) * 1024 + cg * 128 + (((lane & 7) ^ ((g & 1) << 1)) << 4);
    const u32 v1 = (v0 ^ 64) + 7168;
    dma2(smb + XT + wave * 2048, v0, v1, rx, 0);
  }
  const int pi = pr * 256 + tid;
  u32 r_are = are[pi], r_aim = aim[pi], r_bre = bre[pi], r_bim = bim[pi], r_cre = cre[pi], r_cim = cim[pi];
  u32 r_ldt = logdt[2 * pr + ch];
  u32 r_dsk = *(const u32*)(dskip + 2 * pr);
  fence_sched();
  asm volatile("s_waitcnt vmcnt(0)" : "+v"(r_are), "+v"(r_aim), "+v"(r_bre), "+v"(r_bim), "+v"(r_cre), "+v"(r_cim), "+v"(r_ldt), "+v"(r_dsk), "+v"(ep) :: "memory");
  const u64 tag = epoch_tag(ep);
  const u32 tg32 = (u32)(tag >> 32);
  fence_sched();
  STAMP(1);
  {
    LAS char* tb = sm + XT + wave * 2048 + (8 * (p & 1) + q) * 128 + (p >> 1) * 8;
    const int sw = (q & 2) | ((p & 1) << 2);
    const u32 vo = WS_UT + (u32)((16 * cg + 2 * g + (q >> 1)) * 256 + 8 * tt + 2 * wave + (q & 1)) * 64 + p * 16;
    const bf8 d0 = lds_tr2(tb + ((g ^ sw) << 4), tb + ((g ^ sw) << 4) + 512);
    const bf8 d1 = lds_tr2(tb + (((4 + g) ^ sw) << 4), tb + (((4 + g) ^ sw) << 4) + 512);
    st128_sc1<0>(__builtin_bit_cast(v4u, d0), vo, rws, 0);
    st128_sc1<0>(__builtin_bit_cast(v4u, d1), vo, rws, 8 * 256 * 64);
  }
  fence_sched();
  u32 tch = 0;
  {
    const u32 wv = 4 * ci + wave, big = 1u << 30;
    if (PF & 1) touch(tch, lane < 7 ? (wv >> 1) * 3584 + ((wv & 1) * 7 + lane) * 256 + 128 : big, mkrsrc(w1, 512 * 1792 * 2));
    if (PF & 2) touch(tch, lane < 14 ? (wv * 14 + lane) * 128 : big, mkrsrc(w2, 512 * 1792 * 2));
    if (PF & 4) {
      const u32 wx = 4 * (ci >> 3) + wave;
      touch(tch, (wx * 112 + lane) * 128, mkrsrc(w2, 512 * 1792 * 2));
      touch(tch, lane < 48 ? (wx * 112 + 64 + lane) * 128 : big, mkrsrc(w2, 512 * 1792 * 2));
    }
    if (PF & 8) touch_ws(tch, lane < 56 ? WS_H + (wv * 56 + lane) * 128 : big, rws);
    if (PF & 16) touch_ws(tch, lane < 32 ? WS_A + (wv * 32 + lane) * 128 : big, rws);
  }
  fence_sched();

  __builtin_amdgcn_s_setprio(PRC1);
  const u32 uf = WS_UFLAG + (u32)(cg * 128 + tt * 4 + wave) * FSP;
  {
    const float dt = bflo(pk2(expf(bflo(r_ldt)), 0.f));
    const float lr = dt * bflo(r_are) * 1.44269504088896341f, rv = dt * bflo(r_aim) * 0.15915494309189535f;
    const cf db = {opaque(dt * bflo(r_bre)), opaque(-dt * bflo(r_bim))};
    const cf cb = cmul(cf{bflo(r_cre), -bflo(r_cim)}, db);
    const cf e16 = cexp_s(lr, rv, 16.f), e1 = cexp_s(lr, -rv, 1.f), e256 = cexp_s(lr, -rv, 256.f);
    const cf e2 = cexp_s(lr, -rv, 2.f), e4 = cexp_s(lr, -rv, 4.f), e8 = cexp_s(lr, -rv, 8.f);
    LAS char* tw = sm + ch * TCH + (tid & 127) * 4;
    auto put = [&](int row, cf v) { *(volatile LAS u32*)(tw + row * TROW) = pk2(v.x, v.y); };
    cf a = e16, t0 = cb, tb[8], v[8];
    put(0, cf{1.f, 0.f}); put(16, t0);
    tb[0] = cb;
#pragma unroll
    for (int i = 1; i < 16; ++i) {
      if (i == UFP) { wait_vm<NPF>(); flag_store(uf, rws, tag); }
      put(i, a); if (i < 15) a = cmul(a, e16);
      t0 = cmul(t0, e1); put(16 + i, t0);
      if (i < 8) { tb[i] = cmul(tb[i - 1], e256); v[i] = tb[i]; }
#pragma unroll
      for (int tl = 1; tl < 8; ++tl) {
        const int base = tl == 1 ? 32 : (tl < 4 ? 41 + 5 * (tl - 2) : 51 + 3 * (tl - 4));
        const int cnt = tl == 1 ? 9 : (tl < 4 ? 5 : 3), k = i - tl;
        if (k >= 0 && k < cnt) {
          if (k > 0) v[tl] = cmul(v[tl], tl == 1 ? e2 : (tl < 4 ? e4 : e8));
          put(base + k, v[tl]);
        }
      }
    }
  }
  fence_sched();
  if (UFP >= 16) { wait_vm<NPF>(); flag_store(uf, rws, tag); }
  const u32 vf = WS_UFLAG + (u32)((ci & 7) * 128 + wave * 32 + (lane & 31)) * FSP;
  v2u pf = ld64_sc1(vf, rws);
  STAMP(2);
  lds_bar();

  f4 kk[4];
  v4u uv[2];
  {
    LAS char* tb = sm + ch * TCH + g * 16;
    const int m2 = (n + ((n >> 1) & 1)) >> 1, m4 = (n + 1 + ((n >> 2) & 1)) >> 2, m8 = (n + 3 + ((n >> 3) & 1)) >> 3;
    LAS char* pr_[4] = {tb + (half ? 51 + m8 : 16 + n) * TROW, tb + (half ? 54 + m8 : 32 + m2) * TROW,
                        tb + (half ? 57 + m8 : 41 + m4) * TROW, tb + (half ? 60 + m8 : 46 + m4) * TROW};
    LAS char* pe = tb + n * TROW;
#pragma unroll
    for (int i = 0; i < 4; ++i) kk[i] = (f4)(0.f);
#pragma unroll
    for (int ks = 0; ks < 8; ++ks) {
      if (ks == 4) {
        fence_sched();
        wait_pin<0>(pf);
        asm volatile("" :: "v"(tch));
        if (__builtin_expect(flag_bad(pf, tag), 0)) flag_wait(vf, rws, tag);
        STAMP(3);
        const u32 vo = WS_UT + (u32)((pr >> 1) * 256 + 64 * wave + (lane >> 1)) * 64 + ((pr & 1) * 2 + (lane & 1)) * 16;
        uv[0] = ld128_sc1<0>(vo, rws, 0); uv[1] = ld128_sc1<2048>(vo, rws, 0);
        fence_sched();
      }
      const bf8 e = lds_rd128(pe + ks * 64);
#pragma unroll
      for (int i = 0; i < 4; ++i) kk[i] = mfma16(lds_rd128(pr_[i] + ks * 64), e, kk[i]);
    }
  }
  fence_sched();
  STAMP(4);
  lds_bar();
  {
    LAS char* kb = sm + ch * KCH;
#pragma unroll
    for (int i = 0; i < 4; ++i) {
      const int l0 = 256 * (4 * half + i) + 16 * n + 4 * g;
      const float k0 = opaque(kk[i][0]), k1 = opaque(kk[i][1]), k2 = opaque(kk[i][2]), k3 = opaque(kk[i][3]);
      const u32 a = pk2(k3, k2), bq = pk2(k1, k0), c = pk2(k2, k1);
      *(LAS v2u*)(kb + KOO + 2 * (2044 - l0)) = (v2u){a, bq};
      *(LAS u16*)(kb + 2 * (2051 - l0)) = (u16)a;
      *(LAS u32*)(kb + 2 * (2052 - l0)) = c;
      *(LAS u16*)(kb + 2 * (2054 - l0)) = (u16)(bq >> 16);
    }
    const v4u z = {0, 0, 0, 0};
    if (lane < 29) *(LAS v4u*)(kb + (half ? KOO + 4096 : 4112) + lane * 16) = z;
    else if (lane < 33) *(LAS v4u*)(sm + UA0 + ch * UCH + (half ? UBO : 0) + (lane - 29) * 16 - (half ? 0 : 16)) = z;
    else if (lane == 33 && !half) *(LAS u16*)(kb + 2 * 2055) = 0;
    else if (lane == 34 && !half) *(LAS v3u*)(sm + UA0 + ch * UCH + 48) = (v3u){0, 0, 0};
  }
  asm volatile("s_waitcnt vmcnt(0)" : "+v"(uv[0]), "+v"(uv[1]) :: "memory");
#pragma unroll
  for (int k = 0; k < 2; ++k) {
    LAS char* ub = sm + UA0 + (lane & 1) * UCH + 64 + (64 * wave + 32 * k + (lane >> 1)) * 16;
    *(LAS v4u*)(ub + UBO) = uv[k];
    *(LAS u32*)(ub - 4) = uv[k].x;
    *(LAS v3u*)(ub) = (v3u){uv[k].y, uv[k].z, uv[k].w};
  }
  lds_bar();
  STAMP(5);
  __builtin_amdgcn_s_setprio(PRCV);

  f4 y[8];
  {
    LAS char* pk = sm + ch * KCH + ((n & 1) ? KOO + 4032 : 4048) - 64 * (n >> 1) + 16 * g - 3584 + 256 * half;
    LAS char* pu = sm + UA0 + ch * UCH + (n < 8 ? 8 * n : UBO + 8 * (n - 8) + 8) + 16 * g + 256 * half;
    bf8 kf[4][8], uf[4][8];
    auto rd = [&](int rr, int i) {
      kf[rr][i] = lds_rd128(pk + 512 * (7 - i) + 64 * rr);
      const v2u lo = *(volatile LAS v2u*)(pu + 512 * i + 64 * rr), hi = *(volatile LAS v2u*)(pu + 512 * i + 64 * rr + 8);
      uf[rr][i] = __builtin_bit_cast(bf8, (v4u){lo.x, lo.y, hi.x, hi.y});
    };
#pragma unroll
    for (int i = 0; i < 8; ++i) { rd(0, i); fence_sched(); }
    static_for<0, 144>([&](auto N) {
      constexpr int nn = decltype(N)::value, rr = nn / 36, v = nn % 36;
      constexpr int s = v < 1 ? 0 : v < 3 ? 1 : v < 6 ? 2 : v < 10 ? 3 : v < 15 ? 4 : v < 21 ? 5 : v < 28 ? 6 : 7, m = v - s * (s + 1) / 2;
      if constexpr (rr == 0 && m == 0) y[s] = mfma16(kf[rr][s - m], uf[rr][m], (f4)(0.f));
      else y[s] = mfma16(kf[rr][s - m], uf[rr][m], y[s]);
      fence_sched();
      if constexpr (rr < 3 && v % 4 == 2 && v / 4 < 8) rd(rr + 1, v / 4);
      fence_sched();
    });
  }
  STAMP(6);
#pragma unroll
  for (int a = 0; a < 8; ++a) *(LAS f4*)(sm + EX + (wave * 8 + a) * 1024 + lane * 16) = y[a];
  lds_bar();
  {
    const float d0 = bflo(r_dsk), d1 = bfhi(r_dsk);
    const int rn = n < 8 ? 2 * n : 2 * (n - 8) + 1;
#pragma unroll
    for (int i = 0; i < 2; ++i) {
      const int al = 2 * wave + i;
      LAS char* ex = sm + EX + al * 1024 + lane * 16;
      const f4 y0 = *(LAS f4*)(ex) + *(LAS f4*)(ex + 8192), y1 = *(LAS f4*)(ex + 16384) + *(LAS f4*)(ex + 24576);
      LAS char* up = sm + UA0 + UBO + 64 + (256 * al + 2 * rn + 64 * g) * 2;
      const u32 u00 = *(LAS u32*)(up), u01 = *(LAS u32*)(up + 64), u10 = *(LAS u32*)(up + UCH), u11 = *(LAS u32*)(up + UCH + 64);
      auto mix = [&](float y, float u, float d) { return opaque(__builtin_fmaf(u, d, opaque(y))); };
      const u32 x0 = pk2(mix(y0[0], bflo(u00), d0), mix(y1[0], bflo(u10), d1)), x1 = pk2(mix(y0[1], bfhi(u00), d0), mix(y1[1], bfhi(u10), d1));
      const u32 z0 = pk2(mix(y0[2], bflo(u01), d0), mix(y1[2], bflo(u11), d1)), z1 = pk2(mix(y0[3], bfhi(u01), d0), mix(y1[3], bfhi(u11), d1));
      const u32 vo = WS_A + (u32)(4 * al + g) * 131072 + pr * 512 + rn * 16;
      st128_sc1<0>((v4u){x0, tg32, x1, tg32}, vo, rws, 0);
      st128_sc1<256>((v4u){z0, tg32, z1, tg32}, vo, rws, 0);
    }
  }
  STAMP(7);
  if (ci == 0) {
    granule_wait(WS_A + (u32)tid * 512, rws, tg32);
    if (tid < 224) flag_wait(WS_EFLAG + (u32)tid * 4 * FSP, rws, tag);
    else granule_wait(WS_G + (u32)(tid - 224) * 512, rws, tg32);
    lds_bar();
    if (wave == 0) flag_store(0, rws, tag);
  }
  STAMP_OUT;
}

__global__ __launch_bounds__(256, 2) void fused_kernel(const u16* __restrict__ x, const u16* __restrict__ are, const u16* __restrict__ aim,
                                                       const u16* __restrict__ bre, const u16* __restrict__ bim, const u16* __restrict__ cre,
                                                       const u16* __restrict__ cim, const u16* __restrict__ logdt, const u16* __restrict__ dskip,
                                                       const u16* __restrict__ w1, const u16* __restrict__ b1, const u16* __restrict__ w2,
                                                       const u16* __restrict__ b2, const u16* __restrict__ gw, u16* __restrict__ out, char* ws, const char* pb) {
  __shared__ __attribute__((aligned(256))) char smem[81920];
  LAS char* sm = (LAS char*)smem;
  asm volatile("" :: "s"(x), "s"(are), "s"(aim), "s"(bre), "s"(bim), "s"(cre), "s"(cim), "s"(logdt), "s"(dskip), "s"(w1), "s"(b1),
               "s"(w2), "s"(b2), "s"(gw), "s"(out), "s"(ws));
  const int b = blockIdx.x;
  const bool mlp = (b < 256) == (GFIRST != 0);
  if (mlp && (b & 255) < 224) gemm_role<false>(b & 255, sm, x, w1, b1, w2, b2, gw, out, ws);
  else if (mlp) gemm_role<true>(b & 255, sm, x, w1, b1, w2, b2, gw, out, ws);
  else conv_role(b & 255, sm, x, are, aim, bre, bim, cre, cim, logdt, dskip, w1, w2, ws);
  if (b < 256) {
    const rsrc_t rp = mkrsrc(pb, 32u << 20);
    const u32 vo = (b * 4 + (threadIdx.x >> 6)) * 32768 + (threadIdx.x & 63) * 16;
#pragma unroll
    for (int i = 0; i < 32; ++i) st128_sc1<0>((v4u){vo, vo, vo, vo}, vo, rp, i * 1024);
    wait_vm<0>();
  }
}

extern "C" void kernel_launch(void* const* d_in, const int* in_sizes, int n_in, void* d_out, int out_size, void* d_ws, size_t ws_size,
                              hipStream_t stream) {
  const u16* const* in = (const u16* const*)d_in;
  fused_kernel<<<512, 256, 0, stream>>>(in[0], in[1], in[2], in[3], in[4], in[5], in[6], in[7], in[8], in[10], in[11], in[12], in[13],
                                        in[14], (u16*)d_out, (char*)d_ws + (((ws_size - WS_END) / 16 * WSPOS) & ~(size_t)0xfffff),
    PROBE == 1 ? (const char*)d_in[9] : PROBE == 3 ? (const char*)d_ws + ws_size - (2u << 20) : (const char*)d_ws + (128u << 20));
}
```

```cpp
#include <hip/hip_runtime.h>
#include <stdint.h>
#include <type_traits>
#pragma clang diagnostic ignored "-Winline-asm"

typedef unsigned short u16;
typedef unsigned int u32;
typedef unsigned long long u64;
typedef __attribute__((ext_vector_type(2))) u32 v2u;
typedef __attribute__((ext_vector_type(3))) u32 v3u;
typedef __attribute__((ext_vector_type(4))) u32 v4u;
typedef __attribute__((ext_vector_type(2))) float f2;
typedef __attribute__((ext_vector_type(4))) float f4;
typedef __attribute__((ext_vector_type(16))) float f16v;
typedef __attribute__((ext_vector_type(2))) __bf16 bf2;
typedef __attribute__((ext_vector_type(8))) __bf16 bf8;
typedef __attribute__((ext_vector_type(4))) short s4;
typedef __amdgpu_buffer_rsrc_t rsrc_t;
#define DI __device__ __forceinline__
#define LAS __attribute__((address_space(3)))
#define CAS __attribute__((address_space(4)))

#define GFIRST 1
#define LAM 4
#define UPP 2
#define PA 110
#define PC 127
#define PD 8
#define KP 6
#define KC 9
#define KA 19
#define KB 25
#define NT 2
#define W2PF 0
#define PRG1 2
#define PRG2 1
#define PRC0 3
#define PRC1 1
#define PRCV 2
#define UFP 8
#define PF 35
#define KO 0
#define HOLD -1
#define HOLDPT(n) do { if (HOLD == n) { for (int i_ = 0; i_ < 4000 && __builtin_amdgcn_s_memrealtime() - tst < 900; ++i_) __builtin_amdgcn_s_sleep(8); } } while (0)
#define BURST 1
#define PRCB 3
#define CDLY 4
#define ESLP 40
#define C2X 0
#define PFP 0
#define ROT2 1
#define TK0 4
#define TKS 20
#define WSPOS 0
#define PFQ "sc1"

constexpr u32 FSP = 256;
constexpr u32 WS_G = 4096, WS_EFLAG = 65536, WS_UFLAG = WS_EFLAG + 1024 * FSP;
constexpr u32 WS_UT = 1u << 20, WS_A = 3u << 20, WS_H = 7u << 20, WS_END = 15u << 20;
constexpr int SPIN_MAX = 1 << 16;
constexpr int A0 = 0;

constexpr int E0 = 0;

#define STAMP(i)
#define STAMPQ(i)
#define STAMPC(i)
#define STAMP_DECL
#define STAMP_OUT

DI rsrc_t mkrsrc(const void* p, u32 bytes) { return __builtin_amdgcn_make_buffer_rsrc((void*)p, (short)0, (int)bytes, 0x00020000); }
DI float bflo(u32 v) { return __uint_as_float(v << 16); }
DI float bfhi(u32 v) { return __uint_as_float(v & 0xffff0000u); }
DI u32 pk2(float a, float b) { bf2 r = __builtin_convertvector((f2){a, b}, bf2); return __builtin_bit_cast(u32, r); }
DI float dot2(u32 a, u32 b, float c) { return __builtin_amdgcn_fdot2_f32_bf16(__builtin_bit_cast(bf2, a), __builtin_bit_cast(bf2, b), c, false); }
DI float relu(float v) { return __int_as_float(max(__float_as_int(v), 0)); }
DI void fence_sched() { __builtin_amdgcn_sched_barrier(0); }

template <int N> DI void wait_vm() { asm volatile("s_waitcnt vmcnt(%0)" :: "n"(N) : "memory"); }
template <int N> DI void wait_vm_bar() { asm volatile("s_waitcnt vmcnt(%0) lgkmcnt(0)\n\ts_barrier" :: "n"(N) : "memory"); }
DI void lds_bar() { asm volatile("s_waitcnt lgkmcnt(0)\n\ts_barrier" ::: "memory"); }
DI void wait_lgkm() { asm volatile("s_waitcnt lgkmcnt(0)" ::: "memory"); }

DI void dma2(u32 m0v, u32 v0, u32 v1, rsrc_t rs, u32 so) {
  asm volatile("s_mov_b32 m0, %0\n\ts_nop 0\n\t"
               "buffer_load_dwordx4 %1, %3, %4 offen lds\n\t"
               "buffer_load_dwordx4 %2, %3, %4 offen offset:1024 lds"
               :: "s"(m0v), "v"(v0), "v"(v1), "s"(rs), "s"(so) : "m0", "memory");
}
template <u32 off> DI void set_m0(u32 base) { asm volatile("s_add_u32 m0, %0, %1" :: "s"(base), "n"(off) : "m0", "scc"); }
template <int OFF> DI void dma1(u32 v, rsrc_t rs, u32 so) {
  asm volatile("buffer_load_dwordx4 %0, %1, %2 offen offset:%3 lds" :: "v"(v), "s"(rs), "s"(so), "n"(OFF) : "memory");
}
template <int OFF> DI void reld128_sc1(v4u& r, u32 vo, rsrc_t rs, u32 so) {
  asm volatile("buffer_load_dwordx4 %0, %1, %2, %3 offen offset:%4 sc1" : "+v"(r) : "v"(vo), "s"(rs), "s"(so), "n"(OFF) : "memory");
}
template <int OFF> DI u32 ld32_sc1(u32 vo, rsrc_t rs, u32 so) {
  u32 r; asm volatile("buffer_load_dword %0, %1, %2, %3 offen offset:%4 sc1" : "=&v"(r) : "v"(vo), "s"(rs), "s"(so), "n"(OFF) : "memory"); return r;
}
template <int OFF> DI v4u ld128_sc1(u32 vo, rsrc_t rs, u32 so) {
  v4u r; asm volatile("buffer_load_dwordx4 %0, %1, %2, %3 offen offset:%4 sc1" : "=&v"(r) : "v"(vo), "s"(rs), "s"(so), "n"(OFF) : "memory"); return r;
}
DI v4u ld128(u32 vo, rsrc_t rs) {
  v4u r; asm volatile("buffer_load_dwordx4 %0, %1, %2, 0 offen" : "=&v"(r) : "v"(vo), "s"(rs) : "memory"); return r;
}
DI v2u ld64_sc1(u32 vo, rsrc_t rs) {
  v2u r; asm volatile("buffer_load_dwordx2 %0, %1, %2, 0 offen sc1" : "=&v"(r) : "v"(vo), "s"(rs) : "memory"); return r;
}
template <int OFF> DI u32 ld16(u32 vo, rsrc_t rs) {
  u32 r; asm volatile("buffer_load_ushort %0, %1, %2, 0 offen offset:%3" : "=&v"(r) : "v"(vo), "s"(rs), "n"(OFF) : "memory"); return r;
}
template <int OFF> DI void st128_sc1(v4u d, u32 vo, rsrc_t rs, u32 so) {
  asm volatile("buffer_store_dwordx4 %0, %1, %2, %3 offen offset:%4 sc1\n\ts_nop 1" :: "v"(d), "v"(vo), "s"(rs), "s"(so), "n"(OFF) : "memory");
}
DI void st64_sc1(v2u d, u32 vo, rsrc_t rs) {
  asm volatile("buffer_store_dwordx2 %0, %1, %2, 0 offen sc1" :: "v"(d), "v"(vo), "s"(rs) : "memory");
}
DI void flag_store(u32 off, rsrc_t rs, u64 tag) {
  const v2u d = {(u32)tag, (u32)(tag >> 32)};
  asm volatile("s_mov_b64 exec, 1\n\tbuffer_store_dwordx2 %0, off, %1, %2 sc1\n\ts_mov_b64 exec, -1" :: "v"(d), "s"(rs), "s"(off) : "memory");
}
DI u64 epoch_tag(v2u e) {
  const u64 v = (u64)(u32)__builtin_amdgcn_readfirstlane(e.x) | ((u64)(u32)__builtin_amdgcn_readfirstlane(e.y) << 32);
  return ((v * 0x9E3779B97F4A7C15ull + 0x632BE59BD9B4E019ull) & ~0xffffull) | (~v & 0xffffull);
}
constexpr u32 SGN = 0x80008000u;
DI u32 sign_of(u64 tag, int t) { return ((((u32)tag >> (t >> 1)) ^ t) & 1) ? SGN : 0u; }
DI u32 strip(v4u& d, u32 s) { d ^= s; return (d.x | d.y) | (d.z | d.w); }
DI bool any(u32 m) { return __builtin_amdgcn_ballot_w64(m != 0) != 0; }
DI bool flag_bad(v2u v, u64 tag) { return __builtin_amdgcn_ballot_w64((v.x != (u32)tag) | (v.y != (u32)(tag >> 32))) != 0; }
template <int SL = 1> DI void flag_wait(u32 vo, rsrc_t rs, u64 tag) {
#pragma unroll 1
  for (int it = 0; it < SPIN_MAX; ++it) {
    v2u v; asm volatile("buffer_load_dwordx2 %0, %1, %2, 0 offen sc1\n\ts_waitcnt vmcnt(0)" : "=&v"(v) : "v"(vo), "s"(rs) : "memory");
    if (!flag_bad(v, tag)) break;
    __builtin_amdgcn_s_sleep(SL);
  }
}

template <int SL = 1> DI void granule_wait(u32 vo, rsrc_t rs, u32 tg) {
#pragma unroll 1
  for (int it = 0; it < SPIN_MAX; ++it) {
    v2u v; asm volatile("buffer_load_dwordx2 %0, %1, %2, 0 offen sc1\n\ts_waitcnt vmcnt(0)" : "=&v"(v) : "v"(vo), "s"(rs) : "memory");
    if (__builtin_amdgcn_ballot_w64(v.y != tg) == 0) break;
    __builtin_amdgcn_s_sleep(SL);
  }
}
DI float opaque(float v) { asm("" : "+v"(v)); return v; }

template <int OFF> DI void lds_put_tid(u32 v) { asm volatile("ds_write_addtid_b32 %0 offset:%1" :: "v"(v), "n"(OFF) : "memory"); }
DI bf8 lds_rd128(LAS char* p) { return *(LAS bf8*)p; }
DI bf8 lds_tr2(LAS char* p0, LAS char* p1) {
  s4 a = __builtin_amdgcn_ds_read_tr16_b64_v4i16((LAS s4*)p0);
  s4 b = __builtin_amdgcn_ds_read_tr16_b64_v4i16((LAS s4*)p1);
  v2u x = __builtin_bit_cast(v2u, a), y = __builtin_bit_cast(v2u, b);
  return __builtin_bit_cast(bf8, (v4u){x.x, x.y, y.x, y.y});
}
DI f16v mfma32(bf8 a, bf8 b, f16v c) { return __builtin_amdgcn_mfma_f32_32x32x16_bf16(a, b, c, 0, 0, 0); }
DI f4 mfma16(bf8 a, bf8 b, f4 c) { return __builtin_amdgcn_mfma_f32_16x16x32_bf16(a, b, c, 0, 0, 0); }

template <int I, int N, class F> DI void static_for(F&& f) {
  if constexpr (I < N) { f(std::integral_constant<int, I>{}); static_for<I + 1, N>(f); }
}

constexpr u32 REG = 20480;
constexpr int wblk(int t) { constexpr int b[16] = {6, 7, 8, 9, 6, 7, 8, 9, 6, 7, 4, 5, 8, 0, 1, 9}; return b[t]; }
constexpr int rblk(int k) { constexpr int b[10] = {2, 3, 6, 7, 4, 5, 8, 0, 1, 9}; return b[k % 10]; }
constexpr u32 w2row(int k) { return 64 * (k / 14) + 8 * (k & 1); }

enum { OX, OW, OR, OS, OE, OH, OP, OA, OF };
struct Op { int pos, kind, idx, sub; };
struct OpTab {
  Op op[256]; int n;
  constexpr void add(int pos, int kind, int idx, int sub) { op[n] = Op{pos, kind, idx, sub}; ++n; }
  constexpr int at(int kind, int idx, int sub) const {
    for (int i = 0; i < n; ++i) if (op[i].kind == kind && op[i].idx == idx && op[i].sub == sub) return i;
    return -1;
  }
  constexpr int from(int pos) const { int i = 0; while (i < n && op[i].pos < pos) ++i; return i; }
  constexpr int younger(int kind, int idx, int sub, int pos) const {
    int c = 0, a = at(kind, idx, sub);
    if (a < 0 || op[a].pos >= pos) return -1000;
    for (int i = a + 1; i < n; ++i) if (op[i].pos < pos) ++c;
    return c > 63 ? 63 : c;
  }
  constexpr int lds(int i) const {
    const Op o = op[i];
    return o.kind == OX ? (o.idx % 3) * 4096 + (o.sub >> 1) * 2048 : o.kind == OW ? wblk(o.idx) * 2048 : o.kind == OR ? rblk(o.idx) * 2048 : -1;
  }
  constexpr int m0_at(int pos) const {
    int cur = -1;
    for (int i = 0; i < n; ++i) if (lds(i) >= 0) { if (op[i].pos == pos) return lds(i) != cur ? lds(i) : -1; cur = lds(i); }
    return -1;
  }
  constexpr bool m0_ok() const {
    for (int i = 0; i < n; ++i) for (int j = i + 1; j < n; ++j)
      if (op[i].pos == op[j].pos && op[i].pos >= 0 && lds(i) >= 0 && lds(j) >= 0 && lds(i) != lds(j)) return false;
    return true;
  }
};
constexpr int EP0 = 61, G2P = 200;
constexpr int mn(int a, int b) { return a < b ? a : b; }
template <bool AUX> constexpr OpTab optab() {
  OpTab t{};
  int nh = 0;
  if (!AUX) {
    for (int c = 0; c < 3; ++c) { for (int s = 0; s < 4; ++s) t.add(-1, OX, c, s); t.add(-1, OW, c, 0); t.add(-1, OW, c, 1); }
    t.add(-1, OW, 3, 0); t.add(-1, OW, 3, 1);
    for (int n = 0; n < 128; ++n) {
      const int T = (n + LAM) / 8, i = (n + LAM) % 8;
      if (T >= 1 && T <= 5) { if (i < 4) t.add(n, OX, T + 2, i); else if (i < 6) t.add(n, OW, T + 3, i - 4); }
      if (T >= 6 && T <= 7 && i < 6) t.add(n, OW, 9 + 3 * (T - 6) + i / 2, i & 1);
      if (T == 8 && i < 6) { if (i < 2) t.add(n, OW, 15, i); else t.add(n, OR, i / 2 - 1, i & 1); }
      if (T >= 9 && i < 2) t.add(n, OR, T - 7, i);
      for (int s = 0; s < 4; ++s) if (EP0 + (4 * s + 3) / UPP == n) t.add(n, OS, s, 0);
      if (n == EP0 + 15 / UPP + 1) t.add(n, OE, 0, 0);
      if (PFP && (n == PFP || n == PFP + 1)) t.add(n, OF, 0, n - PFP);
      if (n == PA) t.add(n, OP, 0, 0);
      if (n >= PC && nh < 2 * PD) { t.add(n, OH, nh >> 1, nh & 1); ++nh; }
    }
    for (; nh < 2 * PD; ++nh) t.add(128, OH, nh >> 1, nh & 1);
    for (int s = 4; s < 8; ++s) t.add(129 + s, OS, s, 0);
  } else {
    for (; nh < 2 * PD; ++nh) t.add(-1, OH, nh >> 1, nh & 1);
  }
  int na = 0;
  for (int k = 0; k < 28 - NT; ++k)
    for (int g = 0; g < 4; ++g) {
      const int pos = G2P + 4 * k + g;
      const bool ring = (g == 1 || g == 2) && k + 10 < 28;
      if (ring) t.add(pos, OR, k + 10, g - 1);
      if (k == KP && g == 0) t.add(pos, OP, 1, 0);
      const int lim = mn(k < KC ? 28 : 56, 2 * (k + PD) + (g == 3 ? 2 : 1));
      int room = ring ? 0 : 1;
      if (k >= KC && nh + 2 < lim) ++room;
      for (; room > 0 && nh < lim; --room, ++nh) t.add(pos, OH, nh >> 1, nh & 1);
      if (k >= KA && na < 6 && room > 0) { t.add(pos, OA, 0, na); ++na; }
    }
  return t;
}
constexpr OpTab GT[2] = {optab<false>(), optab<true>()};
constexpr int g1wait(int T) {
  const int pos = T ? 8 * T - LAM : 0;
  return mn(GT[0].younger(OW, T, 1, pos), T < 8 ? GT[0].younger(OX, T, 3, pos) : 63);
}
template <bool AUX> constexpr int g2wait(int k) {
  const int pos = G2P + 4 * k, kl = k == 27 - NT ? 27 : k + 1;
  return mn(GT[AUX].younger(OH, kl, 1, pos), (AUX && kl < 10) ? 63 : GT[AUX].younger(OR, kl, 1, pos));
}
template <bool AUX> constexpr bool sched_ok() {
  for (int k = 0; k < 28 - NT; ++k) if (g2wait<AUX>(k) < 0) return false;
  for (int T = 0; T < 16; ++T) if (g1wait(T) < 0) return false;
  return GT[AUX].m0_ok() && GT[AUX].younger(OA, 0, 5, G2P + 4 * KB) >= 0 && GT[AUX].younger(OP, 1, 0, G2P + 4 * KC) >= 0;
}
static_assert(sched_ok<false>() && sched_ok<true>() && GT[0].at(OW, 15, 1) < GT[0].at(OS, 0, 0), "schedule");
static_assert(KP < KC && KC + 1 < 14 && KB < 28 - NT && KB <= 25 && KB >= 19 && NT >= 2 && NT <= 4, "schedule");

template <int N> DI void wait_pin(v4u& a, v4u& b) { asm volatile("s_waitcnt vmcnt(%2)" : "+v"(a), "+v"(b) : "n"(N) : "memory"); }
template <int N> DI void wait_pin(v2u& a) { asm volatile("s_waitcnt vmcnt(%1)" : "+v"(a) : "n"(N) : "memory"); }
template <int N> DI void wait_pin(u32& a) { asm volatile("s_waitcnt vmcnt(%1)" : "+v"(a) : "n"(N) : "memory"); }
template <int N> DI void wait_pin(v4u (&a)[6]) {
  asm volatile("s_waitcnt vmcnt(%6)" : "+v"(a[0]), "+v"(a[1]), "+v"(a[2]), "+v"(a[3]), "+v"(a[4]), "+v"(a[5]) : "n"(N) : "memory");
}
DI void pin(v4u& a, v4u& b) { asm volatile("" : "+v"(a), "+v"(b)); }
template <int I> using IC = std::integral_constant<int, I>;

DI void touch(u32& d, u32 vo, rsrc_t rs, u32 so = 0) { asm volatile("buffer_load_dword %0, %1, %2, %3 offen" : "+v"(d) : "v"(vo), "s"(rs), "s"(so) : "memory"); }
DI void touch_ws(u32& d, u32 vo, rsrc_t rs) { asm volatile("buffer_load_dword %0, %1, %2, 0 offen " PFQ : "+v"(d) : "v"(vo), "s"(rs) : "memory"); }
DI u32 late_row(u32 i) {
  const u32 hf = i >= 576, m = i - 576 * hf;
  return 896 * hf + (m < 128 ? 640 + 2 * (m & 64) + (m & 63) : 2 * ((m - 128) & ~63u) + 64 + (m & 63));
}

template <bool AUX>
DI void gemm_role(int b, LAS char* sm, const u16* x, const u16* w1, const u16* b1, const u16* w2, const u16* b2, const u16* gw,
                  u16* out, char* ws) {
  constexpr const OpTab& TB = GT[AUX];
  const int lane = threadIdx.x & 63, wave = __builtin_amdgcn_readfirstlane(threadIdx.x >> 6);
  STAMP_DECL;
  STAMP(0);
  const int xcd = b & 7, r = b >> 3;
  const rsrc_t rws = mkrsrc(ws, WS_END), rx = mkrsrc(x, 2048 * 512 * 2), rw2 = mkrsrc(w2, 1792 * 512 * 2), rb2 = mkrsrc(b2, 512 * 2);
  const rsrc_t rw1 = mkrsrc(w1, 512 * 1792 * 2);
  v2u ep = ld64_sc1(0, rws);
  const int g = lane >> 4, q = (lane >> 2) & 3, p = lane & 3, l31 = lane & 31, hh = lane >> 5, l3 = lane >> 3, l7 = lane & 7;
  const u32 mreg = (u32)(uintptr_t)sm + wave * REG;
  const int t2 = 4 * xcd + (r >> 3), c2 = (r & 7) ^ C2X;
  const int tau = 4 * (xcd >> 1) + r / 7, j = 7 * (xcd & 1) + r % 7, rot = (r % 7) + 2 * (r / 7);

  const u32 wch = ((l7 ^ ((g & 1) << 2)) << 4);
  const u32 vx0 = (u32)(128 * tau + 32 * wave + l3) * 1024 + ((l7 ^ g) << 4), vx1 = (vx0 ^ 64) + 7168;
  const u32 vw0 = (u32)(16 * wave + l3) * 3584 + j * 256 + wch, vw1 = vw0 + 8 * 3584 - 1024;
  const u32 vv0 = (u32)(896 * (wave >> 1) + 32 * (wave & 1) + l3) * 1024 + c2 * 128 + wch, vv1 = vv0 + 15360;
  const u32 vh = WS_H + (u32)((4 * tau + wave) * 112 + 8 * j) * 1024 + lane * 16;
  const u32 vhh = WS_H + (u32)(2 * t2 * 112 + 56 * (wave >> 1) + 2 * (wave & 1)) * 1024 + lane * 16;
  const u32 vp = vhh + (u32)((l3 & 1) * 112 + 8 * min(l7, 6) + 1) * 1024;
  const u32 va = WS_A + (u32)t2 * 131072 + (u32)(32 * c2 + 4 * l3) * 512 + (u32)(16 * wave + 2 * l7) * 8;
  const u32 vg = WS_G + (u32)(64 * t2 + 16 * wave + 2 * l3) * 8;
  const u32 vf0 = late_row(32 * r + 8 * wave + l3) * 1024 + l7 * 128, vf1 = lane < 8 ? late_row(1024 + 4 * r + wave) * 1024 + lane * 128 : 1u << 30;
  u32 tch = 0;
  const int rot2 = ROT2 ? (c2 + 2 * (r >> 3)) % 7 : 0;
  auto prod = [&](int k) { const int q_ = ((k % 14) >> 1) + rot2; return (u32)(q_ >= 7 ? q_ - 7 : q_); };
  u32 aoff[2];
#pragma unroll
  for (int mi = 0; mi < 2; ++mi) aoff[mi] = (8 * (g >> 1) + q) * 128 + (((4 * mi + 2 * (p & 1) + (g & 1)) ^ ((q >> 1) << 2)) << 4) + (p >> 1) * 8;

  u64 tag;
  u32 sg, pf;
  v4u hd[8], hb[28][2], av[6];
  auto hload = [&](auto K, auto S, auto AGAIN) {
    constexpr int k = decltype(K)::value, s = decltype(S)::value;
    const u32 so = prod(k) * 8192 + (k / 14) * 4096 + s * 114688;
    if constexpr (decltype(AGAIN)::value) reld128_sc1<(k & 1) * 1024>(hb[k][s], vhh, rws, so);
    else hb[k][s] = ld128_sc1<(k & 1) * 1024>(vhh, rws, so);
  };
  auto aload = [&](auto S) {
    constexpr int s = decltype(S)::value;
    if constexpr (s < 4) av[s] = ld128_sc1<s * 512>(va, rws, 0);
    else if constexpr (s == 4) av[4] = ld128_sc1<0>(vg, rws, 0);
    else av[5] = ld128((u32)(64 * c2 + 8 * l7) * 2, rb2);
  };
  auto issue = [&](auto I) {
    constexpr Op o = TB.op[decltype(I)::value];
    constexpr int lo = (o.sub & 1) * 1024;
    if constexpr (o.pos < 0 && TB.lds(decltype(I)::value) >= 0 && lo == 0) { set_m0<TB.lds(decltype(I)::value)>(mreg); asm volatile("s_nop 0"); }
    if constexpr (o.kind == OX) dma1<lo>(lo ? vx1 : vx0, rx, (u32)((o.idx + rot) & 7) * 128 + (o.sub >> 1) * 16384);
    if constexpr (o.kind == OW) dma1<lo>(lo ? vw1 : vw0, rw1, (u32)((o.idx + rot) & 7) * (64 * 3584) + (o.idx >> 3) * 128);
    if constexpr (o.kind == OR) dma1<lo>(lo ? vv1 : vv0, rw2, prod(o.idx) * 131072 + w2row(o.idx) * 1024);
    if constexpr (o.kind == OS) st128_sc1<(o.idx & 3) * 1024>(hd[o.idx], vh, rws, (o.idx >> 2) * 4096);
    if constexpr (o.kind == OE) flag_store(WS_EFLAG + (u32)b * 8, rws, tag);
    if constexpr (o.kind == OF) touch(tch, o.sub ? vf1 : vf0, rw2);
    if constexpr (o.kind == OP) pf = ld32_sc1<0>(vp, rws, o.idx * 4096);
    if constexpr (o.kind == OH) hload(IC<o.idx>{}, IC<o.sub>{}, IC<0>{});
    if constexpr (o.kind == OA) aload(IC<o.sub>{});
    if constexpr (o.pos < 0 && o.kind == OW && o.sub == 1 && o.idx < 2) asm volatile("s_barrier");
  };
  auto issue_at = [&](auto N) {
    static_for<TB.from(decltype(N)::value), TB.from(decltype(N)::value + 1)>(issue);
  };
  auto m0_for = [&](auto N) { if constexpr (TB.m0_at(decltype(N)::value) >= 0) set_m0<TB.m0_at(decltype(N)::value)>(mreg); };
  auto gate = [&](int ph) {
    if (__builtin_expect(any((pf ^ sg) & SGN), 0)) {
#pragma unroll 1
      for (int it = 0; it < SPIN_MAX; ++it) {
        __builtin_amdgcn_s_sleep(1);
        pf = ld32_sc1<0>(vp, rws, ph * 4096);
        wait_pin<0>(pf);
        if (!any((pf ^ sg) & SGN)) break;
      }
    }
  };

  if constexpr (!AUX) {
    const rsrc_t rb1 = mkrsrc(b1, 1792 * 2);
    const u32 vb = (u32)(128 * j + 32 * hh + 16 * ((l31 >> 2) & 1) + 4 * (l31 >> 3) + p) * 2;
    u32 bq0 = ld16<0>(vb, rb1), bq1 = ld16<128>(vb, rb1);
    f16v acc[4];
    issue_at(IC<-1>{});
    __builtin_amdgcn_s_setprio(PRG1);

    LAS char* pa[2][2]; LAS char* pb[4];
#pragma unroll
    for (int mi = 0; mi < 2; ++mi) { pa[mi][0] = sm + aoff[mi]; pa[mi][1] = sm + aoff[mi] + 2 * REG; }
#pragma unroll
    for (int u = 0; u < 4; ++u) pb[u] = sm + wave * REG + l31 * 128 + (((2 * u + hh) ^ ((l31 >> 1) & 7)) << 4);

    asm volatile("s_waitcnt vmcnt(20)" : "+v"(ep), "+v"(bq0), "+v"(bq1) :: "memory");
    tag = epoch_tag(ep);
    sg = sign_of(tag, l31);
    {
      const bf8 o0 = __builtin_bit_cast(bf8, (v4u){hh ? 0u : 0x3f80u, 0, 0, 0}), o1 = __builtin_bit_cast(bf8, (v4u){hh ? 0x3f80u : 0u, 0, 0, 0});
      const bf8 a0 = __builtin_bit_cast(bf8, (v4u){bq0, 0, 0, 0}), a1 = __builtin_bit_cast(bf8, (v4u){bq1, 0, 0, 0});
      acc[0] = mfma32(a0, o0, (f16v)(0.f)); acc[1] = mfma32(a0, o1, (f16v)(0.f));
      acc[2] = mfma32(a1, o0, (f16v)(0.f)); acc[3] = mfma32(a1, o1, (f16v)(0.f));
    }
    constexpr auto ms = [](int n) constexpr { const int T = n >> 3, v = n & 7; return (T & 7) == 7 ? (v & 3) : (v >> 1); };
    constexpr auto mt = [](int n) constexpr { const int T = n >> 3, v = n & 7; return (T & 7) == 7 ? (v >> 2) : (v & 1); };
    bf8 af[128], xf[32];
    auto rda = [&](auto N) {
      constexpr int n = decltype(N)::value, u = ms(n);
      LAS char* a = pa[mt(n)][u >> 1] + (u & 1) * REG + wblk(n >> 3) * 2048;
      af[n] = lds_tr2(a, a + 512);
    };
    auto rdx = [&](auto S) { constexpr int s = decltype(S)::value; xf[s] = lds_rd128(pb[s & 3] + ((s >> 2) % 3) * 4096); };
    auto unit = [&](auto U) {
      constexpr int un = decltype(U)::value, tl = un >> 3, e = (un & 7) * 2;
      const float v0 = relu(acc[tl][e]), v1 = relu(acc[tl][e + 1]);
      hd[un >> 2][un & 3] = (pk2(opaque(v0 * v0), opaque(v1 * v1)) & ~SGN) | sg;
    };
    STAMP(1);
    fence_sched();
    wait_vm_bar<g1wait(0)>();
    fence_sched();
    STAMP(2); STAMPC(60);
    static_for<0, 4>(rdx);
    static_for<0, LAM>(rda);
    static_for<0, 128>([&](auto N) {
      constexpr int n = decltype(N)::value, T = (n + LAM) / 8, i = (n + LAM) % 8;
      fence_sched();
      if constexpr (i == 0) { if constexpr (T < 16) wait_vm_bar<g1wait(T)>(); else lds_bar(); }
      if constexpr (i == 0) { STAMPQ(15 + T); }
      if constexpr (n == PC) { wait_pin<TB.younger(OP, 0, 0, n)>(pf); asm volatile("" :: "v"(tch)); gate(0); }
      m0_for(N);
      fence_sched();
      acc[2 * (n >> 6) + mt(n)] = mfma32(af[n], xf[(4 * (n >> 3) + ms(n)) & 31], acc[2 * (n >> 6) + mt(n)]);
      fence_sched();
      if constexpr (n + LAM < 128) rda(IC<n + LAM>{});
      if constexpr (T >= 1 && T < 8 && i < 4) rdx(IC<4 * T + i>{});
      if constexpr (n >= EP0 && n < EP0 + 16 / UPP) static_for<UPP * (n - EP0), UPP * (n - EP0 + 1)>(unit);
      if constexpr (n >= 64 + EP0) static_for<16 + UPP * (n - 64 - EP0), 16 + UPP * (n - 64 - EP0 + 1)>(unit);
      fence_sched();
      issue_at(N);
    });
    fence_sched();
    STAMP(5); STAMPC(61);
    issue_at(IC<128>{});
    static_for<16, 32>([&](auto U) {
      constexpr int un = decltype(U)::value;
      if constexpr (64 + EP0 + (un & 15) / UPP > 127) unit(U);
      if constexpr ((un & 3) == 3) { fence_sched(); issue_at(IC<129 + (un >> 2)>{}); fence_sched(); }
    });
    STAMP(6);
  } else {
    const int tg = xcd * 4 + (r - 28);
    const v4u gv = *(const v4u*)(gw + lane * 8);
    const u16* xr = x + (size_t)(tg * 64 + wave * 16) * 512 + lane * 8;
    float s[16];
#pragma unroll
    for (int i = 0; i < 16; ++i) {
      const v4u xv = *(const v4u*)(xr + i * 512);
      s[i] = 0.f;
#pragma unroll
      for (int k = 0; k < 4; ++k) s[i] = dot2(xv[k], gv[k], s[i]);
    }
#pragma unroll
    for (int i = 0; i < 8; ++i) {
      const v2u t = __builtin_amdgcn_permlane32_swap(__float_as_uint(s[i]), __float_as_uint(s[i + 8]), false, false);
      s[i] = opaque(__uint_as_float(t.x) + __uint_as_float(t.y));
    }
#pragma unroll
    for (int i = 0; i < 4; ++i) {
      const v2u t = __builtin_amdgcn_permlane16_swap(__float_as_uint(s[i]), __float_as_uint(s[i + 4]), false, false);
      float v = __uint_as_float(t.x) + __uint_as_float(t.y);
      v += __int_as_float(__builtin_amdgcn_update_dpp(0, __float_as_int(v), 0x128, 0xf, 0xf, false));
      v += __int_as_float(__builtin_amdgcn_update_dpp(0, __float_as_int(v), 0x124, 0xf, 0xf, false));
      v += __int_as_float(__builtin_amdgcn_update_dpp(0, __float_as_int(v), 0x122, 0xf, 0xf, false));
      v += __int_as_float(__builtin_amdgcn_update_dpp(0, __float_as_int(v), 0x121, 0xf, 0xf, false));
      s[i] = opaque(v);
    }
    const float m01 = opaque(p & 1 ? s[1] : s[0]), m23 = opaque(p & 1 ? s[3] : s[2]), mine = p & 2 ? m23 : m01;
    const float sl = bflo(pk2(mine, 0.f));
    const float gt = bflo(pk2(1.f / (1.f + __expf(-sl)), 0.f));
    fence_sched();
    asm volatile("s_waitcnt vmcnt(0)\n\ts_barrier" : "+v"(ep) :: "memory");
    tag = epoch_tag(ep);
    sg = sign_of(tag, l31);
    if (q == 0) st64_sc1((v2u){__float_as_uint(gt), (u32)(tag >> 32)}, WS_G + (u32)(tg * 64 + wave * 16 + 4 * g + p) * 8, rws);
    STAMP(1);
    static_for<0, 10>([&](auto I) { constexpr int i = decltype(I)::value; dma2(mreg + rblk(i) * 2048, vv0, vv1, rw2, prod(i) * 131072 + w2row(i) * 1024); });
    flag_store(WS_EFLAG + (u32)b * 8, rws, tag);
    if (PFP) { touch(tch, vf0, rw2); touch(tch, vf1, rw2); }
    STAMP(2);
    pf = ld32_sc1<0>(vp, rws, 0);
    wait_pin<0>(pf);
    asm volatile("" :: "v"(tch));
    gate(0);
    STAMP(4);
    issue_at(IC<-1>{});
  }
  __builtin_amdgcn_s_setprio(PRG2);

  const u32 tg32 = (u32)(tag >> 32);
  LAS char* pv[2] = {sm + wave * REG + aoff[0], sm + wave * REG + aoff[1]};
  auto vfrag = [&](int k, int mi) { LAS char* a = pv[mi] + rblk(k) * 2048; return lds_tr2(a, a + 512); };
  auto fresh = [&](auto K0, auto K1, u32 m) {
    constexpr int k0 = decltype(K0)::value, k1 = decltype(K1)::value, ke = mn(k1 | 1, 27);
    if (__builtin_expect(any(m & SGN), 0)) {
#pragma unroll 1
      for (int it = 0; it < SPIN_MAX; ++it) {
        static_for<k0, ke + 1>([&](auto K) { hload(K, IC<0>{}, IC<1>{}); hload(K, IC<1>{}, IC<1>{}); });
        wait_vm<0>();
        u32 z = 0;
        static_for<k0, ke + 1>([&](auto K) { constexpr int k = decltype(K)::value; pin(hb[k][0], hb[k][1]); if constexpr (k <= k1) z |= strip(hb[k][0], sg) | strip(hb[k][1], sg); });
        if (!any(z & SGN)) break;
        __builtin_amdgcn_s_sleep(1);
      }
    }
  };
  auto abad = [&]() {
    u32 m = 0;
#pragma unroll
    for (int i = A0; i < 5; ++i) m |= (av[i].y ^ tg32) | (av[i].w ^ tg32);
    return any(m);
  };
  LAS char* ap = sm + wave * REG + 9 * 2048;
  v4u a4[2];
  float ga[2][8], gs[2];
  auto aput = [&]() {
    *(LAS v4u*)(ap + l7 * 128 + ((l3 ^ l7) << 4)) = (v4u){av[0].x, av[1].x, av[2].x, av[3].x};
    *(LAS v4u*)(ap + l7 * 128 + ((l3 ^ l7) << 4) + 1024) = (v4u){av[0].z, av[1].z, av[2].z, av[3].z};
    a4[0] = *(LAS v4u*)(ap + l3 * 128 + ((l3 ^ l7) << 4));
    a4[1] = *(LAS v4u*)(ap + l3 * 128 + ((l3 ^ l7) << 4) + 1024);
  };
  auto amix = [&](int e, int k) {
    const float gt = __uint_as_float(e ? av[4].z : av[4].x), gn = opaque(1.f - gt);
    ga[e][2 * k] = opaque(__builtin_fmaf(gt, bflo(a4[e][k]), opaque(gn * bflo(av[5][k]))));
    ga[e][2 * k + 1] = opaque(__builtin_fmaf(gt, bfhi(a4[e][k]), opaque(gn * bfhi(av[5][k]))));
    if (k == 0) gs[e] = gn;
  };
  f16v o2[2][2];
  bf8 wb[28][2];
  bool late = false;
  fence_sched();
  wait_pin<mn(TB.younger(OH, 0, 1, G2P), AUX ? 63 : TB.younger(OR, 0, 1, G2P))>(hb[0][0], hb[0][1]);
  wb[0][0] = vfrag(0, 0); wb[0][1] = vfrag(0, 1);
  fresh(IC<0>{}, IC<0>{}, strip(hb[0][0], sg) | strip(hb[0][1], sg));
  STAMP(7);
  static_for<0, 28 - NT>([&](auto K) {
    constexpr int k = decltype(K)::value, kl = k == 27 - NT ? 27 : k + 1;
    fence_sched();
    wait_vm<g2wait<AUX>(k)>();
    static_for<k + 1, kl + 1>([&](auto J) { constexpr int jn = decltype(J)::value; pin(hb[jn][0], hb[jn][1]); });
    if constexpr (k == KC) { wait_pin<TB.younger(OP, 1, 0, G2P + 4 * k)>(pf); gate(1); }
    if constexpr (k == KB) { wait_pin<TB.younger(OA, 0, 5, G2P + 4 * k)>(av); }
    STAMPQ(32 + k);
    u32 m = 0;
    static_for<0, 4>([&](auto G) {
      constexpr int gi = decltype(G)::value, mi = gi & 1, ni = gi >> 1;
      m0_for(IC<G2P + 4 * k + gi>{});
      fence_sched();
      if constexpr (k == 0) o2[mi][ni] = mfma32(wb[k][mi], __builtin_bit_cast(bf8, hb[k][ni]), (f16v)(0.f));
      else o2[mi][ni] = mfma32(wb[k][mi], __builtin_bit_cast(bf8, hb[k][ni]), o2[mi][ni]);
      fence_sched();
      if constexpr (gi < 2) static_for<k + 1, kl + 1>([&](auto J) {
        constexpr int jn = decltype(J)::value;
        wb[jn][gi] = vfrag(jn, gi);
        m |= strip(hb[jn][gi], sg);
      });
      if constexpr (k == KB && gi == 0) aput();
      if constexpr (k == KB && gi == 2) late = abad();
      if constexpr (k > KB && k <= KB + 2) amix(k - KB - 1, gi);
      fence_sched();
      issue_at(IC<G2P + 4 * k + gi>{});
      if constexpr (gi == 2) fresh(IC<k + 1>{}, IC<kl>{}, m);
    });
  });
  fence_sched();
  LAS char* pw = sm + wave * REG + l31 * 256;
  const int kw = (4 * hh) ^ (((l31 >> 1) & 3) | ((l31 & 1) << 2));
  auto pput = [&](int tl, int i) {
    const f16v& a = o2[tl >> 1][tl & 1];
    *(LAS f4*)(pw + ((kw ^ i) << 4) + (tl >> 1) * 128 + (tl & 1) * 8192) = (f4){a[4 * i], a[4 * i + 1], a[4 * i + 2], a[4 * i + 3]};
  };
  static_for<0, 4 * NT>([&](auto N) {
    constexpr int n = decltype(N)::value, tl = n / NT, v = n % NT, k = 28 - NT + v;
    o2[tl >> 1][tl & 1] = mfma32(wb[k][tl >> 1], __builtin_bit_cast(bf8, hb[k][tl & 1]), o2[tl >> 1][tl & 1]);
    fence_sched();
    if constexpr (constexpr int u = 4 * (27 - NT - KB) + n; u < 8) amix(u >> 2, u & 3);
    if constexpr (tl >= 1 && v == NT - 1) { pput(tl - 1, 0); pput(tl - 1, 1); pput(tl - 1, 2); pput(tl - 1, 3); }
    fence_sched();
  });
  STAMP(10); STAMPC(62);
  if (__builtin_expect(late, 0)) {
#pragma unroll 1
    for (int it = 0; it < SPIN_MAX; ++it) {
      static_for<0, 5>(aload);
      wait_pin<0>(av);
      if (!abad()) break;
      __builtin_amdgcn_s_sleep(1);
    }
    aput();
#pragma unroll
    for (int i = 0; i < 8; ++i) amix(i >> 2, i & 3);
  }
#pragma unroll
  for (int i = 0; i < 4; ++i) pput(3, i);
  lds_bar();
  STAMP(11);
  if constexpr (!AUX) asm volatile(".Lpf_end:");
  {
    LAS char* pr = sm + (16 * wave + 2 * l3) * 256;
    const int kr = (2 * l7) ^ (l3 & 3);
#pragma unroll
    for (int e = 0; e < 2; ++e) {
      v4u d;
#pragma unroll
      for (int cc = 0; cc < 2; ++cc) {
        LAS char* a = pr + e * 256 + ((kr ^ cc ^ (4 * e)) << 4);
        const f4 s = (*(LAS f4*)(a) + *(LAS f4*)(a + REG)) + (*(LAS f4*)(a + 2 * REG) + *(LAS f4*)(a + 3 * REG));
#pragma unroll
        for (int k = 0; k < 2; ++k)
          d[2 * cc + k] = pk2(opaque(__builtin_fmaf(gs[e], s[2 * k], ga[e][4 * cc + 2 * k])), opaque(__builtin_fmaf(gs[e], s[2 * k + 1], ga[e][4 * cc + 2 * k + 1])));
      }
      *(v4u*)(out + (size_t)(64 * t2 + 16 * wave + 2 * l3 + e) * 512 + 64 * c2 + 8 * l7) = d;
    }
  }
  STAMP(12);
  STAMP_OUT;
}

struct cf { float x, y; };
DI cf cmul(cf a, cf m) { return cf{opaque(__builtin_fmaf(a.x, m.x, -opaque(a.y * m.y))), opaque(__builtin_fmaf(a.x, m.y, opaque(a.y * m.x)))}; }
DI cf cexp_s(float lr, float rv, float m) {
  const float e = __builtin_amdgcn_exp2f(lr * m), f = __builtin_amdgcn_fractf(rv * m);
  return cf{opaque(e * __builtin_amdgcn_cosf(f)), opaque(e * __builtin_amdgcn_sinf(f))};
}

constexpr u32 TROW = 544, TCH = 63 * TROW, XT = 69632;
constexpr u32 KCH = 9472, KOO = 4656, UA0 = 18944, UCH = 8768, UBO = 4224, EX = 40960;

constexpr int NPF = !!(PF & 1) + !!(PF & 2) + 2 * !!(PF & 4) + !!(PF & 8) + !!(PF & 16);

DI void conv_role(int ci, LAS char* sm, const u16* x, const u16* are, const u16* aim, const u16* bre, const u16* bim,
                  const u16* cre, const u16* cim, const u16* logdt, const u16* dskip, const u16* w1, const u16* w2, char* ws) {
  const int tid = threadIdx.x, lane = tid & 63, wave = __builtin_amdgcn_readfirstlane(tid >> 6);
  STAMP_DECL;
  STAMP(0);
  const rsrc_t rws = mkrsrc(ws, WS_END), rx = mkrsrc(x, 2048 * 512 * 2);
  const u64 tst = HOLD >= 0 ? __builtin_amdgcn_s_memrealtime() : 0;
  HOLDPT(0);
  if (CDLY) __builtin_amdgcn_s_sleep(CDLY);
  v2u ep = ld64_sc1(0, rws);
  u32 tch = 0;
  {
    u64 pc; int len;
    asm volatile("s_getpc_b64 %0\n.Lpf_here:\n\ts_max_i32 %1, 0, .Lpf_end-.Lpf_here\n\ts_min_i32 %1, %1, 0x10000" : "=s"(pc), "=s"(len) :: "scc");
    touch(tch, lane < 4 ? (u32)((4 * (ci >> 3) + wave) * 4 + lane) * 128 : 1u << 30, mkrsrc((const void*)pc, (u32)len));
  }
  __builtin_amdgcn_s_setprio(PRC0);
  const u32 smb = (u32)(uintptr_t)sm;
  const int pr = (ci & 7) * 32 + (ci >> 3), cg = (ci >> 3) & 7, tt = 4 * (ci & 7) + (ci >> 6);
  const int g = lane >> 4, q = (lane >> 2) & 3, p = lane & 3, n = lane & 15;
  const int ch = wave >> 1, half = wave & 1;

  {
    const u32 v0 = (u32)(64 * tt + 16 * wave + (lane >> 3)) * 1024 + cg * 128 + (((lane & 7) ^ ((g & 1) << 1)) << 4);
    const u32 v1 = (v0 ^ 64) + 7168;
    dma2(smb + XT + wave * 2048, v0, v1, rx, 0);
  }
  const int pi = pr * 256 + tid;
  u32 r_are = are[pi], r_aim = aim[pi], r_bre = bre[pi], r_bim = bim[pi], r_cre = cre[pi], r_cim = cim[pi];
  u32 r_ldt = logdt[2 * pr + ch];
  u32 r_dsk = *(const u32*)(dskip + 2 * pr);
  fence_sched();
  asm volatile("s_waitcnt vmcnt(0)" : "+v"(r_are), "+v"(r_aim), "+v"(r_bre), "+v"(r_bim), "+v"(r_cre), "+v"(r_cim), "+v"(r_ldt), "+v"(r_dsk), "+v"(ep) :: "memory");
  const u64 tag = epoch_tag(ep);
  const u32 tg32 = (u32)(tag >> 32);
  fence_sched();
  STAMP(1);
  {
    LAS char* tb = sm + XT + wave * 2048 + (8 * (p & 1) + q) * 128 + (p >> 1) * 8;
    const int sw = (q & 2) | ((p & 1) << 2);
    const u32 vo = WS_UT + (u32)((16 * cg + 2 * g + (q >> 1)) * 256 + 8 * tt + 2 * wave + (q & 1)) * 64 + p * 16;
    const bf8 d0 = lds_tr2(tb + ((g ^ sw) << 4), tb + ((g ^ sw) << 4) + 512);
    const bf8 d1 = lds_tr2(tb + (((4 + g) ^ sw) << 4), tb + (((4 + g) ^ sw) << 4) + 512);
    st128_sc1<0>(__builtin_bit_cast(v4u, d0), vo, rws, 0);
    st128_sc1<0>(__builtin_bit_cast(v4u, d1), vo, rws, 8 * 256 * 64);
  }
  fence_sched();
  {
    const u32 wv = 4 * ci + wave, big = 1u << 30;
    if (PF & 1) touch(tch, lane < 7 ? (wv >> 1) * 3584 + ((wv & 1) * 7 + lane) * 256 + 128 : big, mkrsrc(w1, 512 * 1792 * 2));
    if (PF & 2) touch(tch, lane < 14 ? (wv * 14 + lane) * 128 : big, mkrsrc(w2, 512 * 1792 * 2));
    if (PF & 4) {
      const u32 wx = 4 * (ci >> 3) + wave;
      touch(tch, (wx * 112 + lane) * 128, mkrsrc(w2, 512 * 1792 * 2));
      touch(tch, lane < 48 ? (wx * 112 + 64 + lane) * 128 : big, mkrsrc(w2, 512 * 1792 * 2));
    }
    if (PF & 8) touch_ws(tch, lane < 56 ? WS_H + (wv * 56 + lane) * 128 : big, rws);
    if (PF & 16) touch_ws(tch, lane < 32 ? WS_A + (wv * 32 + lane) * 128 : big, rws);
  }
  fence_sched();

  HOLDPT(1);
  __builtin_amdgcn_s_setprio(PRC1);
  const u32 uf = WS_UFLAG + (u32)(cg * 128 + tt * 4 + wave) * FSP;
  {
    const float dt = bflo(pk2(expf(bflo(r_ldt)), 0.f));
    const float lr = dt * bflo(r_are) * 1.44269504088896341f, rv = dt * bflo(r_aim) * 0.15915494309189535f;
    const cf db = {opaque(dt * bflo(r_bre)), opaque(-dt * bflo(r_bim))};
    const cf cb = cmul(cf{bflo(r_cre), -bflo(r_cim)}, db);
    const cf e16 = cexp_s(lr, rv, 16.f), e1 = cexp_s(lr, -rv, 1.f), e256 = cexp_s(lr, -rv, 256.f);
    const cf e2 = cexp_s(lr, -rv, 2.f), e4 = cexp_s(lr, -rv, 4.f), e8 = cexp_s(lr, -rv, 8.f);
    LAS char* tw = sm + ch * TCH + (tid & 127) * 4;
    u32 tv[63];
    auto put = [&](int row, cf v) {
      if (BURST) tv[row] = pk2(v.x, v.y);
      else if (KO & 1) { const u32 q_ = pk2(v.x, v.y); asm volatile("" :: "v"(q_)); }
      else if (KO & 2) *(volatile LAS u32*)(tw + row * TROW) = r_are;
      else *(volatile LAS u32*)(tw + row * TROW) = pk2(v.x, v.y);
    };
    cf a = e16, t0 = cb, tb[8], v[8];
    put(0, cf{1.f, 0.f}); put(16, t0);
    tb[0] = cb;
#pragma unroll
    for (int i = 1; i < 16; ++i) {
      if (i == UFP) { wait_vm<NPF>(); flag_store(uf, rws, tag); }
      put(i, a); if (i < 15) a = cmul(a, e16);
      t0 = cmul(t0, e1); put(16 + i, t0);
      if (i < 8) { tb[i] = cmul(tb[i - 1], e256); v[i] = tb[i]; }
#pragma unroll
      for (int tl = 1; tl < 8; ++tl) {
        const int base = tl == 1 ? 32 : (tl < 4 ? 41 + 5 * (tl - 2) : 51 + 3 * (tl - 4));
        const int cnt = tl == 1 ? 9 : (tl < 4 ? 5 : 3), k = i - tl;
        if (k >= 0 && k < cnt) {
          if (k > 0) v[tl] = cmul(v[tl], tl == 1 ? e2 : (tl < 4 ? e4 : e8));
          put(base + k, v[tl]);
        }
      }
    }
    if (BURST) {
#pragma unroll
      for (int i = 0; i < 63; ++i) asm volatile("" : "+v"(tv[i]));
      fence_sched();
      __builtin_amdgcn_s_setprio(PRCB);
      asm volatile("s_mov_b32 m0, %0\n\ts_nop 0" :: "s"(smb + ch * TCH + half * 256) : "m0");
      static_for<0, 63>([&](auto R) { lds_put_tid<decltype(R)::value * TROW>(tv[decltype(R)::value]); });
      __builtin_amdgcn_s_setprio(PRC1);
    }
  }
  fence_sched();
  if (UFP >= 16) { wait_vm<NPF>(); flag_store(uf, rws, tag); }
  HOLDPT(2);
  const u32 vf = WS_UFLAG + (u32)((ci & 7) * 128 + wave * 32 + (lane & 31)) * FSP;
  v2u pf = ld64_sc1(vf, rws);
  STAMP(2);
  lds_bar();

  f4 kk[4];
  v4u uv[2];
  {
    LAS char* tb = sm + ch * TCH + g * 16;
    const int m2 = (n + ((n >> 1) & 1)) >> 1, m4 = (n + 1 + ((n >> 2) & 1)) >> 2, m8 = (n + 3 + ((n >> 3) & 1)) >> 3;
    LAS char* pr_[4] = {tb + (half ? 51 + m8 : 16 + n) * TROW, tb + (half ? 54 + m8 : 32 + m2) * TROW,
                        tb + (half ? 57 + m8 : 41 + m4) * TROW, tb + (half ? 60 + m8 : 46 + m4) * TROW};
    LAS char* pe = tb + n * TROW;
#pragma unroll
    for (int i = 0; i < 4; ++i) kk[i] = (f4)(0.f);
#pragma unroll
    for (int ks = 0; ks < 8; ++ks) {
      if (ks == 4) {
        fence_sched();
        wait_pin<0>(pf);
        asm volatile("" :: "v"(tch));
        if (__builtin_expect(flag_bad(pf, tag), 0)) flag_wait(vf, rws, tag);
        STAMP(3);
        const u32 vo = WS_UT + (u32)((pr >> 1) * 256 + 64 * wave + (lane >> 1)) * 64 + ((pr & 1) * 2 + (lane & 1)) * 16;
        uv[0] = ld128_sc1<0>(vo, rws, 0); uv[1] = ld128_sc1<2048>(vo, rws, 0);
        fence_sched();
      }
      if (KO & 4) continue;
      const bf8 e = lds_rd128(pe + ks * 64);
#pragma unroll
      for (int i = 0; i < 4; ++i) kk[i] = mfma16(lds_rd128(pr_[i] + ks * 64), e, kk[i]);
    }
  }
  fence_sched();
  STAMP(4);
  HOLDPT(3);
  lds_bar();
  if (KO & 8) asm volatile("" :: "v"(kk[0]), "v"(kk[1]), "v"(kk[2]), "v"(kk[3]));
  else {
    LAS char* kb = sm + ch * KCH;
#pragma unroll
    for (int i = 0; i < 4; ++i) {
      const int l0 = 256 * (4 * half + i) + 16 * n + 4 * g;
      const float k0 = opaque(kk[i][0]), k1 = opaque(kk[i][1]), k2 = opaque(kk[i][2]), k3 = opaque(kk[i][3]);
      const u32 a = pk2(k3, k2), bq = pk2(k1, k0), c = pk2(k2, k1);
      *(LAS v2u*)(kb + KOO + 2 * (2044 - l0)) = (v2u){a, bq};
      *(LAS u16*)(kb + 2 * (2051 - l0)) = (u16)a;
      *(LAS u32*)(kb + 2 * (2052 - l0)) = c;
      *(LAS u16*)(kb + 2 * (2054 - l0)) = (u16)(bq >> 16);
    }
    const v4u z = {0, 0, 0, 0};
    if (lane < 29) *(LAS v4u*)(kb + (half ? KOO + 4096 : 4112) + lane * 16) = z;
    else if (lane < 33) *(LAS v4u*)(sm + UA0 + ch * UCH + (half ? UBO : 0) + (lane - 29) * 16 - (half ? 0 : 16)) = z;
    else if (lane == 33 && !half) *(LAS u16*)(kb + 2 * 2055) = 0;
    else if (lane == 34 && !half) *(LAS v3u*)(sm + UA0 + ch * UCH + 48) = (v3u){0, 0, 0};
  }
  asm volatile("s_waitcnt vmcnt(0)" : "+v"(uv[0]), "+v"(uv[1]) :: "memory");
#pragma unroll
  for (int k = 0; k < 2; ++k) {
    if (KO & 8) continue;
    LAS char* ub = sm + UA0 + (lane & 1) * UCH + 64 + (64 * wave + 32 * k + (lane >> 1)) * 16;
    *(LAS v4u*)(ub + UBO) = uv[k];
    *(LAS u32*)(ub - 4) = uv[k].x;
    *(LAS v3u*)(ub) = (v3u){uv[k].y, uv[k].z, uv[k].w};
  }
  lds_bar();
  STAMP(5);
  flag_store(WS_EFLAG + (u32)(256 + ci) * 8, rws, tag);
  HOLDPT(4);
  const rsrc_t rw2 = mkrsrc(w2, 512 * 1792 * 2);
  const u32 wx = 4 * (ci >> 3) + wave, vt = lane < 8 ? (128 * (wx >> 6) + 64 + (wx & 63)) * 1024 + lane * 128 : 1u << 30;
  __builtin_amdgcn_s_setprio(PRCV);

  f4 y[8];
  if (KO & 16) { for (int i = 0; i < 8; ++i) y[i] = kk[i & 3]; }
  else {
    LAS char* pk = sm + ch * KCH + ((n & 1) ? KOO + 4032 : 4048) - 64 * (n >> 1) + 16 * g - 3584 + 256 * half;
    LAS char* pu = sm + UA0 + ch * UCH + (n < 8 ? 8 * n : UBO + 8 * (n - 8) + 8) + 16 * g + 256 * half;
    bf8 kf[4][8], uf[4][8];
    auto rd = [&](int rr, int i) {
      kf[rr][i] = lds_rd128(pk + 512 * (7 - i) + 64 * rr);
      const v2u lo = *(volatile LAS v2u*)(pu + 512 * i + 64 * rr), hi = *(volatile LAS v2u*)(pu + 512 * i + 64 * rr + 8);
      uf[rr][i] = __builtin_bit_cast(bf8, (v4u){lo.x, lo.y, hi.x, hi.y});
    };
#pragma unroll
    for (int i = 0; i < 8; ++i) { rd(0, i); fence_sched(); }
    static_for<0, 144>([&](auto N) {
      constexpr int nn = decltype(N)::value, rr = nn / 36, v = nn % 36;
      constexpr int s = v < 1 ? 0 : v < 3 ? 1 : v < 6 ? 2 : v < 10 ? 3 : v < 15 ? 4 : v < 21 ? 5 : v < 28 ? 6 : 7, m = v - s * (s + 1) / 2;
      if constexpr (rr == 0 && m == 0) y[s] = mfma16(kf[rr][s - m], uf[rr][m], (f4)(0.f));
      else y[s] = mfma16(kf[rr][s - m], uf[rr][m], y[s]);
      fence_sched();
      if constexpr (rr < 3 && v % 4 == 2 && v / 4 < 8) rd(rr + 1, v / 4);
      if constexpr ((PF & 64) && nn >= TK0 && (nn - TK0) % TKS == 0 && (nn - TK0) / TKS < 7) touch(tch, vt, rw2, (nn - TK0) / TKS * 262144);
      fence_sched();
    });
  }
  STAMP(6);
  HOLDPT(5);
#pragma unroll
  for (int a = 0; a < 8; ++a) *(LAS f4*)(sm + EX + (wave * 8 + a) * 1024 + lane * 16) = y[a];
  lds_bar();
  {
    const float d0 = bflo(r_dsk), d1 = bfhi(r_dsk);
    const int rn = n < 8 ? 2 * n : 2 * (n - 8) + 1;
#pragma unroll
    for (int i = 0; i < 2; ++i) {
      const int al = 2 * wave + i;
      LAS char* ex = sm + EX + al * 1024 + lane * 16;
      const f4 y0 = *(LAS f4*)(ex) + *(LAS f4*)(ex + 8192), y1 = *(LAS f4*)(ex + 16384) + *(LAS f4*)(ex + 24576);
      LAS char* up = sm + UA0 + UBO + 64 + (256 * al + 2 * rn + 64 * g) * 2;
      const u32 u00 = *(LAS u32*)(up), u01 = *(LAS u32*)(up + 64), u10 = *(LAS u32*)(up + UCH), u11 = *(LAS u32*)(up + UCH + 64);
      auto mix = [&](float y, float u, float d) { return opaque(__builtin_fmaf(u, d, opaque(y))); };
      const u32 x0 = pk2(mix(y0[0], bflo(u00), d0), mix(y1[0], bflo(u10), d1)), x1 = pk2(mix(y0[1], bfhi(u00), d0), mix(y1[1], bfhi(u10), d1));
      const u32 z0 = pk2(mix(y0[2], bflo(u01), d0), mix(y1[2], bflo(u11), d1)), z1 = pk2(mix(y0[3], bfhi(u01), d0), mix(y1[3], bfhi(u11), d1));
      const u32 vo = WS_A + (u32)(4 * al + g) * 131072 + pr * 512 + rn * 16;
      st128_sc1<0>((v4u){x0, tg32, x1, tg32}, vo, rws, 0);
      st128_sc1<256>((v4u){z0, tg32, z1, tg32}, vo, rws, 0);
    }
  }
  STAMP(7);
  if (PF & 64) asm volatile("s_waitcnt vmcnt(4)" :: "v"(tch) : "memory");
  if (ci == 0 && wave == 0) {
#pragma unroll 1
    for (int it = 0; it < SPIN_MAX; ++it) {
      v4u f[4];
      static_for<0, 4>([&](auto I) { f[decltype(I)::value] = ld128_sc1<decltype(I)::value * 1024>(WS_EFLAG + lane * 16, rws, 0); });
      wait_pin<0>(f[0], f[1]); pin(f[2], f[3]);
      u32 m = 0;
#pragma unroll
      for (int i = E0; i < 4; ++i) m |= (f[i].x ^ (u32)tag) | (f[i].y ^ tg32) | (f[i].z ^ (u32)tag) | (f[i].w ^ tg32);
      if (!any(m)) break;
      __builtin_amdgcn_s_sleep(8);
    }
    flag_store(0, rws, tag);
  }
  STAMP_OUT;
}

__global__ __launch_bounds__(256, 2) void fused_kernel(const u16* __restrict__ x, const u16* __restrict__ are, const u16* __restrict__ aim,
                                                       const u16* __restrict__ bre, const u16* __restrict__ bim, const u16* __restrict__ cre,
                                                       const u16* __restrict__ cim, const u16* __restrict__ logdt, const u16* __restrict__ dskip,
                                                       const u16* __restrict__ w1, const u16* __restrict__ b1, const u16* __restrict__ w2,
                                                       const u16* __restrict__ b2, const u16* __restrict__ gw, u16* __restrict__ out, char* ws) {
  __shared__ __attribute__((aligned(256))) char smem[81920];
  LAS char* sm = (LAS char*)smem;
  asm volatile("" :: "s"(x), "s"(are), "s"(aim), "s"(bre), "s"(bim), "s"(cre), "s"(cim), "s"(logdt), "s"(dskip), "s"(w1), "s"(b1),
               "s"(w2), "s"(b2), "s"(gw), "s"(out), "s"(ws));
  const int b = blockIdx.x;
  const bool mlp = (b < 256) == (GFIRST != 0);
  if (mlp && (b & 255) < 224) gemm_role<false>(b & 255, sm, x, w1, b1, w2, b2, gw, out, ws);
  else if (mlp) gemm_role<true>(b & 255, sm, x, w1, b1, w2, b2, gw, out, ws);
  else conv_role(b & 255, sm, x, are, aim, bre, bim, cre, cim, logdt, dskip, w1, w2, ws);
}

extern "C" void kernel_launch(void* const* d_in, const int* in_sizes, int n_in, void* d_out, int out_size, void* d_ws, size_t ws_size,
                              hipStream_t stream) {
  const u16* const* in = (const u16* const*)d_in;
  fused_kernel<<<512, 256, 0, stream>>>(in[0], in[1], in[2], in[3], in[4], in[5], in[6], in[7], in[8], in[10], in[11], in[12], in[13],
                                        in[14], (u16*)d_out, (char*)d_ws + (((ws_size - WS_END) / 16 * WSPOS) & ~(size_t)0xfffff));
}
```

```cpp
#define PROBE 0
#define PROLE 2
#include <hip/hip_runtime.h>
#include <stdint.h>
#include <type_traits>
#pragma clang diagnostic ignored "-Winline-asm"

typedef unsigned short u16;
typedef unsigned int u32;
typedef unsigned long long u64;
typedef __attribute__((ext_vector_type(2))) u32 v2u;
typedef __attribute__((ext_vector_type(3))) u32 v3u;
typedef __attribute__((ext_vector_type(4))) u32 v4u;
typedef __attribute__((ext_vector_type(2))) float f2;
typedef __attribute__((ext_vector_type(4))) float f4;
typedef __attribute__((ext_vector_type(16))) float f16v;
typedef __attribute__((ext_vector_type(2))) __bf16 bf2;
typedef __attribute__((ext_vector_type(8))) __bf16 bf8;
typedef __attribute__((ext_vector_type(4))) short s4;
typedef __amdgpu_buffer_rsrc_t rsrc_t;
#define DI __device__ __forceinline__
#define LAS __attribute__((address_space(3)))
#define CAS __attribute__((address_space(4)))

#define GFIRST 1
#define LAM 4
#define UPP 2
#define PA 110
#define PC 127
#define PD 8
#define KP 6
#define KC 9
#define KA 19
#define KB 25
#define NT 2
#define W2PF 0
#define PRG1 2
#define PRG2 1
#define PRC0 3
#define PRC1 1
#define PRCV 2
#define UFP 8
#define PF 35
#define KO 0
#define HOLD -1
#define HOLDPT(n) do { if (HOLD == n) { for (int i_ = 0; i_ < 4000 && __builtin_amdgcn_s_memrealtime() - tst < 900; ++i_) __builtin_amdgcn_s_sleep(8); } } while (0)
#define BURST 1
#define PRCB 3
#define CDLY 4
#define ESLP 40
#define C2X 0
#define PFP 0
#define ROT2 1
#define TK0 4
#define TKS 20
#define LAYOUT 0
#define WSPOS 0
#define PFQ "sc1"

constexpr u32 FSP = 256;
constexpr u32 WS_G = 4096, WS_EFLAG = 65536, WS_UFLAG = WS_EFLAG + 1024 * FSP;
constexpr u32 WS_UT = 1u << 20, WS_A = 3u << 20, WS_H = 7u << 20, WS_END = 15u << 20;
constexpr int SPIN_MAX = 1 << 16;
constexpr int A0 = 0;

constexpr int E0 = 0;

#define STAMP(i) do { if ((i) == 0) t0_ = __builtin_amdgcn_s_memrealtime(); if ((i) == PROBE && role_ == PROLE) tk_ = __builtin_amdgcn_s_memrealtime(); } while (0)
#define STAMPQ(i) STAMP(i)
#define STAMPC(i)
#define STAMP_DECL u64 t0_ = 0, tk_ = 0
#define STAMP_OUT do { if (role_ == PROLE) for (int i_ = 0; i_ < 100000 && __builtin_amdgcn_s_memrealtime() - t0_ < 2500 + 5 * (tk_ - t0_); ++i_) __builtin_amdgcn_s_sleep(2); } while (0)

DI rsrc_t mkrsrc(const void* p, u32 bytes) { return __builtin_amdgcn_make_buffer_rsrc((void*)p, (short)0, (int)bytes, 0x00020000); }
DI float bflo(u32 v) { return __uint_as_float(v << 16); }
DI float bfhi(u32 v) { return __uint_as_float(v & 0xffff0000u); }
DI u32 pk2(float a, float b) { bf2 r = __builtin_convertvector((f2){a, b}, bf2); return __builtin_bit_cast(u32, r); }
DI float dot2(u32 a, u32 b, float c) { return __builtin_amdgcn_fdot2_f32_bf16(__builtin_bit_cast(bf2, a), __builtin_bit_cast(bf2, b), c, false); }
DI float relu(float v) { return __int_as_float(max(__float_as_int(v), 0)); }
DI void fence_sched() { __builtin_amdgcn_sched_barrier(0); }

template <int N> DI void wait_vm() { asm volatile("s_waitcnt vmcnt(%0)" :: "n"(N) : "memory"); }
template <int N> DI void wait_vm_bar() { asm volatile("s_waitcnt vmcnt(%0) lgkmcnt(0)\n\ts_barrier" :: "n"(N) : "memory"); }
DI void lds_bar() { asm volatile("s_waitcnt lgkmcnt(0)\n\ts_barrier" ::: "memory"); }
DI void wait_lgkm() { asm volatile("s_waitcnt lgkmcnt(0)" ::: "memory"); }

DI void dma2(u32 m0v, u32 v0, u32 v1, rsrc_t rs, u32 so) {
  asm volatile("s_mov_b32 m0, %0\n\ts_nop 0\n\t"
               "buffer_load_dwordx4 %1, %3, %4 offen lds\n\t"
               "buffer_load_dwordx4 %2, %3, %4 offen offset:1024 lds"
               :: "s"(m0v), "v"(v0), "v"(v1), "s"(rs), "s"(so) : "m0", "memory");
}
template <u32 off> DI void set_m0(u32 base) { asm volatile("s_add_u32 m0, %0, %1" :: "s"(base), "n"(off) : "m0", "scc"); }
template <int OFF> DI void dma1(u32 v, rsrc_t rs, u32 so) {
  asm volatile("buffer_load_dwordx4 %0, %1, %2 offen offset:%3 lds" :: "v"(v), "s"(rs), "s"(so), "n"(OFF) : "memory");
}
template <int OFF> DI void reld128_sc1(v4u& r, u32 vo, rsrc_t rs, u32 so) {
  asm volatile("buffer_load_dwordx4 %0, %1, %2, %3 offen offset:%4 sc1" : "+v"(r) : "v"(vo), "s"(rs), "s"(so), "n"(OFF) : "memory");
}
template <int OFF> DI u32 ld32_sc1(u32 vo, rsrc_t rs, u32 so) {
  u32 r; asm volatile("buffer_load_dword %0, %1, %2, %3 offen offset:%4 sc1" : "=&v"(r) : "v"(vo), "s"(rs), "s"(so), "n"(OFF) : "memory"); return r;
}
template <int OFF> DI v4u ld128_sc1(u32 vo, rsrc_t rs, u32 so) {
  v4u r; asm volatile("buffer_load_dwordx4 %0, %1, %2, %3 offen offset:%4 sc1" : "=&v"(r) : "v"(vo), "s"(rs), "s"(so), "n"(OFF) : "memory"); return r;
}
DI v4u ld128(u32 vo, rsrc_t rs) {
  v4u r; asm volatile("buffer_load_dwordx4 %0, %1, %2, 0 offen" : "=&v"(r) : "v"(vo), "s"(rs) : "memory"); return r;
}
DI v2u ld64_sc1(u32 vo, rsrc_t rs) {
  v2u r; asm volatile("buffer_load_dwordx2 %0, %1, %2, 0 offen sc1" : "=&v"(r) : "v"(vo), "s"(rs) : "memory"); return r;
}
template <int OFF> DI u32 ld16(u32 vo, rsrc_t rs) {
  u32 r; asm volatile("buffer_load_ushort %0, %1, %2, 0 offen offset:%3" : "=&v"(r) : "v"(vo), "s"(rs), "n"(OFF) : "memory"); return r;
}
template <int OFF> DI void st128_sc1(v4u d, u32 vo, rsrc_t rs, u32 so) {
  asm volatile("buffer_store_dwordx4 %0, %1, %2, %3 offen offset:%4 sc1\n\ts_nop 1" :: "v"(d), "v"(vo), "s"(rs), "s"(so), "n"(OFF) : "memory");
}
DI void st64_sc1(v2u d, u32 vo, rsrc_t rs) {
  asm volatile("buffer_store_dwordx2 %0, %1, %2, 0 offen sc1" :: "v"(d), "v"(vo), "s"(rs) : "memory");
}
DI void flag_store(u32 off, rsrc_t rs, u64 tag) {
  const v2u d = {(u32)tag, (u32)(tag >> 32)};
  asm volatile("s_mov_b64 exec, 1\n\tbuffer_store_dwordx2 %0, off, %1, %2 sc1\n\ts_mov_b64 exec, -1" :: "v"(d), "s"(rs), "s"(off) : "memory");
}
DI u64 epoch_tag(v2u e) {
  const u64 v = (u64)(u32)__builtin_amdgcn_readfirstlane(e.x) | ((u64)(u32)__builtin_amdgcn_readfirstlane(e.y) << 32);
  return ((v * 0x9E3779B97F4A7C15ull + 0x632BE59BD9B4E019ull) & ~0xffffull) | (~v & 0xffffull);
}
constexpr u32 SGN = 0x80008000u;
DI u32 sign_of(u64 tag, int t) { return ((((u32)tag >> (t >> 1)) ^ t) & 1) ? SGN : 0u; }
DI u32 strip(v4u& d, u32 s) { d ^= s; return (d.x | d.y) | (d.z | d.w); }
DI bool any(u32 m) { return __builtin_amdgcn_ballot_w64(m != 0) != 0; }
DI bool flag_bad(v2u v, u64 tag) { return __builtin_amdgcn_ballot_w64((v.x != (u32)tag) | (v.y != (u32)(tag >> 32))) != 0; }
template <int SL = 1> DI void flag_wait(u32 vo, rsrc_t rs, u64 tag) {
#pragma unroll 1
  for (int it = 0; it < SPIN_MAX; ++it) {
    v2u v; asm volatile("buffer_load_dwordx2 %0, %1, %2, 0 offen sc1\n\ts_waitcnt vmcnt(0)" : "=&v"(v) : "v"(vo), "s"(rs) : "memory");
    if (!flag_bad(v, tag)) break;
    __builtin_amdgcn_s_sleep(SL);
  }
}

template <int SL = 1> DI void granule_wait(u32 vo, rsrc_t rs, u32 tg) {
#pragma unroll 1
  for (int it = 0; it < SPIN_MAX; ++it) {
    v2u v; asm volatile("buffer_load_dwordx2 %0, %1, %2, 0 offen sc1\n\ts_waitcnt vmcnt(0)" : "=&v"(v) : "v"(vo), "s"(rs) : "memory");
    if (__builtin_amdgcn_ballot_w64(v.y != tg) == 0) break;
    __builtin_amdgcn_s_sleep(SL);
  }
}
DI float opaque(float v) { asm("" : "+v"(v)); return v; }

template <int OFF> DI void lds_put_tid(u32 v) { asm volatile("ds_write_addtid_b32 %0 offset:%1" :: "v"(v), "n"(OFF) : "memory"); }
DI bf8 lds_rd128(LAS char* p) { return *(LAS bf8*)p; }
DI bf8 lds_tr2(LAS char* p0, LAS char* p1) {
  s4 a = __builtin_amdgcn_ds_read_tr16_b64_v4i16((LAS s4*)p0);
  s4 b = __builtin_amdgcn_ds_read_tr16_b64_v4i16((LAS s4*)p1);
  v2u x = __builtin_bit_cast(v2u, a), y = __builtin_bit_cast(v2u, b);
  return __builtin_bit_cast(bf8, (v4u){x.x, x.y, y.x, y.y});
}
DI f16v mfma32(bf8 a, bf8 b, f16v c) { return __builtin_amdgcn_mfma_f32_32x32x16_bf16(a, b, c, 0, 0, 0); }
DI f4 mfma16(bf8 a, bf8 b, f4 c) { return __builtin_amdgcn_mfma_f32_16x16x32_bf16(a, b, c, 0, 0, 0); }

template <int I, int N, class F> DI void static_for(F&& f) {
  if constexpr (I < N) { f(std::integral_constant<int, I>{}); static_for<I + 1, N>(f); }
}

constexpr u32 REG = 20480;
constexpr int wblk(int t) { constexpr int b[16] = {6, 7, 8, 9, 6, 7, 8, 9, 6, 7, 4, 5, 8, 0, 1, 9}; return b[t]; }
constexpr int rblk(int k) { constexpr int b[10] = {2, 3, 6, 7, 4, 5, 8, 0, 1, 9}; return b[k % 10]; }
constexpr u32 w2row(int k) { return 64 * (k / 14) + 8 * (k & 1); }

enum { OX, OW, OR, OS, OE, OH, OP, OA, OF };
struct Op { int pos, kind, idx, sub; };
struct OpTab {
  Op op[256]; int n;
  constexpr void add(int pos, int kind, int idx, int sub) { op[n] = Op{pos, kind, idx, sub}; ++n; }
  constexpr int at(int kind, int idx, int sub) const {
    for (int i = 0; i < n; ++i) if (op[i].kind == kind && op[i].idx == idx && op[i].sub == sub) return i;
    return -1;
  }
  constexpr int from(int pos) const { int i = 0; while (i < n && op[i].pos < pos) ++i; return i; }
  constexpr int younger(int kind, int idx, int sub, int pos) const {
    int c = 0, a = at(kind, idx, sub);
    if (a < 0 || op[a].pos >= pos) return -1000;
    for (int i = a + 1; i < n; ++i) if (op[i].pos < pos) ++c;
    return c > 63 ? 63 : c;
  }
  constexpr int lds(int i) const {
    const Op o = op[i];
    return o.kind == OX ? (o.idx % 3) * 4096 + (o.sub >> 1) * 2048 : o.kind == OW ? wblk(o.idx) * 2048 : o.kind == OR ? rblk(o.idx) * 2048 : -1;
  }
  constexpr int m0_at(int pos) const {
    int cur = -1;
    for (int i = 0; i < n; ++i) if (lds(i) >= 0) { if (op[i].pos == pos) return lds(i) != cur ? lds(i) : -1; cur = lds(i); }
    return -1;
  }
  constexpr bool m0_ok() const {
    for (int i = 0; i < n; ++i) for (int j = i + 1; j < n; ++j)
      if (op[i].pos == op[j].pos && op[i].pos >= 0 && lds(i) >= 0 && lds(j) >= 0 && lds(i) != lds(j)) return false;
    return true;
  }
};
constexpr int EP0 = 61, G2P = 200;
constexpr int mn(int a, int b) { return a < b ? a : b; }
template <bool AUX> constexpr OpTab optab() {
  OpTab t{};
  int nh = 0;
  if (!AUX) {
    for (int c = 0; c < 3; ++c) { for (int s = 0; s < 4; ++s) t.add(-1, OX, c, s); t.add(-1, OW, c, 0); t.add(-1, OW, c, 1); }
    t.add(-1, OW, 3, 0); t.add(-1, OW, 3, 1);
    for (int n = 0; n < 128; ++n) {
      const int T = (n + LAM) / 8, i = (n + LAM) % 8;
      if (T >= 1 && T <= 5) { if (i < 4) t.add(n, OX, T + 2, i); else if (i < 6) t.add(n, OW, T + 3, i - 4); }
      if (T >= 6 && T <= 7 && i < 6) t.add(n, OW, 9 + 3 * (T - 6) + i / 2, i & 1);
      if (T == 8 && i < 6) { if (i < 2) t.add(n, OW, 15, i); else t.add(n, OR, i / 2 - 1, i & 1); }
      if (T >= 9 && i < 2) t.add(n, OR, T - 7, i);
      for (int s = 0; s < 4; ++s) if (EP0 + (4 * s + 3) / UPP == n) t.add(n, OS, s, 0);
      if (n == EP0 + 15 / UPP + 1) t.add(n, OE, 0, 0);
      if (PFP && (n == PFP || n == PFP + 1)) t.add(n, OF, 0, n - PFP);
      if (n == PA) t.add(n, OP, 0, 0);
      if (n >= PC && nh < 2 * PD) { t.add(n, OH, nh >> 1, nh & 1); ++nh; }
    }
    for (; nh < 2 * PD; ++nh) t.add(128, OH, nh >> 1, nh & 1);
    for (int s = 4; s < 8; ++s) t.add(129 + s, OS, s, 0);
  } else {
    for (; nh < 2 * PD; ++nh) t.add(-1, OH, nh >> 1, nh & 1);
  }
  int na = 0;
  for (int k = 0; k < 28 - NT; ++k)
    for (int g = 0; g < 4; ++g) {
      const int pos = G2P + 4 * k + g;
      const bool ring = (g == 1 || g == 2) && k + 10 < 28;
      if (ring) t.add(pos, OR, k + 10, g - 1);
      if (k == KP && g == 0) t.add(pos, OP, 1, 0);
      const int lim = mn(k < KC ? 28 : 56, 2 * (k + PD) + (g == 3 ? 2 : 1));
      int room = ring ? 0 : 1;
      if (k >= KC && nh + 2 < lim) ++room;
      for (; room > 0 && nh < lim; --room, ++nh) t.add(pos, OH, nh >> 1, nh & 1);
      if (k >= KA && na < 6 && room > 0) { t.add(pos, OA, 0, na); ++na; }
    }
  return t;
}
constexpr OpTab GT[2] = {optab<false>(), optab<true>()};
constexpr int g1wait(int T) {
  const int pos = T ? 8 * T - LAM : 0;
  return mn(GT[0].younger(OW, T, 1, pos), T < 8 ? GT[0].younger(OX, T, 3, pos) : 63);
}
template <bool AUX> constexpr int g2wait(int k) {
  const int pos = G2P + 4 * k, kl = k == 27 - NT ? 27 : k + 1;
  return mn(GT[AUX].younger(OH, kl, 1, pos), (AUX && kl < 10) ? 63 : GT[AUX].younger(OR, kl, 1, pos));
}
template <bool AUX> constexpr bool sched_ok() {
  for (int k = 0; k < 28 - NT; ++k) if (g2wait<AUX>(k) < 0) return false;
  for (int T = 0; T < 16; ++T) if (g1wait(T) < 0) return false;
  return GT[AUX].m0_ok() && GT[AUX].younger(OA, 0, 5, G2P + 4 * KB) >= 0 && GT[AUX].younger(OP, 1, 0, G2P + 4 * KC) >= 0;
}
static_assert(sched_ok<false>() && sched_ok<true>() && GT[0].at(OW, 15, 1) < GT[0].at(OS, 0, 0), "schedule");
static_assert(KP < KC && KC + 1 < 14 && KB < 28 - NT && KB <= 25 && KB >= 19 && NT >= 2 && NT <= 4, "schedule");

template <int N> DI void wait_pin(v4u& a, v4u& b) { asm volatile("s_waitcnt vmcnt(%2)" : "+v"(a), "+v"(b) : "n"(N) : "memory"); }
template <int N> DI void wait_pin(v2u& a) { asm volatile("s_waitcnt vmcnt(%1)" : "+v"(a) : "n"(N) : "memory"); }
template <int N> DI void wait_pin(u32& a) { asm volatile("s_waitcnt vmcnt(%1)" : "+v"(a) : "n"(N) : "memory"); }
template <int N> DI void wait_pin(v4u (&a)[6]) {
  asm volatile("s_waitcnt vmcnt(%6)" : "+v"(a[0]), "+v"(a[1]), "+v"(a[2]), "+v"(a[3]), "+v"(a[4]), "+v"(a[5]) : "n"(N) : "memory");
}
DI void pin(v4u& a, v4u& b) { asm volatile("" : "+v"(a), "+v"(b)); }
template <int I> using IC = std::integral_constant<int, I>;

DI void touch(u32& d, u32 vo, rsrc_t rs, u32 so = 0) { asm volatile("buffer_load_dword %0, %1, %2, %3 offen" : "+v"(d) : "v"(vo), "s"(rs), "s"(so) : "memory"); }
DI void touch_ws(u32& d, u32 vo, rsrc_t rs) { asm volatile("buffer_load_dword %0, %1, %2, 0 offen " PFQ : "+v"(d) : "v"(vo), "s"(rs) : "memory"); }
DI u32 late_row(u32 i) {
  const u32 hf = i >= 576, m = i - 576 * hf;
  return 896 * hf + (m < 128 ? 640 + 2 * (m & 64) + (m & 63) : 2 * ((m - 128) & ~63u) + 64 + (m & 63));
}

template <bool AUX>
DI void gemm_role(int b, LAS char* sm, const u16* x, const u16* w1, const u16* b1, const u16* w2, const u16* b2, const u16* gw,
                  u16* out, char* ws) {
  constexpr const OpTab& TB = GT[AUX];
  [[maybe_unused]] constexpr int role_ = AUX;
  const int lane = threadIdx.x & 63, wave = __builtin_amdgcn_readfirstlane(threadIdx.x >> 6);
  STAMP_DECL;
  STAMP(0);
  const int xcd = b & 7, r = b >> 3;
  const rsrc_t rws = mkrsrc(ws, WS_END), rx = mkrsrc(x, 2048 * 512 * 2), rw2 = mkrsrc(w2, 1792 * 512 * 2), rb2 = mkrsrc(b2, 512 * 2);
  const rsrc_t rw1 = mkrsrc(w1, 512 * 1792 * 2);
  v2u ep = ld64_sc1(0, rws);
  const int g = lane >> 4, q = (lane >> 2) & 3, p = lane & 3, l31 = lane & 31, hh = lane >> 5, l3 = lane >> 3, l7 = lane & 7;
  const u32 mreg = (u32)(uintptr_t)sm + wave * REG;
  const int t2 = 4 * xcd + (r >> 3), c2 = (r & 7) ^ C2X;
  const int tau = 4 * (xcd >> 1) + r / 7, j = 7 * (xcd & 1) + r % 7, rot = (r % 7) + 2 * (r / 7);

  const u32 wch = ((l7 ^ ((g & 1) << 2)) << 4);
  const u32 vx0 = (u32)(128 * tau + 32 * wave + l3) * 1024 + ((l7 ^ g) << 4), vx1 = (vx0 ^ 64) + 7168;
  const u32 vw0 = (u32)(16 * wave + l3) * 3584 + j * 256 + wch, vw1 = vw0 + 8 * 3584 - 1024;
  const u32 vv0 = (u32)(896 * (wave >> 1) + 32 * (wave & 1) + l3) * 1024 + c2 * 128 + wch, vv1 = vv0 + 15360;
  const u32 vh = WS_H + (u32)((4 * tau + wave) * 112 + 8 * j) * 1024 + lane * 16;
  const u32 vhh = WS_H + (u32)(2 * t2 * 112 + 56 * (wave >> 1) + 2 * (wave & 1)) * 1024 + lane * 16;
  const u32 vp = vhh + (u32)((l3 & 1) * 112 + 8 * min(l7, 6) + 1) * 1024;
  const u32 va = WS_A + (u32)t2 * 131072 + (u32)(32 * c2 + 4 * l3) * 512 + (u32)(16 * wave + 2 * l7) * 8;
  const u32 vg = WS_G + (u32)(64 * t2 + 16 * wave + 2 * l3) * 8;
  const u32 vf0 = late_row(32 * r + 8 * wave + l3) * 1024 + l7 * 128, vf1 = lane < 8 ? late_row(1024 + 4 * r + wave) * 1024 + lane * 128 : 1u << 30;
  u32 tch = 0;
  const int rot2 = ROT2 ? (c2 + 2 * (r >> 3)) % 7 : 0;
  auto prod = [&](int k) { const int q_ = ((k % 14) >> 1) + rot2; return (u32)(q_ >= 7 ? q_ - 7 : q_); };
  u32 aoff[2];
#pragma unroll
  for (int mi = 0; mi < 2; ++mi) aoff[mi] = (8 * (g >> 1) + q) * 128 + (((4 * mi + 2 * (p & 1) + (g & 1)) ^ ((q >> 1) << 2)) << 4) + (p >> 1) * 8;

  u64 tag;
  u32 sg, pf;
  v4u hd[8], hb[28][2], av[6];
  auto hload = [&](auto K, auto S, auto AGAIN) {
    constexpr int k = decltype(K)::value, s = decltype(S)::value;
    const u32 so = prod(k) * 8192 + (k / 14) * 4096 + s * 114688;
    if constexpr (decltype(AGAIN)::value) reld128_sc1<(k & 1) * 1024>(hb[k][s], vhh, rws, so);
    else hb[k][s] = ld128_sc1<(k & 1) * 1024>(vhh, rws, so);
  };
  auto aload = [&](auto S) {
    constexpr int s = decltype(S)::value;
    if constexpr (s < 4) av[s] = ld128_sc1<s * 512>(va, rws, 0);
    else if constexpr (s == 4) av[4] = ld128_sc1<0>(vg, rws, 0);
    else av[5] = ld128((u32)(64 * c2 + 8 * l7) * 2, rb2);
  };
  auto issue = [&](auto I) {
    constexpr Op o = TB.op[decltype(I)::value];
    constexpr int lo = (o.sub & 1) * 1024;
    if constexpr (o.pos < 0 && TB.lds(decltype(I)::value) >= 0 && lo == 0) { set_m0<TB.lds(decltype(I)::value)>(mreg); asm volatile("s_nop 0"); }
    if constexpr (o.kind == OX) dma1<lo>(lo ? vx1 : vx0, rx, (u32)((o.idx + rot) & 7) * 128 + (o.sub >> 1) * 16384);
    if constexpr (o.kind == OW) dma1<lo>(lo ? vw1 : vw0, rw1, (u32)((o.idx + rot) & 7) * (64 * 3584) + (o.idx >> 3) * 128);
    if constexpr (o.kind == OR) dma1<lo>(lo ? vv1 : vv0, rw2, prod(o.idx) * 131072 + w2row(o.idx) * 1024);
    if constexpr (o.kind == OS) st128_sc1<(o.idx & 3) * 1024>(hd[o.idx], vh, rws, (o.idx >> 2) * 4096);
    if constexpr (o.kind == OE) flag_store(WS_EFLAG + (u32)b * 8, rws, tag);
    if constexpr (o.kind == OF) touch(tch, o.sub ? vf1 : vf0, rw2);
    if constexpr (o.kind == OP) pf = ld32_sc1<0>(vp, rws, o.idx * 4096);
    if constexpr (o.kind == OH) hload(IC<o.idx>{}, IC<o.sub>{}, IC<0>{});
    if constexpr (o.kind == OA) aload(IC<o.sub>{});
    if constexpr (o.pos < 0 && o.kind == OW && o.sub == 1 && o.idx < 2) asm volatile("s_barrier");
  };
  auto issue_at = [&](auto N) {
    static_for<TB.from(decltype(N)::value), TB.from(decltype(N)::value + 1)>(issue);
  };
  auto m0_for = [&](auto N) { if constexpr (TB.m0_at(decltype(N)::value) >= 0) set_m0<TB.m0_at(decltype(N)::value)>(mreg); };
  auto gate = [&](int ph) {
    if (__builtin_expect(any((pf ^ sg) & SGN), 0)) {
#pragma unroll 1
      for (int it = 0; it < SPIN_MAX; ++it) {
        __builtin_amdgcn_s_sleep(1);
        pf = ld32_sc1<0>(vp, rws, ph * 4096);
        wait_pin<0>(pf);
        if (!any((pf ^ sg) & SGN)) break;
      }
    }
  };

  if constexpr (!AUX) {
    const rsrc_t rb1 = mkrsrc(b1, 1792 * 2);
    const u32 vb = (u32)(128 * j + 32 * hh + 16 * ((l31 >> 2) & 1) + 4 * (l31 >> 3) + p) * 2;
    u32 bq0 = ld16<0>(vb, rb1), bq1 = ld16<128>(vb, rb1);
    f16v acc[4];
    issue_at(IC<-1>{});
    __builtin_amdgcn_s_setprio(PRG1);

    LAS char* pa[2][2]; LAS char* pb[4];
#pragma unroll
    for (int mi = 0; mi < 2; ++mi) { pa[mi][0] = sm + aoff[mi]; pa[mi][1] = sm + aoff[mi] + 2 * REG; }
#pragma unroll
    for (int u = 0; u < 4; ++u) pb[u] = sm + wave * REG + l31 * 128 + (((2 * u + hh) ^ ((l31 >> 1) & 7)) << 4);

    asm volatile("s_waitcnt vmcnt(20)" : "+v"(ep), "+v"(bq0), "+v"(bq1) :: "memory");
    tag = epoch_tag(ep);
    sg = sign_of(tag, l31);
    {
      const bf8 o0 = __builtin_bit_cast(bf8, (v4u){hh ? 0u : 0x3f80u, 0, 0, 0}), o1 = __builtin_bit_cast(bf8, (v4u){hh ? 0x3f80u : 0u, 0, 0, 0});
      const bf8 a0 = __builtin_bit_cast(bf8, (v4u){bq0, 0, 0, 0}), a1 = __builtin_bit_cast(bf8, (v4u){bq1, 0, 0, 0});
      acc[0] = mfma32(a0, o0, (f16v)(0.f)); acc[1] = mfma32(a0, o1, (f16v)(0.f));
      acc[2] = mfma32(a1, o0, (f16v)(0.f)); acc[3] = mfma32(a1, o1, (f16v)(0.f));
    }
    constexpr auto ms = [](int n) constexpr { const int T = n >> 3, v = n & 7; return (T & 7) == 7 ? (v & 3) : (v >> 1); };
    constexpr auto mt = [](int n) constexpr { const int T = n >> 3, v = n & 7; return (T & 7) == 7 ? (v >> 2) : (v & 1); };
    bf8 af[128], xf[32];
    auto rda = [&](auto N) {
      constexpr int n = decltype(N)::value, u = ms(n);
      LAS char* a = pa[mt(n)][u >> 1] + (u & 1) * REG + wblk(n >> 3) * 2048;
      af[n] = lds_tr2(a, a + 512);
    };
    auto rdx = [&](auto S) { constexpr int s = decltype(S)::value; xf[s] = lds_rd128(pb[s & 3] + ((s >> 2) % 3) * 4096); };
    auto unit = [&](auto U) {
      constexpr int un = decltype(U)::value, tl = un >> 3, e = (un & 7) * 2;
      const float v0 = relu(acc[tl][e]), v1 = relu(acc[tl][e + 1]);
      hd[un >> 2][un & 3] = (pk2(opaque(v0 * v0), opaque(v1 * v1)) & ~SGN) | sg;
    };
    STAMP(1);
    fence_sched();
    wait_vm_bar<g1wait(0)>();
    fence_sched();
    STAMP(2); STAMPC(60);
    static_for<0, 4>(rdx);
    static_for<0, LAM>(rda);
    static_for<0, 128>([&](auto N) {
      constexpr int n = decltype(N)::value, T = (n + LAM) / 8, i = (n + LAM) % 8;
      fence_sched();
      if constexpr (i == 0) { if constexpr (T < 16) wait_vm_bar<g1wait(T)>(); else lds_bar(); }
      if constexpr (i == 0) { STAMPQ(15 + T); }
      if constexpr (n == PC) { wait_pin<TB.younger(OP, 0, 0, n)>(pf); asm volatile("" :: "v"(tch)); gate(0); }
      m0_for(N);
      fence_sched();
      acc[2 * (n >> 6) + mt(n)] = mfma32(af[n], xf[(4 * (n >> 3) + ms(n)) & 31], acc[2 * (n >> 6) + mt(n)]);
      fence_sched();
      if constexpr (n + LAM < 128) rda(IC<n + LAM>{});
      if constexpr (T >= 1 && T < 8 && i < 4) rdx(IC<4 * T + i>{});
      if constexpr (n >= EP0 && n < EP0 + 16 / UPP) static_for<UPP * (n - EP0), UPP * (n - EP0 + 1)>(unit);
      if constexpr (n >= 64 + EP0) static_for<16 + UPP * (n - 64 - EP0), 16 + UPP * (n - 64 - EP0 + 1)>(unit);
      fence_sched();
      issue_at(N);
    });
    fence_sched();
    STAMP(5); STAMPC(61);
    issue_at(IC<128>{});
    static_for<16, 32>([&](auto U) {
      constexpr int un = decltype(U)::value;
      if constexpr (64 + EP0 + (un & 15) / UPP > 127) unit(U);
      if constexpr ((un & 3) == 3) { fence_sched(); issue_at(IC<129 + (un >> 2)>{}); fence_sched(); }
    });
    STAMP(6);
  } else {
    const int tg = xcd * 4 + (r - 28);
    const v4u gv = *(const v4u*)(gw + lane * 8);
    const u16* xr = x + (size_t)(tg * 64 + wave * 16) * 512 + lane * 8;
    float s[16];
#pragma unroll
    for (int i = 0; i < 16; ++i) {
      const v4u xv = *(const v4u*)(xr + i * 512);
      s[i] = 0.f;
#pragma unroll
      for (int k = 0; k < 4; ++k) s[i] = dot2(xv[k], gv[k], s[i]);
    }
#pragma unroll
    for (int i = 0; i < 8; ++i) {
      const v2u t = __builtin_amdgcn_permlane32_swap(__float_as_uint(s[i]), __float_as_uint(s[i + 8]), false, false);
      s[i] = opaque(__uint_as_float(t.x) + __uint_as_float(t.y));
    }
#pragma unroll
    for (int i = 0; i < 4; ++i) {
      const v2u t = __builtin_amdgcn_permlane16_swap(__float_as_uint(s[i]), __float_as_uint(s[i + 4]), false, false);
      float v = __uint_as_float(t.x) + __uint_as_float(t.y);
      v += __int_as_float(__builtin_amdgcn_update_dpp(0, __float_as_int(v), 0x128, 0xf, 0xf, false));
      v += __int_as_float(__builtin_amdgcn_update_dpp(0, __float_as_int(v), 0x124, 0xf, 0xf, false));
      v += __int_as_float(__builtin_amdgcn_update_dpp(0, __float_as_int(v), 0x122, 0xf, 0xf, false));
      v += __int_as_float(__builtin_amdgcn_update_dpp(0, __float_as_int(v), 0x121, 0xf, 0xf, false));
      s[i] = opaque(v);
    }
    const float m01 = opaque(p & 1 ? s[1] : s[0]), m23 = opaque(p & 1 ? s[3] : s[2]), mine = p & 2 ? m23 : m01;
    const float sl = bflo(pk2(mine, 0.f));
    const float gt = bflo(pk2(1.f / (1.f + __expf(-sl)), 0.f));
    fence_sched();
    asm volatile("s_waitcnt vmcnt(0)\n\ts_barrier" : "+v"(ep) :: "memory");
    tag = epoch_tag(ep);
    sg = sign_of(tag, l31);
    if (q == 0) st64_sc1((v2u){__float_as_uint(gt), (u32)(tag >> 32)}, WS_G + (u32)(tg * 64 + wave * 16 + 4 * g + p) * 8, rws);
    STAMP(1);
    static_for<0, 10>([&](auto I) { constexpr int i = decltype(I)::value; dma2(mreg + rblk(i) * 2048, vv0, vv1, rw2, prod(i) * 131072 + w2row(i) * 1024); });
    flag_store(WS_EFLAG + (u32)b * 8, rws, tag);
    if (PFP) { touch(tch, vf0, rw2); touch(tch, vf1, rw2); }
    STAMP(2);
    pf = ld32_sc1<0>(vp, rws, 0);
    wait_pin<0>(pf);
    asm volatile("" :: "v"(tch));
    gate(0);
    STAMP(4);
    issue_at(IC<-1>{});
  }
  __builtin_amdgcn_s_setprio(PRG2);

  const u32 tg32 = (u32)(tag >> 32);
  LAS char* pv[2] = {sm + wave * REG + aoff[0], sm + wave * REG + aoff[1]};
  auto vfrag = [&](int k, int mi) { LAS char* a = pv[mi] + rblk(k) * 2048; return lds_tr2(a, a + 512); };
  auto fresh = [&](auto K0, auto K1, u32 m) {
    constexpr int k0 = decltype(K0)::value, k1 = decltype(K1)::value, ke = mn(k1 | 1, 27);
    if (__builtin_expect(any(m & SGN), 0)) {
#pragma unroll 1
      for (int it = 0; it < SPIN_MAX; ++it) {
        static_for<k0, ke + 1>([&](auto K) { hload(K, IC<0>{}, IC<1>{}); hload(K, IC<1>{}, IC<1>{}); });
        wait_vm<0>();
        u32 z = 0;
        static_for<k0, ke + 1>([&](auto K) { constexpr int k = decltype(K)::value; pin(hb[k][0], hb[k][1]); if constexpr (k <= k1) z |= strip(hb[k][0], sg) | strip(hb[k][1], sg); });
        if (!any(z & SGN)) break;
        __builtin_amdgcn_s_sleep(1);
      }
    }
  };
  auto abad = [&]() {
    u32 m = 0;
#pragma unroll
    for (int i = A0; i < 5; ++i) m |= (av[i].y ^ tg32) | (av[i].w ^ tg32);
    return any(m);
  };
  LAS char* ap = sm + wave * REG + 9 * 2048;
  v4u a4[2];
  float ga[2][8], gs[2];
  auto aput = [&]() {
    *(LAS v4u*)(ap + l7 * 128 + ((l3 ^ l7) << 4)) = (v4u){av[0].x, av[1].x, av[2].x, av[3].x};
    *(LAS v4u*)(ap + l7 * 128 + ((l3 ^ l7) << 4) + 1024) = (v4u){av[0].z, av[1].z, av[2].z, av[3].z};
    a4[0] = *(LAS v4u*)(ap + l3 * 128 + ((l3 ^ l7) << 4));
    a4[1] = *(LAS v4u*)(ap + l3 * 128 + ((l3 ^ l7) << 4) + 1024);
  };
  auto amix = [&](int e, int k) {
    const float gt = __uint_as_float(e ? av[4].z : av[4].x), gn = opaque(1.f - gt);
    ga[e][2 * k] = opaque(__builtin_fmaf(gt, bflo(a4[e][k]), opaque(gn * bflo(av[5][k]))));
    ga[e][2 * k + 1] = opaque(__builtin_fmaf(gt, bfhi(a4[e][k]), opaque(gn * bfhi(av[5][k]))));
    if (k == 0) gs[e] = gn;
  };
  f16v o2[2][2];
  bf8 wb[28][2];
  bool late = false;
  fence_sched();
  wait_pin<mn(TB.younger(OH, 0, 1, G2P), AUX ? 63 : TB.younger(OR, 0, 1, G2P))>(hb[0][0], hb[0][1]);
  wb[0][0] = vfrag(0, 0); wb[0][1] = vfrag(0, 1);
  fresh(IC<0>{}, IC<0>{}, strip(hb[0][0], sg) | strip(hb[0][1], sg));
  STAMP(7);
  static_for<0, 28 - NT>([&](auto K) {
    constexpr int k = decltype(K)::value, kl = k == 27 - NT ? 27 : k + 1;
    fence_sched();
    wait_vm<g2wait<AUX>(k)>();
    static_for<k + 1, kl + 1>([&](auto J) { constexpr int jn = decltype(J)::value; pin(hb[jn][0], hb[jn][1]); });
    if constexpr (k == KC) { wait_pin<TB.younger(OP, 1, 0, G2P + 4 * k)>(pf); gate(1); }
    if constexpr (k == KB) { wait_pin<TB.younger(OA, 0, 5, G2P + 4 * k)>(av); }
    STAMPQ(32 + k);
    u32 m = 0;
    static_for<0, 4>([&](auto G) {
      constexpr int gi = decltype(G)::value, mi = gi & 1, ni = gi >> 1;
      m0_for(IC<G2P + 4 * k + gi>{});
      fence_sched();
      if constexpr (k == 0) o2[mi][ni] = mfma32(wb[k][mi], __builtin_bit_cast(bf8, hb[k][ni]), (f16v)(0.f));
      else o2[mi][ni] = mfma32(wb[k][mi], __builtin_bit_cast(bf8, hb[k][ni]), o2[mi][ni]);
      fence_sched();
      if constexpr (gi < 2) static_for<k + 1, kl + 1>([&](auto J) {
        constexpr int jn = decltype(J)::value;
        wb[jn][gi] = vfrag(jn, gi);
        m |= strip(hb[jn][gi], sg);
      });
      if constexpr (k == KB && gi == 0) aput();
      if constexpr (k == KB && gi == 2) late = abad();
      if constexpr (k > KB && k <= KB + 2) amix(k - KB - 1, gi);
      fence_sched();
      issue_at(IC<G2P + 4 * k + gi>{});
      if constexpr (gi == 2) fresh(IC<k + 1>{}, IC<kl>{}, m);
    });
  });
  fence_sched();
  LAS char* pw = sm + wave * REG + l31 * 256;
  const int kw = (4 * hh) ^ (((l31 >> 1) & 3) | ((l31 & 1) << 2));
  auto pput = [&](int tl, int i) {
    const f16v& a = o2[tl >> 1][tl & 1];
    *(LAS f4*)(pw + ((kw ^ i) << 4) + (tl >> 1) * 128 + (tl & 1) * 8192) = (f4){a[4 * i], a[4 * i + 1], a[4 * i + 2], a[4 * i + 3]};
  };
  static_for<0, 4 * NT>([&](auto N) {
    constexpr int n = decltype(N)::value, tl = n / NT, v = n % NT, k = 28 - NT + v;
    o2[tl >> 1][tl & 1] = mfma32(wb[k][tl >> 1], __builtin_bit_cast(bf8, hb[k][tl & 1]), o2[tl >> 1][tl & 1]);
    fence_sched();
    if constexpr (constexpr int u = 4 * (27 - NT - KB) + n; u < 8) amix(u >> 2, u & 3);
    if constexpr (tl >= 1 && v == NT - 1) { pput(tl - 1, 0); pput(tl - 1, 1); pput(tl - 1, 2); pput(tl - 1, 3); }
    fence_sched();
  });
  STAMP(10); STAMPC(62);
  if (__builtin_expect(late, 0)) {
#pragma unroll 1
    for (int it = 0; it < SPIN_MAX; ++it) {
      static_for<0, 5>(aload);
      wait_pin<0>(av);
      if (!abad()) break;
      __builtin_amdgcn_s_sleep(1);
    }
    aput();
#pragma unroll
    for (int i = 0; i < 8; ++i) amix(i >> 2, i & 3);
  }
#pragma unroll
  for (int i = 0; i < 4; ++i) pput(3, i);
  lds_bar();
  STAMP(11);
  if constexpr (!AUX) asm volatile(".Lpf_end:");
  {
    LAS char* pr = sm + (16 * wave + 2 * l3) * 256;
    const int kr = (2 * l7) ^ (l3 & 3);
#pragma unroll
    for (int e = 0; e < 2; ++e) {
      v4u d;
#pragma unroll
      for (int cc = 0; cc < 2; ++cc) {
        LAS char* a = pr + e * 256 + ((kr ^ cc ^ (4 * e)) << 4);
        const f4 s = (*(LAS f4*)(a) + *(LAS f4*)(a + REG)) + (*(LAS f4*)(a + 2 * REG) + *(LAS f4*)(a + 3 * REG));
#pragma unroll
        for (int k = 0; k < 2; ++k)
          d[2 * cc + k] = pk2(opaque(__builtin_fmaf(gs[e], s[2 * k], ga[e][4 * cc + 2 * k])), opaque(__builtin_fmaf(gs[e], s[2 * k + 1], ga[e][4 * cc + 2 * k + 1])));
      }
      *(v4u*)(out + (size_t)(64 * t2 + 16 * wave + 2 * l3 + e) * 512 + 64 * c2 + 8 * l7) = d;
    }
  }
  STAMP(12);
  STAMP_OUT;
}

struct cf { float x, y; };
DI cf cmul(cf a, cf m) { return cf{opaque(__builtin_fmaf(a.x, m.x, -opaque(a.y * m.y))), opaque(__builtin_fmaf(a.x, m.y, opaque(a.y * m.x)))}; }
DI cf cexp_s(float lr, float rv, float m) {
  const float e = __builtin_amdgcn_exp2f(lr * m), f = __builtin_amdgcn_fractf(rv * m);
  return cf{opaque(e * __builtin_amdgcn_cosf(f)), opaque(e * __builtin_amdgcn_sinf(f))};
}

constexpr u32 TROW = 544, TCH = 63 * TROW, XT = 69632;
constexpr u32 KCH = 9472, KOO = 4656, UA0 = 18944, UCH = 8768, UBO = 4224, EX = 40960;

constexpr int NPF = !!(PF & 1) + !!(PF & 2) + 2 * !!(PF & 4) + !!(PF & 8) + !!(PF & 16);

DI void conv_role(int ci, LAS char* sm, const u16* x, const u16* are, const u16* aim, const u16* bre, const u16* bim,
                  const u16* cre, const u16* cim, const u16* logdt, const u16* dskip, const u16* w1, const u16* w2, char* ws) {
  const int tid = threadIdx.x, lane = tid & 63, wave = __builtin_amdgcn_readfirstlane(tid >> 6);
  [[maybe_unused]] constexpr int role_ = 2;
  STAMP_DECL;
  STAMP(0);
  const rsrc_t rws = mkrsrc(ws, WS_END), rx = mkrsrc(x, 2048 * 512 * 2);
  const u64 tst = HOLD >= 0 ? __builtin_amdgcn_s_memrealtime() : 0;
  HOLDPT(0);
  if (CDLY) __builtin_amdgcn_s_sleep(CDLY);
  v2u ep = ld64_sc1(0, rws);
  u32 tch = 0;
  {
    u64 pc; int len;
    asm volatile("s_getpc_b64 %0\n.Lpf_here:\n\ts_max_i32 %1, 0, .Lpf_end-.Lpf_here\n\ts_min_i32 %1, %1, 0x10000" : "=s"(pc), "=s"(len) :: "scc");
    touch(tch, lane < 4 ? (u32)((4 * (ci >> 3) + wave) * 4 + lane) * 128 : 1u << 30, mkrsrc((const void*)pc, (u32)len));
  }
  __builtin_amdgcn_s_setprio(PRC0);
  const u32 smb = (u32)(uintptr_t)sm;
  const int pr = (ci & 7) * 32 + (ci >> 3), cg = (ci >> 3) & 7, tt = 4 * (ci & 7) + (ci >> 6);
  const int g = lane >> 4, q = (lane >> 2) & 3, p = lane & 3, n = lane & 15;
  const int ch = wave >> 1, half = wave & 1;

  {
    const u32 v0 = (u32)(64 * tt + 16 * wave + (lane >> 3)) * 1024 + cg * 128 + (((lane & 7) ^ ((g & 1) << 1)) << 4);
    const u32 v1 = (v0 ^ 64) + 7168;
    dma2(smb + XT + wave * 2048, v0, v1, rx, 0);
  }
  const int pi = pr * 256 + tid;
  u32 r_are = are[pi], r_aim = aim[pi], r_bre = bre[pi], r_bim = bim[pi], r_cre = cre[pi], r_cim = cim[pi];
  u32 r_ldt = logdt[2 * pr + ch];
  u32 r_dsk = *(const u32*)(dskip + 2 * pr);
  fence_sched();
  asm volatile("s_waitcnt vmcnt(0)" : "+v"(r_are), "+v"(r_aim), "+v"(r_bre), "+v"(r_bim), "+v"(r_cre), "+v"(r_cim), "+v"(r_ldt), "+v"(r_dsk), "+v"(ep) :: "memory");
  const u64 tag = epoch_tag(ep);
  const u32 tg32 = (u32)(tag >> 32);
  fence_sched();
  STAMP(1);
  {
    LAS char* tb = sm + XT + wave * 2048 + (8 * (p & 1) + q) * 128 + (p >> 1) * 8;
    const int sw = (q & 2) | ((p & 1) << 2);
    const u32 vo = WS_UT + (u32)((16 * cg + 2 * g + (q >> 1)) * 256 + 8 * tt + 2 * wave + (q & 1)) * 64 + p * 16;
    const bf8 d0 = lds_tr2(tb + ((g ^ sw) << 4), tb + ((g ^ sw) << 4) + 512);
    const bf8 d1 = lds_tr2(tb + (((4 + g) ^ sw) << 4), tb + (((4 + g) ^ sw) << 4) + 512);
    st128_sc1<0>(__builtin_bit_cast(v4u, d0), vo, rws, 0);
    st128_sc1<0>(__builtin_bit_cast(v4u, d1), vo, rws, 8 * 256 * 64);
  }
  fence_sched();
  {
    const u32 wv = 4 * ci + wave, big = 1u << 30;
    if (PF & 1) touch(tch, lane < 7 ? (wv >> 1) * 3584 + ((wv & 1) * 7 + lane) * 256 + 128 : big, mkrsrc(w1, 512 * 1792 * 2));
    if (PF & 2) touch(tch, lane < 14 ? (wv * 14 + lane) * 128 : big, mkrsrc(w2, 512 * 1792 * 2));
    if (PF & 4) {
      const u32 wx = 4 * (ci >> 3) + wave;
      touch(tch, (wx * 112 + lane) * 128, mkrsrc(w2, 512 * 1792 * 2));
      touch(tch, lane < 48 ? (wx * 112 + 64 + lane) * 128 : big, mkrsrc(w2, 512 * 1792 * 2));
    }
    if (PF & 8) touch_ws(tch, lane < 56 ? WS_H + (wv * 56 + lane) * 128 : big, rws);
    if (PF & 16) touch_ws(tch, lane < 32 ? WS_A + (wv * 32 + lane) * 128 : big, rws);
  }
  fence_sched();

  HOLDPT(1);
  __builtin_amdgcn_s_setprio(PRC1);
  const u32 uf = WS_UFLAG + (u32)(cg * 128 + tt * 4 + wave) * FSP;
  {
    const float dt = bflo(pk2(expf(bflo(r_ldt)), 0.f));
    const float lr = dt * bflo(r_are) * 1.44269504088896341f, rv = dt * bflo(r_aim) * 0.15915494309189535f;
    const cf db = {opaque(dt * bflo(r_bre)), opaque(-dt * bflo(r_bim))};
    const cf cb = cmul(cf{bflo(r_cre), -bflo(r_cim)}, db);
    const cf e16 = cexp_s(lr, rv, 16.f), e1 = cexp_s(lr, -rv, 1.f), e256 = cexp_s(lr, -rv, 256.f);
    const cf e2 = cexp_s(lr, -rv, 2.f), e4 = cexp_s(lr, -rv, 4.f), e8 = cexp_s(lr, -rv, 8.f);
    LAS char* tw = sm + ch * TCH + (tid & 127) * 4;
    u32 tv[63];
    auto put = [&](int row, cf v) {
      if (BURST) tv[row] = pk2(v.x, v.y);
      else if (KO & 1) { const u32 q_ = pk2(v.x, v.y); asm volatile("" :: "v"(q_)); }
      else if (KO & 2) *(volatile LAS u32*)(tw + row * TROW) = r_are;
      else *(volatile LAS u32*)(tw + row * TROW) = pk2(v.x, v.y);
    };
    cf a = e16, t0 = cb, tb[8], v[8];
    put(0, cf{1.f, 0.f}); put(16, t0);
    tb[0] = cb;
#pragma unroll
    for (int i = 1; i < 16; ++i) {
      if (i == UFP) { wait_vm<NPF>(); flag_store(uf, rws, tag); }
      put(i, a); if (i < 15) a = cmul(a, e16);
      t0 = cmul(t0, e1); put(16 + i, t0);
      if (i < 8) { tb[i] = cmul(tb[i - 1], e256); v[i] = tb[i]; }
#pragma unroll
      for (int tl = 1; tl < 8; ++tl) {
        const int base = tl == 1 ? 32 : (tl < 4 ? 41 + 5 * (tl - 2) : 51 + 3 * (tl - 4));
        const int cnt = tl == 1 ? 9 : (tl < 4 ? 5 : 3), k = i - tl;
        if (k >= 0 && k < cnt) {
          if (k > 0) v[tl] = cmul(v[tl], tl == 1 ? e2 : (tl < 4 ? e4 : e8));
          put(base + k, v[tl]);
        }
      }
    }
    if (BURST) {
#pragma unroll
      for (int i = 0; i < 63; ++i) asm volatile("" : "+v"(tv[i]));
      fence_sched();
      __builtin_amdgcn_s_setprio(PRCB);
      asm volatile("s_mov_b32 m0, %0\n\ts_nop 0" :: "s"(smb + ch * TCH + half * 256) : "m0");
      static_for<0, 63>([&](auto R) { lds_put_tid<decltype(R)::value * TROW>(tv[decltype(R)::value]); });
      __builtin_amdgcn_s_setprio(PRC1);
    }
  }
  fence_sched();
  if (UFP >= 16) { wait_vm<NPF>(); flag_store(uf, rws, tag); }
  HOLDPT(2);
  const u32 vf = WS_UFLAG + (u32)((ci & 7) * 128 + wave * 32 + (lane & 31)) * FSP;
  v2u pf = ld64_sc1(vf, rws);
  STAMP(2);
  lds_bar();

  f4 kk[4];
  v4u uv[2];
  {
    LAS char* tb = sm + ch * TCH + g * 16;
    const int m2 = (n + ((n >> 1) & 1)) >> 1, m4 = (n + 1 + ((n >> 2) & 1)) >> 2, m8 = (n + 3 + ((n >> 3) & 1)) >> 3;
    LAS char* pr_[4] = {tb + (half ? 51 + m8 : 16 + n) * TROW, tb + (half ? 54 + m8 : 32 + m2) * TROW,
                        tb + (half ? 57 + m8 : 41 + m4) * TROW, tb + (half ? 60 + m8 : 46 + m4) * TROW};
    LAS char* pe = tb + n * TROW;
#pragma unroll
    for (int i = 0; i < 4; ++i) kk[i] = (f4)(0.f);
#pragma unroll
    for (int ks = 0; ks < 8; ++ks) {
      if (ks == 4) {
        fence_sched();
        wait_pin<0>(pf);
        asm volatile("" :: "v"(tch));
        if (__builtin_expect(flag_bad(pf, tag), 0)) flag_wait(vf, rws, tag);
        STAMP(3);
        const u32 vo = WS_UT + (u32)((pr >> 1) * 256 + 64 * wave + (lane >> 1)) * 64 + ((pr & 1) * 2 + (lane & 1)) * 16;
        uv[0] = ld128_sc1<0>(vo, rws, 0); uv[1] = ld128_sc1<2048>(vo, rws, 0);
        fence_sched();
      }
      if (KO & 4) continue;
      const bf8 e = lds_rd128(pe + ks * 64);
#pragma unroll
      for (int i = 0; i < 4; ++i) kk[i] = mfma16(lds_rd128(pr_[i] + ks * 64), e, kk[i]);
    }
  }
  fence_sched();
  STAMP(4);
  HOLDPT(3);
  lds_bar();
  if (KO & 8) asm volatile("" :: "v"(kk[0]), "v"(kk[1]), "v"(kk[2]), "v"(kk[3]));
  else {
    LAS char* kb = sm + ch * KCH;
#pragma unroll
    for (int i = 0; i < 4; ++i) {
      const int l0 = 256 * (4 * half + i) + 16 * n + 4 * g;
      const float k0 = opaque(kk[i][0]), k1 = opaque(kk[i][1]), k2 = opaque(kk[i][2]), k3 = opaque(kk[i][3]);
      const u32 a = pk2(k3, k2), bq = pk2(k1, k0), c = pk2(k2, k1);
      *(LAS v2u*)(kb + KOO + 2 * (2044 - l0)) = (v2u){a, bq};
      *(LAS u16*)(kb + 2 * (2051 - l0)) = (u16)a;
      *(LAS u32*)(kb + 2 * (2052 - l0)) = c;
      *(LAS u16*)(kb + 2 * (2054 - l0)) = (u16)(bq >> 16);
    }
    const v4u z = {0, 0, 0, 0};
    if (lane < 29) *(LAS v4u*)(kb + (half ? KOO + 4096 : 4112) + lane * 16) = z;
    else if (lane < 33) *(LAS v4u*)(sm + UA0 + ch * UCH + (half ? UBO : 0) + (lane - 29) * 16 - (half ? 0 : 16)) = z;
    else if (lane == 33 && !half) *(LAS u16*)(kb + 2 * 2055) = 0;
    else if (lane == 34 && !half) *(LAS v3u*)(sm + UA0 + ch * UCH + 48) = (v3u){0, 0, 0};
  }
  asm volatile("s_waitcnt vmcnt(0)" : "+v"(uv[0]), "+v"(uv[1]) :: "memory");
#pragma unroll
  for (int k = 0; k < 2; ++k) {
    if (KO & 8) continue;
    LAS char* ub = sm + UA0 + (lane & 1) * UCH + 64 + (64 * wave + 32 * k + (lane >> 1)) * 16;
    *(LAS v4u*)(ub + UBO) = uv[k];
    *(LAS u32*)(ub - 4) = uv[k].x;
    *(LAS v3u*)(ub) = (v3u){uv[k].y, uv[k].z, uv[k].w};
  }
  lds_bar();
  STAMP(5);
  flag_store(WS_EFLAG + (u32)(256 + ci) * 8, rws, tag);
  HOLDPT(4);
  const rsrc_t rw2 = mkrsrc(w2, 512 * 1792 * 2);
  const u32 wx = 4 * (ci >> 3) + wave, vt = lane < 8 ? (128 * (wx >> 6) + 64 + (wx & 63)) * 1024 + lane * 128 : 1u << 30;
  __builtin_amdgcn_s_setprio(PRCV);

  f4 y[8];
  if (KO & 16) { for (int i = 0; i < 8; ++i) y[i] = kk[i & 3]; }
  else {
    LAS char* pk = sm + ch * KCH + ((n & 1) ? KOO + 4032 : 4048) - 64 * (n >> 1) + 16 * g - 3584 + 256 * half;
    LAS char* pu = sm + UA0 + ch * UCH + (n < 8 ? 8 * n : UBO + 8 * (n - 8) + 8) + 16 * g + 256 * half;
    bf8 kf[4][8], uf[4][8];
    auto rd = [&](int rr, int i) {
      kf[rr][i] = lds_rd128(pk + 512 * (7 - i) + 64 * rr);
      const v2u lo = *(volatile LAS v2u*)(pu + 512 * i + 64 * rr), hi = *(volatile LAS v2u*)(pu + 512 * i + 64 * rr + 8);
      uf[rr][i] = __builtin_bit_cast(bf8, (v4u){lo.x, lo.y, hi.x, hi.y});
    };
#pragma unroll
    for (int i = 0; i < 8; ++i) { rd(0, i); fence_sched(); }
    static_for<0, 144>([&](auto N) {
      constexpr int nn = decltype(N)::value, rr = nn / 36, v = nn % 36;
      constexpr int s = v < 1 ? 0 : v < 3 ? 1 : v < 6 ? 2 : v < 10 ? 3 : v < 15 ? 4 : v < 21 ? 5 : v < 28 ? 6 : 7, m = v - s * (s + 1) / 2;
      if constexpr (rr == 0 && m == 0) y[s] = mfma16(kf[rr][s - m], uf[rr][m], (f4)(0.f));
      else y[s] = mfma16(kf[rr][s - m], uf[rr][m], y[s]);
      fence_sched();
      if constexpr (rr < 3 && v % 4 == 2 && v / 4 < 8) rd(rr + 1, v / 4);
      if constexpr ((PF & 64) && nn >= TK0 && (nn - TK0) % TKS == 0 && (nn - TK0) / TKS < 7) touch(tch, vt, rw2, (nn - TK0) / TKS * 262144);
      fence_sched();
    });
  }
  STAMP(6);
  HOLDPT(5);
#pragma unroll
  for (int a = 0; a < 8; ++a) *(LAS f4*)(sm + EX + (wave * 8 + a) * 1024 + lane * 16) = y[a];
  lds_bar();
  {
    const float d0 = bflo(r_dsk), d1 = bfhi(r_dsk);
    const int rn = n < 8 ? 2 * n : 2 * (n - 8) + 1;
#pragma unroll
    for (int i = 0; i < 2; ++i) {
      const int al = 2 * wave + i;
      LAS char* ex = sm + EX + al * 1024 + lane * 16;
      const f4 y0 = *(LAS f4*)(ex) + *(LAS f4*)(ex + 8192), y1 = *(LAS f4*)(ex + 16384) + *(LAS f4*)(ex + 24576);
      LAS char* up = sm + UA0 + UBO + 64 + (256 * al + 2 * rn + 64 * g) * 2;
      const u32 u00 = *(LAS u32*)(up), u01 = *(LAS u32*)(up + 64), u10 = *(LAS u32*)(up + UCH), u11 = *(LAS u32*)(up + UCH + 64);
      auto mix = [&](float y, float u, float d) { return opaque(__builtin_fmaf(u, d, opaque(y))); };
      const u32 x0 = pk2(mix(y0[0], bflo(u00), d0), mix(y1[0], bflo(u10), d1)), x1 = pk2(mix(y0[1], bfhi(u00), d0), mix(y1[1], bfhi(u10), d1));
      const u32 z0 = pk2(mix(y0[2], bflo(u01), d0), mix(y1[2], bflo(u11), d1)), z1 = pk2(mix(y0[3], bfhi(u01), d0), mix(y1[3], bfhi(u11), d1));
      const u32 vo = WS_A + (u32)(4 * al + g) * 131072 + pr * 512 + rn * 16;
      st128_sc1<0>((v4u){x0, tg32, x1, tg32}, vo, rws, 0);
      st128_sc1<256>((v4u){z0, tg32, z1, tg32}, vo, rws, 0);
    }
  }
  STAMP(7);
  if (PF & 64) asm volatile("s_waitcnt vmcnt(4)" :: "v"(tch) : "memory");
  if (ci == 0 && wave == 0) {
#pragma unroll 1
    for (int it = 0; it < SPIN_MAX; ++it) {
      v4u f[4];
      static_for<0, 4>([&](auto I) { f[decltype(I)::value] = ld128_sc1<decltype(I)::value * 1024>(WS_EFLAG + lane * 16, rws, 0); });
      wait_pin<0>(f[0], f[1]); pin(f[2], f[3]);
      u32 m = 0;
#pragma unroll
      for (int i = E0; i < 4; ++i) m |= (f[i].x ^ (u32)tag) | (f[i].y ^ tg32) | (f[i].z ^ (u32)tag) | (f[i].w ^ tg32);
      if (!any(m)) break;
      __builtin_amdgcn_s_sleep(8);
    }
    flag_store(0, rws, tag);
  }
  STAMP_OUT;
}

__global__ __launch_bounds__(256, 2) void fused_kernel(const u16* __restrict__ x, const u16* __restrict__ are, const u16* __restrict__ aim,
                                                       const u16* __restrict__ bre, const u16* __restrict__ bim, const u16* __restrict__ cre,
                                                       const u16* __restrict__ cim, const u16* __restrict__ logdt, const u16* __restrict__ dskip,
                                                       const u16* __restrict__ w1, const u16* __restrict__ b1, const u16* __restrict__ w2,
                                                       const u16* __restrict__ b2, const u16* __restrict__ gw, u16* __restrict__ out, char* ws) {
  __shared__ __attribute__((aligned(256))) char smem[81920];
  LAS char* sm = (LAS char*)smem;
  asm volatile("" :: "s"(x), "s"(are), "s"(aim), "s"(bre), "s"(bim), "s"(cre), "s"(cim), "s"(logdt), "s"(dskip), "s"(w1), "s"(b1),
               "s"(w2), "s"(b2), "s"(gw), "s"(out), "s"(ws));
  const int b = blockIdx.x;
  const bool mlp = (b < 256) == (GFIRST != 0);
  if (mlp && (b & 255) < 224) gemm_role<false>(b & 255, sm, x, w1, b1, w2, b2, gw, out, ws);
  else if (mlp) gemm_role<true>(b & 255, sm, x, w1, b1, w2, b2, gw, out, ws);
  else conv_role(b & 255, sm, x, are, aim, bre, bim, cre, cim, logdt, dskip, w1, w2, ws);
}

extern "C" void kernel_launch(void* const* d_in, const int* in_sizes, int n_in, void* d_out, int out_size, void* d_ws, size_t ws_size,
                              hipStream_t stream) {
  const u16* const* in = (const u16* const*)d_in;
  fused_kernel<<<512, 256, 0, stream>>>(in[0], in[1], in[2], in[3], in[4], in[5], in[6], in[7], in[8], in[10], in[11], in[12], in[13],
                                        in[14], (u16*)d_out, (char*)d_ws + (((ws_size - WS_END) / 16 * WSPOS) & ~(size_t)0xfffff));
}
```

```cpp
#include <hip/hip_runtime.h>
#include <stdint.h>
#include <type_traits>
#pragma clang diagnostic ignored "-Winline-asm"

typedef unsigned short u16;
typedef unsigned int u32;
typedef unsigned long long u64;
typedef __attribute__((ext_vector_type(2))) u32 v2u;
typedef __attribute__((ext_vector_type(3))) u32 v3u;
typedef __attribute__((ext_vector_type(4))) u32 v4u;
typedef __attribute__((ext_vector_type(2))) float f2;
typedef __attribute__((ext_vector_type(4))) float f4;
typedef __attribute__((ext_vector_type(16))) float f16v;
typedef __attribute__((ext_vector_type(2))) __bf16 bf2;
typedef __attribute__((ext_vector_type(8))) __bf16 bf8;
typedef __attribute__((ext_vector_type(4))) short s4;
typedef __amdgpu_buffer_rsrc_t rsrc_t;
#define DI __device__ __forceinline__
#define LAS __attribute__((address_space(3)))
#define CAS __attribute__((address_space(4)))

#define GFIRST 1
#define LAM 4
#define UPP 2
#define PA 110
#define PC 127
#define PD 8
#define KP 6
#define KC 9
#define KA 19
#define KB 25
#define NT 2
#define W2PF 0
#define PRG1 2
#define PRG2 1
#define PRC0 3
#define PRC1 1
#define PRCV 2
#define UFP 8
#define PF 35
#define KO 0
#define HOLD -1
#define HOLDPT(n) do { if (HOLD == n) { for (int i_ = 0; i_ < 4000 && __builtin_amdgcn_s_memrealtime() - tst < 900; ++i_) __builtin_amdgcn_s_sleep(8); } } while (0)
#define BURST 1
#define PRCB 3
#define CDLY 4
#define ESLP 40
#define C2X 0
#define PFP 0
#define ROT2 1
#define TK0 4
#define TKS 20
#define LAYOUT 1
#define WSPOS 0
#define PFQ "sc1"

constexpr u32 FSP = 256;
constexpr u32 WS_G = 4096, WS_EFLAG = 65536, WS_UFLAG = WS_EFLAG + 1024 * FSP;
constexpr u32 WS_UT = 1u << 20, WS_A = 3u << 20, WS_H = 7u << 20, WS_END = 15u << 20;
constexpr int SPIN_MAX = 1 << 16;
constexpr int A0 = 0;

constexpr bool CODEPF = PF & 32;
constexpr int E0 = 0;

#define STAMP(i)
#define STAMPQ(i)
#define STAMPC(i)
#define STAMP_DECL
#define STAMP_OUT

DI rsrc_t mkrsrc(const void* p, u32 bytes) { return __builtin_amdgcn_make_buffer_rsrc((void*)p, (short)0, (int)bytes, 0x00020000); }
DI float bflo(u32 v) { return __uint_as_float(v << 16); }
DI float bfhi(u32 v) { return __uint_as_float(v & 0xffff0000u); }
DI u32 pk2(float a, float b) { bf2 r = __builtin_convertvector((f2){a, b}, bf2); return __builtin_bit_cast(u32, r); }
DI float dot2(u32 a, u32 b, float c) { return __builtin_amdgcn_fdot2_f32_bf16(__builtin_bit_cast(bf2, a), __builtin_bit_cast(bf2, b), c, false); }
DI float relu(float v) { return __int_as_float(max(__float_as_int(v), 0)); }
DI void fence_sched() { __builtin_amdgcn_sched_barrier(0); }

template <int N> DI void wait_vm() { asm volatile("s_waitcnt vmcnt(%0)" :: "n"(N) : "memory"); }
template <int N> DI void wait_vm_bar() { asm volatile("s_waitcnt vmcnt(%0) lgkmcnt(0)\n\ts_barrier" :: "n"(N) : "memory"); }
DI void lds_bar() { asm volatile("s_waitcnt lgkmcnt(0)\n\ts_barrier" ::: "memory"); }
DI void wait_lgkm() { asm volatile("s_waitcnt lgkmcnt(0)" ::: "memory"); }

DI void dma2(u32 m0v, u32 v0, u32 v1, rsrc_t rs, u32 so) {
  asm volatile("s_mov_b32 m0, %0\n\ts_nop 0\n\t"
               "buffer_load_dwordx4 %1, %3, %4 offen lds\n\t"
               "buffer_load_dwordx4 %2, %3, %4 offen offset:1024 lds"
               :: "s"(m0v), "v"(v0), "v"(v1), "s"(rs), "s"(so) : "m0", "memory");
}
template <u32 off> DI void set_m0(u32 base) { asm volatile("s_add_u32 m0, %0, %1" :: "s"(base), "n"(off) : "m0", "scc"); }
template <int OFF> DI void dma1(u32 v, rsrc_t rs, u32 so) {
  asm volatile("buffer_load_dwordx4 %0, %1, %2 offen offset:%3 lds" :: "v"(v), "s"(rs), "s"(so), "n"(OFF) : "memory");
}
template <int OFF> DI void reld128_sc1(v4u& r, u32 vo, rsrc_t rs, u32 so) {
  asm volatile("buffer_load_dwordx4 %0, %1, %2, %3 offen offset:%4 sc1" : "+v"(r) : "v"(vo), "s"(rs), "s"(so), "n"(OFF) : "memory");
}
template <int OFF> DI u32 ld32_sc1(u32 vo, rsrc_t rs, u32 so) {
  u32 r; asm volatile("buffer_load_dword %0, %1, %2, %3 offen offset:%4 sc1" : "=&v"(r) : "v"(vo), "s"(rs), "s"(so), "n"(OFF) : "memory"); return r;
}
template <int OFF> DI v4u ld128_sc1(u32 vo, rsrc_t rs, u32 so) {
  v4u r; asm volatile("buffer_load_dwordx4 %0, %1, %2, %3 offen offset:%4 sc1" : "=&v"(r) : "v"(vo), "s"(rs), "s"(so), "n"(OFF) : "memory"); return r;
}
DI v4u ld128(u32 vo, rsrc_t rs) {
  v4u r; asm volatile("buffer_load_dwordx4 %0, %1, %2, 0 offen" : "=&v"(r) : "v"(vo), "s"(rs) : "memory"); return r;
}
DI v2u ld64_sc1(u32 vo, rsrc_t rs) {
  v2u r; asm volatile("buffer_load_dwordx2 %0, %1, %2, 0 offen sc1" : "=&v"(r) : "v"(vo), "s"(rs) : "memory"); return r;
}
template <int OFF> DI u32 ld16(u32 vo, rsrc_t rs) {
  u32 r; asm volatile("buffer_load_ushort %0, %1, %2, 0 offen offset:%3" : "=&v"(r) : "v"(vo), "s"(rs), "n"(OFF) : "memory"); return r;
}
template <int OFF> DI void st128_sc1(v4u d, u32 vo, rsrc_t rs, u32 so) {
  asm volatile("buffer_store_dwordx4 %0, %1, %2, %3 offen offset:%4 sc1\n\ts_nop 1" :: "v"(d), "v"(vo), "s"(rs), "s"(so), "n"(OFF) : "memory");
}
DI void st64_sc1(v2u d, u32 vo, rsrc_t rs) {
  asm volatile("buffer_store_dwordx2 %0, %1, %2, 0 offen sc1" :: "v"(d), "v"(vo), "s"(rs) : "memory");
}
DI void flag_store(u32 off, rsrc_t rs, u64 tag) {
  const v2u d = {(u32)tag, (u32)(tag >> 32)};
  asm volatile("s_mov_b64 exec, 1\n\tbuffer_store_dwordx2 %0, off, %1, %2 sc1\n\ts_mov_b64 exec, -1" :: "v"(d), "s"(rs), "s"(off) : "memory");
}
DI u64 epoch_tag(v2u e) {
  const u64 v = (u64)(u32)__builtin_amdgcn_readfirstlane(e.x) | ((u64)(u32)__builtin_amdgcn_readfirstlane(e.y) << 32);
  return ((v * 0x9E3779B97F4A7C15ull + 0x632BE59BD9B4E019ull) & ~0xffffull) | (~v & 0xffffull);
}
constexpr u32 SGN = 0x80008000u;
DI u32 sign_of(u64 tag, int t) { return ((((u32)tag >> (t >> 1)) ^ t) & 1) ? SGN : 0u; }
DI u32 strip(v4u& d, u32 s) { d ^= s; return (d.x | d.y) | (d.z | d.w); }
DI bool any(u32 m) { return __builtin_amdgcn_ballot_w64(m != 0) != 0; }
DI bool flag_bad(v2u v, u64 tag) { return __builtin_amdgcn_ballot_w64((v.x != (u32)tag) | (v.y != (u32)(tag >> 32))) != 0; }
template <int SL = 1> DI void flag_wait(u32 vo, rsrc_t rs, u64 tag) {
#pragma unroll 1
  for (int it = 0; it < SPIN_MAX; ++it) {
    v2u v; asm volatile("buffer_load_dwordx2 %0, %1, %2, 0 offen sc1\n\ts_waitcnt vmcnt(0)" : "=&v"(v) : "v"(vo), "s"(rs) : "memory");
    if (!flag_bad(v, tag)) break;
    __builtin_amdgcn_s_sleep(SL);
  }
}

template <int SL = 1> DI void granule_wait(u32 vo, rsrc_t rs, u32 tg) {
#pragma unroll 1
  for (int it = 0; it < SPIN_MAX; ++it) {
    v2u v; asm volatile("buffer_load_dwordx2 %0, %1, %2, 0 offen sc1\n\ts_waitcnt vmcnt(0)" : "=&v"(v) : "v"(vo), "s"(rs) : "memory");
    if (__builtin_amdgcn_ballot_w64(v.y != tg) == 0) break;
    __builtin_amdgcn_s_sleep(SL);
  }
}
DI float opaque(float v) { asm("" : "+v"(v)); return v; }

template <int OFF> DI void lds_put_tid(u32 v) { asm volatile("ds_write_addtid_b32 %0 offset:%1" :: "v"(v), "n"(OFF) : "memory"); }
DI bf8 lds_rd128(LAS char* p) { return *(LAS bf8*)p; }
DI bf8 lds_tr2(LAS char* p0, LAS char* p1) {
  s4 a = __builtin_amdgcn_ds_read_tr16_b64_v4i16((LAS s4*)p0);
  s4 b = __builtin_amdgcn_ds_read_tr16_b64_v4i16((LAS s4*)p1);
  v2u x = __builtin_bit_cast(v2u, a), y = __builtin_bit_cast(v2u, b);
  return __builtin_bit_cast(bf8, (v4u){x.x, x.y, y.x, y.y});
}
DI f16v mfma32(bf8 a, bf8 b, f16v c) { return __builtin_amdgcn_mfma_f32_32x32x16_bf16(a, b, c, 0, 0, 0); }
DI f4 mfma16(bf8 a, bf8 b, f4 c) { return __builtin_amdgcn_mfma_f32_16x16x32_bf16(a, b, c, 0, 0, 0); }

template <int I, int N, class F> DI void static_for(F&& f) {
  if constexpr (I < N) { f(std::integral_constant<int, I>{}); static_for<I + 1, N>(f); }
}

constexpr u32 REG = 20480;
constexpr int wblk(int t) { constexpr int b[16] = {6, 7, 8, 9, 6, 7, 8, 9, 6, 7, 4, 5, 8, 0, 1, 9}; return b[t]; }
constexpr int rblk(int k) { constexpr int b[10] = {2, 3, 6, 7, 4, 5, 8, 0, 1, 9}; return b[k % 10]; }
constexpr u32 w2row(int k) { return 64 * (k / 14) + 8 * (k & 1); }

enum { OX, OW, OR, OS, OE, OH, OP, OA, OF };
struct Op { int pos, kind, idx, sub; };
struct OpTab {
  Op op[256]; int n;
  constexpr void add(int pos, int kind, int idx, int sub) { op[n] = Op{pos, kind, idx, sub}; ++n; }
  constexpr int at(int kind, int idx, int sub) const {
    for (int i = 0; i < n; ++i) if (op[i].kind == kind && op[i].idx == idx && op[i].sub == sub) return i;
    return -1;
  }
  constexpr int from(int pos) const { int i = 0; while (i < n && op[i].pos < pos) ++i; return i; }
  constexpr int younger(int kind, int idx, int sub, int pos) const {
    int c = 0, a = at(kind, idx, sub);
    if (a < 0 || op[a].pos >= pos) return -1000;
    for (int i = a + 1; i < n; ++i) if (op[i].pos < pos) ++c;
    return c > 63 ? 63 : c;
  }
  constexpr int lds(int i) const {
    const Op o = op[i];
    return o.kind == OX ? (o.idx % 3) * 4096 + (o.sub >> 1) * 2048 : o.kind == OW ? wblk(o.idx) * 2048 : o.kind == OR ? rblk(o.idx) * 2048 : -1;
  }
  constexpr int m0_at(int pos) const {
    int cur = -1;
    for (int i = 0; i < n; ++i) if (lds(i) >= 0) { if (op[i].pos == pos) return lds(i) != cur ? lds(i) : -1; cur = lds(i); }
    return -1;
  }
  constexpr bool m0_ok() const {
    for (int i = 0; i < n; ++i) for (int j = i + 1; j < n; ++j)
      if (op[i].pos == op[j].pos && op[i].pos >= 0 && lds(i) >= 0 && lds(j) >= 0 && lds(i) != lds(j)) return false;
    return true;
  }
};
constexpr int EP0 = 61, G2P = 200;
constexpr int mn(int a, int b) { return a < b ? a : b; }
template <bool AUX> constexpr OpTab optab() {
  OpTab t{};
  int nh = 0;
  if (!AUX) {
    for (int c = 0; c < 3; ++c) { for (int s = 0; s < 4; ++s) t.add(-1, OX, c, s); t.add(-1, OW, c, 0); t.add(-1, OW, c, 1); }
    t.add(-1, OW, 3, 0); t.add(-1, OW, 3, 1);
    for (int n = 0; n < 128; ++n) {
      const int T = (n + LAM) / 8, i = (n + LAM) % 8;
      if (T >= 1 && T <= 5) { if (i < 4) t.add(n, OX, T + 2, i); else if (i < 6) t.add(n, OW, T + 3, i - 4); }
      if (T >= 6 && T <= 7 && i < 6) t.add(n, OW, 9 + 3 * (T - 6) + i / 2, i & 1);
      if (T == 8 && i < 6) { if (i < 2) t.add(n, OW, 15, i); else t.add(n, OR, i / 2 - 1, i & 1); }
      if (T >= 9 && i < 2) t.add(n, OR, T - 7, i);
      for (int s = 0; s < 4; ++s) if (EP0 + (4 * s + 3) / UPP == n) t.add(n, OS, s, 0);
      if (n == EP0 + 15 / UPP + 1) t.add(n, OE, 0, 0);
      if (PFP && (n == PFP || n == PFP + 1)) t.add(n, OF, 0, n - PFP);
      if (n == PA) t.add(n, OP, 0, 0);
      if (n >= PC && nh < 2 * PD) { t.add(n, OH, nh >> 1, nh & 1); ++nh; }
    }
    for (; nh < 2 * PD; ++nh) t.add(128, OH, nh >> 1, nh & 1);
    for (int s = 4; s < 8; ++s) t.add(129 + s, OS, s, 0);
  } else {
    for (; nh < 2 * PD; ++nh) t.add(-1, OH, nh >> 1, nh & 1);
  }
  int na = 0;
  for (int k = 0; k < 28 - NT; ++k)
    for (int g = 0; g < 4; ++g) {
      const int pos = G2P + 4 * k + g;
      const bool ring = (g == 1 || g == 2) && k + 10 < 28;
      if (ring) t.add(pos, OR, k + 10, g - 1);
      if (k == KP && g == 0) t.add(pos, OP, 1, 0);
      const int lim = mn(k < KC ? 28 : 56, 2 * (k + PD) + (g == 3 ? 2 : 1));
      int room = ring ? 0 : 1;
      if (k >= KC && nh + 2 < lim) ++room;
      for (; room > 0 && nh < lim; --room, ++nh) t.add(pos, OH, nh >> 1, nh & 1);
      if (k >= KA && na < 6 && room > 0) { t.add(pos, OA, 0, na); ++na; }
    }
  return t;
}
constexpr OpTab GT[2] = {optab<false>(), optab<true>()};
constexpr int g1wait(int T) {
  const int pos = T ? 8 * T - LAM : 0;
  return mn(GT[0].younger(OW, T, 1, pos), T < 8 ? GT[0].younger(OX, T, 3, pos) : 63);
}
template <bool AUX> constexpr int g2wait(int k) {
  const int pos = G2P + 4 * k, kl = k == 27 - NT ? 27 : k + 1;
  return mn(GT[AUX].younger(OH, kl, 1, pos), (AUX && kl < 10) ? 63 : GT[AUX].younger(OR, kl, 1, pos));
}
template <bool AUX> constexpr bool sched_ok() {
  for (int k = 0; k < 28 - NT; ++k) if (g2wait<AUX>(k) < 0) return false;
  for (int T = 0; T < 16; ++T) if (g1wait(T) < 0) return false;
  return GT[AUX].m0_ok() && GT[AUX].younger(OA, 0, 5, G2P + 4 * KB) >= 0 && GT[AUX].younger(OP, 1, 0, G2P + 4 * KC) >= 0;
}
static_assert(sched_ok<false>() && sched_ok<true>() && GT[0].at(OW, 15, 1) < GT[0].at(OS, 0, 0), "schedule");
static_assert(KP < KC && KC + 1 < 14 && KB < 28 - NT && KB <= 25 && KB >= 19 && NT >= 2 && NT <= 4, "schedule");

template <int N> DI void wait_pin(v4u& a, v4u& b) { asm volatile("s_waitcnt vmcnt(%2)" : "+v"(a), "+v"(b) : "n"(N) : "memory"); }
template <int N> DI void wait_pin(v2u& a) { asm volatile("s_waitcnt vmcnt(%1)" : "+v"(a) : "n"(N) : "memory"); }
template <int N> DI void wait_pin(u32& a) { asm volatile("s_waitcnt vmcnt(%1)" : "+v"(a) : "n"(N) : "memory"); }
template <int N> DI void wait_pin(v4u (&a)[6]) {
  asm volatile("s_waitcnt vmcnt(%6)" : "+v"(a[0]), "+v"(a[1]), "+v"(a[2]), "+v"(a[3]), "+v"(a[4]), "+v"(a[5]) : "n"(N) : "memory");
}
DI void pin(v4u& a, v4u& b) { asm volatile("" : "+v"(a), "+v"(b)); }
template <int I> using IC = std::integral_constant<int, I>;

DI void touch(u32& d, u32 vo, rsrc_t rs, u32 so = 0) { asm volatile("buffer_load_dword %0, %1, %2, %3 offen" : "+v"(d) : "v"(vo), "s"(rs), "s"(so) : "memory"); }
DI void touch_ws(u32& d, u32 vo, rsrc_t rs) { asm volatile("buffer_load_dword %0, %1, %2, 0 offen " PFQ : "+v"(d) : "v"(vo), "s"(rs) : "memory"); }
DI u32 late_row(u32 i) {
  const u32 hf = i >= 576, m = i - 576 * hf;
  return 896 * hf + (m < 128 ? 640 + 2 * (m & 64) + (m & 63) : 2 * ((m - 128) & ~63u) + 64 + (m & 63));
}

template <bool AUX>
DI void gemm_role(int b, LAS char* sm, const u16* x, const u16* w1, const u16* b1, const u16* w2, const u16* b2, const u16* gw,
                  u16* out, char* ws, u32 tch) {
  constexpr const OpTab& TB = GT[AUX];
  [[maybe_unused]] constexpr int role_ = AUX;
  const int lane = threadIdx.x & 63, wave = __builtin_amdgcn_readfirstlane(threadIdx.x >> 6);
  STAMP_DECL;
  STAMP(0);
  const int xcd = b & 7, r = b >> 3;
  const rsrc_t rws = mkrsrc(ws, WS_END), rx = mkrsrc(x, 2048 * 512 * 2), rw2 = mkrsrc(w2, 1792 * 512 * 2), rb2 = mkrsrc(b2, 512 * 2);
  const rsrc_t rw1 = mkrsrc(w1, 512 * 1792 * 2);
  v2u ep = ld64_sc1(0, rws);
  const int g = lane >> 4, q = (lane >> 2) & 3, p = lane & 3, l31 = lane & 31, hh = lane >> 5, l3 = lane >> 3, l7 = lane & 7;
  const u32 mreg = (u32)(uintptr_t)sm + wave * REG;
  const int t2 = 4 * xcd + (r >> 3), c2 = (r & 7) ^ C2X;
  const int tau = 4 * (xcd >> 1) + r / 7, j = 7 * (xcd & 1) + r % 7, rot = (r % 7) + 2 * (r / 7);

  const u32 wch = ((l7 ^ ((g & 1) << 2)) << 4);
  const u32 vx0 = (u32)(128 * tau + 32 * wave + l3) * 1024 + ((l7 ^ g) << 4), vx1 = (vx0 ^ 64) + 7168;
  const u32 vw0 = (u32)(16 * wave + l3) * 3584 + j * 256 + wch, vw1 = vw0 + 8 * 3584 - 1024;
  const u32 vv0 = (u32)(896 * (wave >> 1) + 32 * (wave & 1) + l3) * 1024 + c2 * 128 + wch, vv1 = vv0 + 15360;
  const u32 vh = WS_H + (u32)((4 * tau + wave) * 112 + 8 * j) * 1024 + lane * 16;
  const u32 vhh = WS_H + (u32)(2 * t2 * 112 + 56 * (wave >> 1) + 2 * (wave & 1)) * 1024 + lane * 16;
  const u32 vp = vhh + (u32)((l3 & 1) * 112 + 8 * min(l7, 6) + 1) * 1024;
  const u32 va = WS_A + (u32)t2 * 131072 + (u32)(32 * c2 + 4 * l3) * 512 + (u32)(16 * wave + 2 * l7) * 8;
  const u32 vg = WS_G + (u32)(64 * t2 + 16 * wave + 2 * l3) * 8;
  const u32 vf0 = late_row(32 * r + 8 * wave + l3) * 1024 + l7 * 128, vf1 = lane < 8 ? late_row(1024 + 4 * r + wave) * 1024 + lane * 128 : 1u << 30;
  const int rot2 = ROT2 ? (c2 + 2 * (r >> 3)) % 7 : 0;
  auto prod = [&](int k) { const int q_ = ((k % 14) >> 1) + rot2; return (u32)(q_ >= 7 ? q_ - 7 : q_); };
  u32 aoff[2];
#pragma unroll
  for (int mi = 0; mi < 2; ++mi) aoff[mi] = (8 * (g >> 1) + q) * 128 + (((4 * mi + 2 * (p & 1) + (g & 1)) ^ ((q >> 1) << 2)) << 4) + (p >> 1) * 8;

  u64 tag;
  u32 sg, pf;
  v4u hd[8], hb[28][2], av[6];
  auto hload = [&](auto K, auto S, auto AGAIN) {
    constexpr int k = decltype(K)::value, s = decltype(S)::value;
    const u32 so = prod(k) * 8192 + (k / 14) * 4096 + s * 114688;
    if constexpr (decltype(AGAIN)::value) reld128_sc1<(k & 1) * 1024>(hb[k][s], vhh, rws, so);
    else hb[k][s] = ld128_sc1<(k & 1) * 1024>(vhh, rws, so);
  };
  auto aload = [&](auto S) {
    constexpr int s = decltype(S)::value;
    if constexpr (s < 4) av[s] = ld128_sc1<s * 512>(va, rws, 0);
    else if constexpr (s == 4) av[4] = ld128_sc1<0>(vg, rws, 0);
    else av[5] = ld128((u32)(64 * c2 + 8 * l7) * 2, rb2);
  };
  auto issue = [&](auto I) {
    constexpr Op o = TB.op[decltype(I)::value];
    constexpr int lo = (o.sub & 1) * 1024;
    if constexpr (o.pos < 0 && TB.lds(decltype(I)::value) >= 0 && lo == 0) { set_m0<TB.lds(decltype(I)::value)>(mreg); asm volatile("s_nop 0"); }
    if constexpr (o.kind == OX) dma1<lo>(lo ? vx1 : vx0, rx, (u32)((o.idx + rot) & 7) * 128 + (o.sub >> 1) * 16384);
    if constexpr (o.kind == OW) dma1<lo>(lo ? vw1 : vw0, rw1, (u32)((o.idx + rot) & 7) * (64 * 3584) + (o.idx >> 3) * 128);
    if constexpr (o.kind == OR) dma1<lo>(lo ? vv1 : vv0, rw2, prod(o.idx) * 131072 + w2row(o.idx) * 1024);
    if constexpr (o.kind == OS) st128_sc1<(o.idx & 3) * 1024>(hd[o.idx], vh, rws, (o.idx >> 2) * 4096);
    if constexpr (o.kind == OE) flag_store(WS_EFLAG + (u32)b * 8, rws, tag);
    if constexpr (o.kind == OF) touch(tch, o.sub ? vf1 : vf0, rw2);
    if constexpr (o.kind == OP) pf = ld32_sc1<0>(vp, rws, o.idx * 4096);
    if constexpr (o.kind == OH) hload(IC<o.idx>{}, IC<o.sub>{}, IC<0>{});
    if constexpr (o.kind == OA) aload(IC<o.sub>{});
    if constexpr (o.pos < 0 && o.kind == OW && o.sub == 1 && o.idx < 2) asm volatile("s_barrier");
  };
  auto issue_at = [&](auto N) {
    static_for<TB.from(decltype(N)::value), TB.from(decltype(N)::value + 1)>(issue);
  };
  auto m0_for = [&](auto N) { if constexpr (TB.m0_at(decltype(N)::value) >= 0) set_m0<TB.m0_at(decltype(N)::value)>(mreg); };
  auto gate = [&](int ph) {
    if (__builtin_expect(any((pf ^ sg) & SGN), 0)) {
#pragma unroll 1
      for (int it = 0; it < SPIN_MAX; ++it) {
        __builtin_amdgcn_s_sleep(1);
        pf = ld32_sc1<0>(vp, rws, ph * 4096);
        wait_pin<0>(pf);
        if (!any((pf ^ sg) & SGN)) break;
      }
    }
  };

  if constexpr (!AUX) {
    const rsrc_t rb1 = mkrsrc(b1, 1792 * 2);
    const u32 vb = (u32)(128 * j + 32 * hh + 16 * ((l31 >> 2) & 1) + 4 * (l31 >> 3) + p) * 2;
    u32 bq0 = ld16<0>(vb, rb1), bq1 = ld16<128>(vb, rb1);
    f16v acc[4];
    issue_at(IC<-1>{});
    __builtin_amdgcn_s_setprio(PRG1);

    LAS char* pa[2][2]; LAS char* pb[4];
#pragma unroll
    for (int mi = 0; mi < 2; ++mi) { pa[mi][0] = sm + aoff[mi]; pa[mi][1] = sm + aoff[mi] + 2 * REG; }
#pragma unroll
    for (int u = 0; u < 4; ++u) pb[u] = sm + wave * REG + l31 * 128 + (((2 * u + hh) ^ ((l31 >> 1) & 7)) << 4);

    asm volatile("s_waitcnt vmcnt(20)" : "+v"(ep), "+v"(bq0), "+v"(bq1), "+v"(tch) :: "memory");
    tag = epoch_tag(ep);
    sg = sign_of(tag, l31);
    {
      const bf8 o0 = __builtin_bit_cast(bf8, (v4u){hh ? 0u : 0x3f80u, 0, 0, 0}), o1 = __builtin_bit_cast(bf8, (v4u){hh ? 0x3f80u : 0u, 0, 0, 0});
      const bf8 a0 = __builtin_bit_cast(bf8, (v4u){bq0, 0, 0, 0}), a1 = __builtin_bit_cast(bf8, (v4u){bq1, 0, 0, 0});
      acc[0] = mfma32(a0, o0, (f16v)(0.f)); acc[1] = mfma32(a0, o1, (f16v)(0.f));
      acc[2] = mfma32(a1, o0, (f16v)(0.f)); acc[3] = mfma32(a1, o1, (f16v)(0.f));
    }
    constexpr auto ms = [](int n) constexpr { const int T = n >> 3, v = n & 7; return (T & 7) == 7 ? (v & 3) : (v >> 1); };
    constexpr auto mt = [](int n) constexpr { const int T = n >> 3, v = n & 7; return (T & 7) == 7 ? (v >> 2) : (v & 1); };
    bf8 af[128], xf[32];
    auto rda = [&](auto N) {
      constexpr int n = decltype(N)::value, u = ms(n);
      LAS char* a = pa[mt(n)][u >> 1] + (u & 1) * REG + wblk(n >> 3) * 2048;
      af[n] = lds_tr2(a, a + 512);
    };
    auto rdx = [&](auto S) { constexpr int s = decltype(S)::value; xf[s] = lds_rd128(pb[s & 3] + ((s >> 2) % 3) * 4096); };
    auto unit = [&](auto U) {
      constexpr int un = decltype(U)::value, tl = un >> 3, e = (un & 7) * 2;
      const float v0 = relu(acc[tl][e]), v1 = relu(acc[tl][e + 1]);
      hd[un >> 2][un & 3] = (pk2(opaque(v0 * v0), opaque(v1 * v1)) & ~SGN) | sg;
    };
    STAMP(1);
    fence_sched();
    wait_vm_bar<g1wait(0)>();
    fence_sched();
    STAMP(2); STAMPC(60);
    static_for<0, 4>(rdx);
    static_for<0, LAM>(rda);
    static_for<0, 128>([&](auto N) {
      constexpr int n = decltype(N)::value, T = (n + LAM) / 8, i = (n + LAM) % 8;
      fence_sched();
      if constexpr (i == 0) { if constexpr (T < 16) wait_vm_bar<g1wait(T)>(); else lds_bar(); }
      if constexpr (i == 0) { STAMPQ(15 + T); }
      if constexpr (n == PC) { wait_pin<TB.younger(OP, 0, 0, n)>(pf); asm volatile("" :: "v"(tch)); gate(0); }
      m0_for(N);
      fence_sched();
      acc[2 * (n >> 6) + mt(n)] = mfma32(af[n], xf[(4 * (n >> 3) + ms(n)) & 31], acc[2 * (n >> 6) + mt(n)]);
      fence_sched();
      if constexpr (n + LAM < 128) rda(IC<n + LAM>{});
      if constexpr (T >= 1 && T < 8 && i < 4) rdx(IC<4 * T + i>{});
      if constexpr (n >= EP0 && n < EP0 + 16 / UPP) static_for<UPP * (n - EP0), UPP * (n - EP0 + 1)>(unit);
      if constexpr (n >= 64 + EP0) static_for<16 + UPP * (n - 64 - EP0), 16 + UPP * (n - 64 - EP0 + 1)>(unit);
      fence_sched();
      issue_at(N);
    });
    fence_sched();
    STAMP(5); STAMPC(61);
    issue_at(IC<128>{});
    static_for<16, 32>([&](auto U) {
      constexpr int un = decltype(U)::value;
      if constexpr (64 + EP0 + (un & 15) / UPP > 127) unit(U);
      if constexpr ((un & 3) == 3) { fence_sched(); issue_at(IC<129 + (un >> 2)>{}); fence_sched(); }
    });
    STAMP(6);
  } else {
    const int tg = xcd * 4 + (r - 28);
    const v4u gv = *(const v4u*)(gw + lane * 8);
    const u16* xr = x + (size_t)(tg * 64 + wave * 16) * 512 + lane * 8;
    float s[16];
#pragma unroll
    for (int i = 0; i < 16; ++i) {
      const v4u xv = *(const v4u*)(xr + i * 512);
      s[i] = 0.f;
#pragma unroll
      for (int k = 0; k < 4; ++k) s[i] = dot2(xv[k], gv[k], s[i]);
    }
#pragma unroll
    for (int i = 0; i < 8; ++i) {
      const v2u t = __builtin_amdgcn_permlane32_swap(__float_as_uint(s[i]), __float_as_uint(s[i + 8]), false, false);
      s[i] = opaque(__uint_as_float(t.x) + __uint_as_float(t.y));
    }
#pragma unroll
    for (int i = 0; i < 4; ++i) {
      const v2u t = __builtin_amdgcn_permlane16_swap(__float_as_uint(s[i]), __float_as_uint(s[i + 4]), false, false);
      float v = __uint_as_float(t.x) + __uint_as_float(t.y);
      v += __int_as_float(__builtin_amdgcn_update_dpp(0, __float_as_int(v), 0x128, 0xf, 0xf, false));
      v += __int_as_float(__builtin_amdgcn_update_dpp(0, __float_as_int(v), 0x124, 0xf, 0xf, false));
      v += __int_as_float(__builtin_amdgcn_update_dpp(0, __float_as_int(v), 0x122, 0xf, 0xf, false));
      v += __int_as_float(__builtin_amdgcn_update_dpp(0, __float_as_int(v), 0x121, 0xf, 0xf, false));
      s[i] = opaque(v);
    }
    const float m01 = opaque(p & 1 ? s[1] : s[0]), m23 = opaque(p & 1 ? s[3] : s[2]), mine = p & 2 ? m23 : m01;
    const float sl = bflo(pk2(mine, 0.f));
    const float gt = bflo(pk2(1.f / (1.f + __expf(-sl)), 0.f));
    fence_sched();
    asm volatile("s_waitcnt vmcnt(0)\n\ts_barrier" : "+v"(ep), "+v"(tch) :: "memory");
    tag = epoch_tag(ep);
    sg = sign_of(tag, l31);
    if (q == 0) st64_sc1((v2u){__float_as_uint(gt), (u32)(tag >> 32)}, WS_G + (u32)(tg * 64 + wave * 16 + 4 * g + p) * 8, rws);
    STAMP(1);
    static_for<0, 10>([&](auto I) { constexpr int i = decltype(I)::value; dma2(mreg + rblk(i) * 2048, vv0, vv1, rw2, prod(i) * 131072 + w2row(i) * 1024); });
    flag_store(WS_EFLAG + (u32)b * 8, rws, tag);
    if (PFP) { touch(tch, vf0, rw2); touch(tch, vf1, rw2); }
    STAMP(2);
    pf = ld32_sc1<0>(vp, rws, 0);
    wait_pin<0>(pf);
    asm volatile("" :: "v"(tch));
    gate(0);
    STAMP(4);
    issue_at(IC<-1>{});
  }
  __builtin_amdgcn_s_setprio(PRG2);

  const u32 tg32 = (u32)(tag >> 32);
  LAS char* pv[2] = {sm + wave * REG + aoff[0], sm + wave * REG + aoff[1]};
  auto vfrag = [&](int k, int mi) { LAS char* a = pv[mi] + rblk(k) * 2048; return lds_tr2(a, a + 512); };
  auto fresh = [&](auto K0, auto K1, u32 m) {
    constexpr int k0 = decltype(K0)::value, k1 = decltype(K1)::value, ke = mn(k1 | 1, 27);
    if (__builtin_expect(any(m & SGN), 0)) {
#pragma unroll 1
      for (int it = 0; it < SPIN_MAX; ++it) {
        static_for<k0, ke + 1>([&](auto K) { hload(K, IC<0>{}, IC<1>{}); hload(K, IC<1>{}, IC<1>{}); });
        wait_vm<0>();
        u32 z = 0;
        static_for<k0, ke + 1>([&](auto K) { constexpr int k = decltype(K)::value; pin(hb[k][0], hb[k][1]); if constexpr (k <= k1) z |= strip(hb[k][0], sg) | strip(hb[k][1], sg); });
        if (!any(z & SGN)) break;
        __builtin_amdgcn_s_sleep(1);
      }
    }
  };
  auto abad = [&]() {
    u32 m = 0;
#pragma unroll
    for (int i = A0; i < 5; ++i) m |= (av[i].y ^ tg32) | (av[i].w ^ tg32);
    return any(m);
  };
  LAS char* ap = sm + wave * REG + 9 * 2048;
  v4u a4[2];
  float ga[2][8], gs[2];
  auto aput = [&]() {
    *(LAS v4u*)(ap + l7 * 128 + ((l3 ^ l7) << 4)) = (v4u){av[0].x, av[1].x, av[2].x, av[3].x};
    *(LAS v4u*)(ap + l7 * 128 + ((l3 ^ l7) << 4) + 1024) = (v4u){av[0].z, av[1].z, av[2].z, av[3].z};
    a4[0] = *(LAS v4u*)(ap + l3 * 128 + ((l3 ^ l7) << 4));
    a4[1] = *(LAS v4u*)(ap + l3 * 128 + ((l3 ^ l7) << 4) + 1024);
  };
  auto amix = [&](int e, int k) {
    const float gt = __uint_as_float(e ? av[4].z : av[4].x), gn = opaque(1.f - gt);
    ga[e][2 * k] = opaque(__builtin_fmaf(gt, bflo(a4[e][k]), opaque(gn * bflo(av[5][k]))));
    ga[e][2 * k + 1] = opaque(__builtin_fmaf(gt, bfhi(a4[e][k]), opaque(gn * bfhi(av[5][k]))));
    if (k == 0) gs[e] = gn;
  };
  f16v o2[2][2];
  bf8 wb[28][2];
  bool late = false;
  fence_sched();
  wait_pin<mn(TB.younger(OH, 0, 1, G2P), AUX ? 63 : TB.younger(OR, 0, 1, G2P))>(hb[0][0], hb[0][1]);
  wb[0][0] = vfrag(0, 0); wb[0][1] = vfrag(0, 1);
  fresh(IC<0>{}, IC<0>{}, strip(hb[0][0], sg) | strip(hb[0][1], sg));
  STAMP(7);
  static_for<0, 28 - NT>([&](auto K) {
    constexpr int k = decltype(K)::value, kl = k == 27 - NT ? 27 : k + 1;
    fence_sched();
    wait_vm<g2wait<AUX>(k)>();
    static_for<k + 1, kl + 1>([&](auto J) { constexpr int jn = decltype(J)::value; pin(hb[jn][0], hb[jn][1]); });
    if constexpr (k == KC) { wait_pin<TB.younger(OP, 1, 0, G2P + 4 * k)>(pf); gate(1); }
    if constexpr (k == KB) { wait_pin<TB.younger(OA, 0, 5, G2P + 4 * k)>(av); }
    STAMPQ(32 + k);
    u32 m = 0;
    static_for<0, 4>([&](auto G) {
      constexpr int gi = decltype(G)::value, mi = gi & 1, ni = gi >> 1;
      m0_for(IC<G2P + 4 * k + gi>{});
      fence_sched();
      if constexpr (k == 0) o2[mi][ni] = mfma32(wb[k][mi], __builtin_bit_cast(bf8, hb[k][ni]), (f16v)(0.f));
      else o2[mi][ni] = mfma32(wb[k][mi], __builtin_bit_cast(bf8, hb[k][ni]), o2[mi][ni]);
      fence_sched();
      if constexpr (gi < 2) static_for<k + 1, kl + 1>([&](auto J) {
        constexpr int jn = decltype(J)::value;
        wb[jn][gi] = vfrag(jn, gi);
        m |= strip(hb[jn][gi], sg);
      });
      if constexpr (k == KB && gi == 0) aput();
      if constexpr (k == KB && gi == 2) late = abad();
      if constexpr (k > KB && k <= KB + 2) amix(k - KB - 1, gi);
      fence_sched();
      issue_at(IC<G2P + 4 * k + gi>{});
      if constexpr (gi == 2) fresh(IC<k + 1>{}, IC<kl>{}, m);
    });
  });
  fence_sched();
  LAS char* pw = sm + wave * REG + l31 * 256;
  const int kw = (4 * hh) ^ (((l31 >> 1) & 3) | ((l31 & 1) << 2));
  auto pput = [&](int tl, int i) {
    const f16v& a = o2[tl >> 1][tl & 1];
    *(LAS f4*)(pw + ((kw ^ i) << 4) + (tl >> 1) * 128 + (tl & 1) * 8192) = (f4){a[4 * i], a[4 * i + 1], a[4 * i + 2], a[4 * i + 3]};
  };
  static_for<0, 4 * NT>([&](auto N) {
    constexpr int n = decltype(N)::value, tl = n / NT, v = n % NT, k = 28 - NT + v;
    o2[tl >> 1][tl & 1] = mfma32(wb[k][tl >> 1], __builtin_bit_cast(bf8, hb[k][tl & 1]), o2[tl >> 1][tl & 1]);
    fence_sched();
    if constexpr (constexpr int u = 4 * (27 - NT - KB) + n; u < 8) amix(u >> 2, u & 3);
    if constexpr (tl >= 1 && v == NT - 1) { pput(tl - 1, 0); pput(tl - 1, 1); pput(tl - 1, 2); pput(tl - 1, 3); }
    fence_sched();
  });
  STAMP(10); STAMPC(62);
  if (__builtin_expect(late, 0)) {
#pragma unroll 1
    for (int it = 0; it < SPIN_MAX; ++it) {
      static_for<0, 5>(aload);
      wait_pin<0>(av);
      if (!abad()) break;
      __builtin_amdgcn_s_sleep(1);
    }
    aput();
#pragma unroll
    for (int i = 0; i < 8; ++i) amix(i >> 2, i & 3);
  }
#pragma unroll
  for (int i = 0; i < 4; ++i) pput(3, i);
  lds_bar();
  STAMP(11);
  if constexpr (CODEPF) asm volatile(".Lpf_end%0:" :: "n"((int)AUX));
  {
    LAS char* pr = sm + (16 * wave + 2 * l3) * 256;
    const int kr = (2 * l7) ^ (l3 & 3);
#pragma unroll
    for (int e = 0; e < 2; ++e) {
      v4u d;
#pragma unroll
      for (int cc = 0; cc < 2; ++cc) {
        LAS char* a = pr + e * 256 + ((kr ^ cc ^ (4 * e)) << 4);
        const f4 s = (*(LAS f4*)(a) + *(LAS f4*)(a + REG)) + (*(LAS f4*)(a + 2 * REG) + *(LAS f4*)(a + 3 * REG));
#pragma unroll
        for (int k = 0; k < 2; ++k)
          d[2 * cc + k] = pk2(opaque(__builtin_fmaf(gs[e], s[2 * k], ga[e][4 * cc + 2 * k])), opaque(__builtin_fmaf(gs[e], s[2 * k + 1], ga[e][4 * cc + 2 * k + 1])));
      }
      *(v4u*)(out + (size_t)(64 * t2 + 16 * wave + 2 * l3 + e) * 512 + 64 * c2 + 8 * l7) = d;
    }
  }
  STAMP(12);
  STAMP_OUT;
}

struct cf { float x, y; };
DI cf cmul(cf a, cf m) { return cf{opaque(__builtin_fmaf(a.x, m.x, -opaque(a.y * m.y))), opaque(__builtin_fmaf(a.x, m.y, opaque(a.y * m.x)))}; }
DI cf cexp_s(float lr, float rv, float m) {
  const float e = __builtin_amdgcn_exp2f(lr * m), f = __builtin_amdgcn_fractf(rv * m);
  return cf{opaque(e * __builtin_amdgcn_cosf(f)), opaque(e * __builtin_amdgcn_sinf(f))};
}

constexpr u32 TROW = 544, TCH = 63 * TROW, XT = 69632;
constexpr u32 KCH = 9472, KOO = 4656, UA0 = 18944, UCH = 8768, UBO = 4224, EX = 40960;

constexpr int NPF = !!(PF & 1) + !!(PF & 2) + 2 * !!(PF & 4) + !!(PF & 8) + !!(PF & 16);

DI void conv_role(int ci, LAS char* sm, const u16* x, const u16* are, const u16* aim, const u16* bre, const u16* bim,
                  const u16* cre, const u16* cim, const u16* logdt, const u16* dskip, const u16* w1, const u16* w2, char* ws, u32 tch) {
  const int tid = threadIdx.x, lane = tid & 63, wave = __builtin_amdgcn_readfirstlane(tid >> 6);
  [[maybe_unused]] constexpr int role_ = 2;
  STAMP_DECL;
  STAMP(0);
  const rsrc_t rws = mkrsrc(ws, WS_END), rx = mkrsrc(x, 2048 * 512 * 2);
  const u64 tst = HOLD >= 0 ? __builtin_amdgcn_s_memrealtime() : 0;
  HOLDPT(0);
  if (CDLY) __builtin_amdgcn_s_sleep(CDLY);
  v2u ep = ld64_sc1(0, rws);
  __builtin_amdgcn_s_setprio(PRC0);
  const u32 smb = (u32)(uintptr_t)sm;
  const int pr = (ci & 7) * 32 + (ci >> 3), cg = (ci >> 3) & 7, tt = 4 * (ci & 7) + (ci >> 6);
  const int g = lane >> 4, q = (lane >> 2) & 3, p = lane & 3, n = lane & 15;
  const int ch = wave >> 1, half = wave & 1;

  {
    const u32 v0 = (u32)(64 * tt + 16 * wave + (lane >> 3)) * 1024 + cg * 128 + (((lane & 7) ^ ((g & 1) << 1)) << 4);
    const u32 v1 = (v0 ^ 64) + 7168;
    dma2(smb + XT + wave * 2048, v0, v1, rx, 0);
  }
  const int pi = pr * 256 + tid;
  u32 r_are = are[pi], r_aim = aim[pi], r_bre = bre[pi], r_bim = bim[pi], r_cre = cre[pi], r_cim = cim[pi];
  u32 r_ldt = logdt[2 * pr + ch];
  u32 r_dsk = *(const u32*)(dskip + 2 * pr);
  fence_sched();
  asm volatile("s_waitcnt vmcnt(0)" : "+v"(r_are), "+v"(r_aim), "+v"(r_bre), "+v"(r_bim), "+v"(r_cre), "+v"(r_cim), "+v"(r_ldt), "+v"(r_dsk), "+v"(ep) :: "memory");
  const u64 tag = epoch_tag(ep);
  const u32 tg32 = (u32)(tag >> 32);
  fence_sched();
  STAMP(1);
  {
    LAS char* tb = sm + XT + wave * 2048 + (8 * (p & 1) + q) * 128 + (p >> 1) * 8;
    const int sw = (q & 2) | ((p & 1) << 2);
    const u32 vo = WS_UT + (u32)((16 * cg + 2 * g + (q >> 1)) * 256 + 8 * tt + 2 * wave + (q & 1)) * 64 + p * 16;
    const bf8 d0 = lds_tr2(tb + ((g ^ sw) << 4), tb + ((g ^ sw) << 4) + 512);
    const bf8 d1 = lds_tr2(tb + (((4 + g) ^ sw) << 4), tb + (((4 + g) ^ sw) << 4) + 512);
    st128_sc1<0>(__builtin_bit_cast(v4u, d0), vo, rws, 0);
    st128_sc1<0>(__builtin_bit_cast(v4u, d1), vo, rws, 8 * 256 * 64);
  }
  fence_sched();
  {
    const u32 wv = 4 * ci + wave, big = 1u << 30;
    if (PF & 1) touch(tch, lane < 7 ? (wv >> 1) * 3584 + ((wv & 1) * 7 + lane) * 256 + 128 : big, mkrsrc(w1, 512 * 1792 * 2));
    if (PF & 2) touch(tch, lane < 14 ? (wv * 14 + lane) * 128 : big, mkrsrc(w2, 512 * 1792 * 2));
    if (PF & 4) {
      const u32 wx = 4 * (ci >> 3) + wave;
      touch(tch, (wx * 112 + lane) * 128, mkrsrc(w2, 512 * 1792 * 2));
      touch(tch, lane < 48 ? (wx * 112 + 64 + lane) * 128 : big, mkrsrc(w2, 512 * 1792 * 2));
    }
    if (PF & 8) touch_ws(tch, lane < 56 ? WS_H + (wv * 56 + lane) * 128 : big, rws);
    if (PF & 16) touch_ws(tch, lane < 32 ? WS_A + (wv * 32 + lane) * 128 : big, rws);
  }
  fence_sched();

  HOLDPT(1);
  __builtin_amdgcn_s_setprio(PRC1);
  const u32 uf = WS_UFLAG + (u32)(cg * 128 + tt * 4 + wave) * FSP;
  {
    const float dt = bflo(pk2(expf(bflo(r_ldt)), 0.f));
    const float lr = dt * bflo(r_are) * 1.44269504088896341f, rv = dt * bflo(r_aim) * 0.15915494309189535f;
    const cf db = {opaque(dt * bflo(r_bre)), opaque(-dt * bflo(r_bim))};
    const cf cb = cmul(cf{bflo(r_cre), -bflo(r_cim)}, db);
    const cf e16 = cexp_s(lr, rv, 16.f), e1 = cexp_s(lr, -rv, 1.f), e256 = cexp_s(lr, -rv, 256.f);
    const cf e2 = cexp_s(lr, -rv, 2.f), e4 = cexp_s(lr, -rv, 4.f), e8 = cexp_s(lr, -rv, 8.f);
    LAS char* tw = sm + ch * TCH + (tid & 127) * 4;
    u32 tv[63];
    auto put = [&](int row, cf v) {
      if (BURST) tv[row] = pk2(v.x, v.y);
      else if (KO & 1) { const u32 q_ = pk2(v.x, v.y); asm volatile("" :: "v"(q_)); }
      else if (KO & 2) *(volatile LAS u32*)(tw + row * TROW) = r_are;
      else *(volatile LAS u32*)(tw + row * TROW) = pk2(v.x, v.y);
    };
    cf a = e16, t0 = cb, tb[8], v[8];
    put(0, cf{1.f, 0.f}); put(16, t0);
    tb[0] = cb;
#pragma unroll
    for (int i = 1; i < 16; ++i) {
      if (i == UFP) { wait_vm<NPF>(); flag_store(uf, rws, tag); }
      put(i, a); if (i < 15) a = cmul(a, e16);
      t0 = cmul(t0, e1); put(16 + i, t0);
      if (i < 8) { tb[i] = cmul(tb[i - 1], e256); v[i] = tb[i]; }
#pragma unroll
      for (int tl = 1; tl < 8; ++tl) {
        const int base = tl == 1 ? 32 : (tl < 4 ? 41 + 5 * (tl - 2) : 51 + 3 * (tl - 4));
        const int cnt = tl == 1 ? 9 : (tl < 4 ? 5 : 3), k = i - tl;
        if (k >= 0 && k < cnt) {
          if (k > 0) v[tl] = cmul(v[tl], tl == 1 ? e2 : (tl < 4 ? e4 : e8));
          put(base + k, v[tl]);
        }
      }
    }
    if (BURST) {
#pragma unroll
      for (int i = 0; i < 63; ++i) asm volatile("" : "+v"(tv[i]));
      fence_sched();
      __builtin_amdgcn_s_setprio(PRCB);
      asm volatile("s_mov_b32 m0, %0\n\ts_nop 0" :: "s"(smb + ch * TCH + half * 256) : "m0");
      static_for<0, 63>([&](auto R) { lds_put_tid<decltype(R)::value * TROW>(tv[decltype(R)::value]); });
      __builtin_amdgcn_s_setprio(PRC1);
    }
  }
  fence_sched();
  if (UFP >= 16) { wait_vm<NPF>(); flag_store(uf, rws, tag); }
  HOLDPT(2);
  const u32 vf = WS_UFLAG + (u32)((ci & 7) * 128 + wave * 32 + (lane & 31)) * FSP;
  v2u pf = ld64_sc1(vf, rws);
  STAMP(2);
  lds_bar();

  f4 kk[4];
  v4u uv[2];
  {
    LAS char* tb = sm + ch * TCH + g * 16;
    const int m2 = (n + ((n >> 1) & 1)) >> 1, m4 = (n + 1 + ((n >> 2) & 1)) >> 2, m8 = (n + 3 + ((n >> 3) & 1)) >> 3;
    LAS char* pr_[4] = {tb + (half ? 51 + m8 : 16 + n) * TROW, tb + (half ? 54 + m8 : 32 + m2) * TROW,
                        tb + (half ? 57 + m8 : 41 + m4) * TROW, tb + (half ? 60 + m8 : 46 + m4) * TROW};
    LAS char* pe = tb + n * TROW;
#pragma unroll
    for (int i = 0; i < 4; ++i) kk[i] = (f4)(0.f);
#pragma unroll
    for (int ks = 0; ks < 8; ++ks) {
      if (ks == 4) {
        fence_sched();
        wait_pin<0>(pf);
        asm volatile("" :: "v"(tch));
        if (__builtin_expect(flag_bad(pf, tag), 0)) flag_wait(vf, rws, tag);
        STAMP(3);
        const u32 vo = WS_UT + (u32)((pr >> 1) * 256 + 64 * wave + (lane >> 1)) * 64 + ((pr & 1) * 2 + (lane & 1)) * 16;
        uv[0] = ld128_sc1<0>(vo, rws, 0); uv[1] = ld128_sc1<2048>(vo, rws, 0);
        fence_sched();
      }
      if (KO & 4) continue;
      const bf8 e = lds_rd128(pe + ks * 64);
#pragma unroll
      for (int i = 0; i < 4; ++i) kk[i] = mfma16(lds_rd128(pr_[i] + ks * 64), e, kk[i]);
    }
  }
  fence_sched();
  STAMP(4);
  HOLDPT(3);
  lds_bar();
  if (KO & 8) asm volatile("" :: "v"(kk[0]), "v"(kk[1]), "v"(kk[2]), "v"(kk[3]));
  else {
    LAS char* kb = sm + ch * KCH;
#pragma unroll
    for (int i = 0; i < 4; ++i) {
      const int l0 = 256 * (4 * half + i) + 16 * n + 4 * g;
      const float k0 = opaque(kk[i][0]), k1 = opaque(kk[i][1]), k2 = opaque(kk[i][2]), k3 = opaque(kk[i][3]);
      const u32 a = pk2(k3, k2), bq = pk2(k1, k0), c = pk2(k2, k1);
      *(LAS v2u*)(kb + KOO + 2 * (2044 - l0)) = (v2u){a, bq};
      *(LAS u16*)(kb + 2 * (2051 - l0)) = (u16)a;
      *(LAS u32*)(kb + 2 * (2052 - l0)) = c;
      *(LAS u16*)(kb + 2 * (2054 - l0)) = (u16)(bq >> 16);
    }
    const v4u z = {0, 0, 0, 0};
    if (lane < 29) *(LAS v4u*)(kb + (half ? KOO + 4096 : 4112) + lane * 16) = z;
    else if (lane < 33) *(LAS v4u*)(sm + UA0 + ch * UCH + (half ? UBO : 0) + (lane - 29) * 16 - (half ? 0 : 16)) = z;
    else if (lane == 33 && !half) *(LAS u16*)(kb + 2 * 2055) = 0;
    else if (lane == 34 && !half) *(LAS v3u*)(sm + UA0 + ch * UCH + 48) = (v3u){0, 0, 0};
  }
  asm volatile("s_waitcnt vmcnt(0)" : "+v"(uv[0]), "+v"(uv[1]) :: "memory");
#pragma unroll
  for (int k = 0; k < 2; ++k) {
    if (KO & 8) continue;
    LAS char* ub = sm + UA0 + (lane & 1) * UCH + 64 + (64 * wave + 32 * k + (lane >> 1)) * 16;
    *(LAS v4u*)(ub + UBO) = uv[k];
    *(LAS u32*)(ub - 4) = uv[k].x;
    *(LAS v3u*)(ub) = (v3u){uv[k].y, uv[k].z, uv[k].w};
  }
  lds_bar();
  STAMP(5);
  flag_store(WS_EFLAG + (u32)(256 + ci) * 8, rws, tag);
  HOLDPT(4);
  const rsrc_t rw2 = mkrsrc(w2, 512 * 1792 * 2);
  const u32 wx = 4 * (ci >> 3) + wave, vt = lane < 8 ? (128 * (wx >> 6) + 64 + (wx & 63)) * 1024 + lane * 128 : 1u << 30;
  __builtin_amdgcn_s_setprio(PRCV);

  f4 y[8];
  if (KO & 16) { for (int i = 0; i < 8; ++i) y[i] = kk[i & 3]; }
  else {
    LAS char* pk = sm + ch * KCH + ((n & 1) ? KOO + 4032 : 4048) - 64 * (n >> 1) + 16 * g - 3584 + 256 * half;
    LAS char* pu = sm + UA0 + ch * UCH + (n < 8 ? 8 * n : UBO + 8 * (n - 8) + 8) + 16 * g + 256 * half;
    bf8 kf[4][8], uf[4][8];
    auto rd = [&](int rr, int i) {
      kf[rr][i] = lds_rd128(pk + 512 * (7 - i) + 64 * rr);
      const v2u lo = *(volatile LAS v2u*)(pu + 512 * i + 64 * rr), hi = *(volatile LAS v2u*)(pu + 512 * i + 64 * rr + 8);
      uf[rr][i] = __builtin_bit_cast(bf8, (v4u){lo.x, lo.y, hi.x, hi.y});
    };
#pragma unroll
    for (int i = 0; i < 8; ++i) { rd(0, i); fence_sched(); }
    static_for<0, 144>([&](auto N) {
      constexpr int nn = decltype(N)::value, rr = nn / 36, v = nn % 36;
      constexpr int s = v < 1 ? 0 : v < 3 ? 1 : v < 6 ? 2 : v < 10 ? 3 : v < 15 ? 4 : v < 21 ? 5 : v < 28 ? 6 : 7, m = v - s * (s + 1) / 2;
      if constexpr (rr == 0 && m == 0) y[s] = mfma16(kf[rr][s - m], uf[rr][m], (f4)(0.f));
      else y[s] = mfma16(kf[rr][s - m], uf[rr][m], y[s]);
      fence_sched();
      if constexpr (rr < 3 && v % 4 == 2 && v / 4 < 8) rd(rr + 1, v / 4);
      if constexpr ((PF & 64) && nn >= TK0 && (nn - TK0) % TKS == 0 && (nn - TK0) / TKS < 7) touch(tch, vt, rw2, (nn - TK0) / TKS * 262144);
      fence_sched();
    });
  }
  STAMP(6);
  HOLDPT(5);
#pragma unroll
  for (int a = 0; a < 8; ++a) *(LAS f4*)(sm + EX + (wave * 8 + a) * 1024 + lane * 16) = y[a];
  lds_bar();
  {
    const float d0 = bflo(r_dsk), d1 = bfhi(r_dsk);
    const int rn = n < 8 ? 2 * n : 2 * (n - 8) + 1;
#pragma unroll
    for (int i = 0; i < 2; ++i) {
      const int al = 2 * wave + i;
      LAS char* ex = sm + EX + al * 1024 + lane * 16;
      const f4 y0 = *(LAS f4*)(ex) + *(LAS f4*)(ex + 8192), y1 = *(LAS f4*)(ex + 16384) + *(LAS f4*)(ex + 24576);
      LAS char* up = sm + UA0 + UBO + 64 + (256 * al + 2 * rn + 64 * g) * 2;
      const u32 u00 = *(LAS u32*)(up), u01 = *(LAS u32*)(up + 64), u10 = *(LAS u32*)(up + UCH), u11 = *(LAS u32*)(up + UCH + 64);
      auto mix = [&](float y, float u, float d) { return opaque(__builtin_fmaf(u, d, opaque(y))); };
      const u32 x0 = pk2(mix(y0[0], bflo(u00), d0), mix(y1[0], bflo(u10), d1)), x1 = pk2(mix(y0[1], bfhi(u00), d0), mix(y1[1], bfhi(u10), d1));
      const u32 z0 = pk2(mix(y0[2], bflo(u01), d0), mix(y1[2], bflo(u11), d1)), z1 = pk2(mix(y0[3], bfhi(u01), d0), mix(y1[3], bfhi(u11), d1));
      const u32 vo = WS_A + (u32)(4 * al + g) * 131072 + pr * 512 + rn * 16;
      st128_sc1<0>((v4u){x0, tg32, x1, tg32}, vo, rws, 0);
      st128_sc1<256>((v4u){z0, tg32, z1, tg32}, vo, rws, 0);
    }
  }
  STAMP(7);
  if constexpr (CODEPF) asm volatile(".Lpf_end2:");
  if (PF & 64) asm volatile("s_waitcnt vmcnt(4)" :: "v"(tch) : "memory");
  if (ci == 0 && wave == 0) {
#pragma unroll 1
    for (int it = 0; it < SPIN_MAX; ++it) {
      v4u f[4];
      static_for<0, 4>([&](auto I) { f[decltype(I)::value] = ld128_sc1<decltype(I)::value * 1024>(WS_EFLAG + lane * 16, rws, 0); });
      wait_pin<0>(f[0], f[1]); pin(f[2], f[3]);
      u32 m = 0;
#pragma unroll
      for (int i = E0; i < 4; ++i) m |= (f[i].x ^ (u32)tag) | (f[i].y ^ tg32) | (f[i].z ^ (u32)tag) | (f[i].w ^ tg32);
      if (!any(m)) break;
      __builtin_amdgcn_s_sleep(8);
    }
    flag_store(0, rws, tag);
  }
  STAMP_OUT;
}

__global__ __launch_bounds__(256, 2) void fused_kernel(const u16* __restrict__ x, const u16* __restrict__ are, const u16* __restrict__ aim,
                                                       const u16* __restrict__ bre, const u16* __restrict__ bim, const u16* __restrict__ cre,
                                                       const u16* __restrict__ cim, const u16* __restrict__ logdt, const u16* __restrict__ dskip,
                                                       const u16* __restrict__ w1, const u16* __restrict__ b1, const u16* __restrict__ w2,
                                                       const u16* __restrict__ b2, const u16* __restrict__ gw, u16* __restrict__ out, char* ws) {
  __shared__ __attribute__((aligned(256))) char smem[81920];
  LAS char* sm = (LAS char*)smem;
  const int b = blockIdx.x;
  const bool mlp = (b < 256) == (GFIRST != 0);
  u32 tch = 0;
  if constexpr (CODEPF) {
    u64 pc; u32 len;
    asm volatile("s_getpc_b64 %0\n.Lpf_here:\n\ts_max_i32 %1, 0, .Lpf_end0-.Lpf_here\n\ts_max_i32 %1, %1, .Lpf_end1-.Lpf_here\n\t"
                 "s_max_i32 %1, %1, .Lpf_end2-.Lpf_here" : "=s"(pc), "=&s"(len) :: "scc");
    const u32 t = threadIdx.x;
    touch(tch, (t & 63) < 4 ? (((b & 255) >> 3) * 16 + (t >> 6) * 4 + (t & 63)) * 128 : 1u << 30, mkrsrc((const void*)pc, mlp ? 0 : len));
  }
  asm volatile("" :: "s"(x), "s"(are), "s"(aim), "s"(bre), "s"(bim), "s"(cre), "s"(cim), "s"(logdt), "s"(dskip), "s"(w1), "s"(b1),
               "s"(w2), "s"(b2), "s"(gw), "s"(out), "s"(ws));
  if (__builtin_expect(mlp && (b & 255) < 224, 1)) gemm_role<false>(b & 255, sm, x, w1, b1, w2, b2, gw, out, ws, tch);
  else if (mlp) gemm_role<true>(b & 255, sm, x, w1, b1, w2, b2, gw, out, ws, tch);
  else conv_role(b & 255, sm, x, are, aim, bre, bim, cre, cim, logdt, dskip, w1, w2, ws, tch);
}

extern "C" void kernel_launch(void* const* d_in, const int* in_sizes, int n_in, void* d_out, int out_size, void* d_ws, size_t ws_size,
                              hipStream_t stream) {
  const u16* const* in = (const u16* const*)d_in;
  fused_kernel<<<512, 256, 0, stream>>>(in[0], in[1], in[2], in[3], in[4], in[5], in[6], in[7], in[8], in[10], in[11], in[12], in[13],
                                        in[14], (u16*)d_out, (char*)d_ws + (((ws_size - WS_END) / 16 * WSPOS) & ~(size_t)0xfffff));
}
```

```cpp
#include <hip/hip_runtime.h>
#include <stdint.h>
#include <type_traits>
#pragma clang diagnostic ignored "-Winline-asm"

typedef unsigned short u16;
typedef unsigned int u32;
typedef unsigned long long u64;
typedef __attribute__((ext_vector_type(2))) u32 v2u;
typedef __attribute__((ext_vector_type(3))) u32 v3u;
typedef __attribute__((ext_vector_type(4))) u32 v4u;
typedef __attribute__((ext_vector_type(2))) float f2;
typedef __attribute__((ext_vector_type(4))) float f4;
typedef __attribute__((ext_vector_type(16))) float f16v;
typedef __attribute__((ext_vector_type(16))) u32 v16u;
typedef __attribute__((ext_vector_type(2))) __bf16 bf2;
typedef __attribute__((ext_vector_type(8))) __bf16 bf8;
typedef __attribute__((ext_vector_type(2))) short s2;
typedef __attribute__((ext_vector_type(4))) short s4;
typedef __amdgpu_buffer_rsrc_t rsrc_t;
#define DI __device__ __forceinline__
#define LAS __attribute__((address_space(3)))
#define GAS __attribute__((address_space(1)))

#define LAM 4
#define EP0 61
#define EPB 121
#define BMASK 0xAA00
#define TM2 1
#define SPEC0 0
#define SPEC1 0
#define PA 120
#define PC 100
#define PD 8
#define KP 7
#define KC 9
#define KA 14
#define KB 20
#define NT 4
#define PRG1 2
#define PRG2 1
#define PRC0 3
#define PRC1 1
#define PRCV 2
#define PRCB 3
#define UFP 8
#define PF 35
#define CDLY 4
#define ROT2 1
#define PN1 4
#define PN2 1
#define PN3 3
#define HK 0
#define RK 0
#define KO 0
#define OUTA 16

constexpr u32 FSP = 256;
constexpr u32 WS_G = 4096, WS_EFLAG = 65536, WS_UFLAG = WS_EFLAG + 1024 * FSP;
constexpr u32 WS_UT = 1u << 20, WS_A = 3u << 20, WS_H = 7u << 20, WS_END = 15u << 20;
constexpr int SPIN_MAX = 1 << 16;
constexpr int A0 = 0;

constexpr bool CODEPF = PF & 32;
constexpr int E0 = 0;

#define STAMP(i)
#define STAMPQ(i)
#define STAMPC(i)
#define STAMP_DECL
#define STAMP_OUT

DI rsrc_t mkrsrc(u64 p, u32 bytes) {
  rsrc_t r = __builtin_amdgcn_make_buffer_rsrc((void*)p, (short)0, (int)bytes, 0x00020000);
  asm("" : "+s"(r));
  return r;
}
enum { AX, AW1, AB1, AW2, AB2, AGW, AOUT, AWS };
enum { AARE, AAIM, ABRE, ABIM, ACRE, ACIM, ALDT, ADSK };
DI void karg_load() {
  asm volatile("s_load_dwordx16 s[64:79], %0, 0\n\ts_load_dwordx16 s[80:95], %0, 64" :: "s"(__builtin_amdgcn_kernarg_segment_ptr())
               : "s64", "s65", "s66", "s67", "s68", "s69", "s70", "s71", "s72", "s73", "s74", "s75", "s76", "s77", "s78", "s79",
                 "s80", "s81", "s82", "s83", "s84", "s85", "s86", "s87", "s88", "s89", "s90", "s91", "s92", "s93", "s94", "s95");
}
#define KARG_OUT "={s[64:79]}"(ka), "={s[80:95]}"(kb)
DI u64 karg(const v16u& k, int i) { return (u64)k[2 * i] | ((u64)k[2 * i + 1] << 32); }
DI float bflo(u32 v) { return __uint_as_float(v << 16); }
DI float bfhi(u32 v) { return __uint_as_float(v & 0xffff0000u); }
DI u32 pk2(float a, float b) { bf2 r = __builtin_convertvector((f2){a, b}, bf2); return __builtin_bit_cast(u32, r); }
DI float dot2(u32 a, u32 b, float c) { return __builtin_amdgcn_fdot2_f32_bf16(__builtin_bit_cast(bf2, a), __builtin_bit_cast(bf2, b), c, false); }
DI u32 relu2(float a, float b) {
  const u32 q = pk2(a * __builtin_fabsf(a), __builtin_fmaf(b, __builtin_fabsf(b), 0.f));
  return __builtin_bit_cast(u32, __builtin_elementwise_max(__builtin_bit_cast(s2, q), (s2){0, 0}));
}
DI void fence_sched() { asm volatile("" ::: "memory"); __builtin_amdgcn_sched_barrier(0); }

template <int N> DI void wait_vm() { asm volatile("s_waitcnt vmcnt(%0)" :: "n"(N) : "memory"); }
template <int N, int L> DI void wait_bar() { asm volatile("s_waitcnt vmcnt(%0) lgkmcnt(%1)\n\ts_barrier" :: "n"(N), "n"(L) : "memory"); }
template <int L> DI void wait_lds() { asm volatile("s_waitcnt lgkmcnt(%0)" :: "n"(L) : "memory"); }
DI void lds_bar() { asm volatile("s_waitcnt lgkmcnt(0)\n\ts_barrier" ::: "memory"); }

DI void dma2(u32 m0v, u32 v0, u32 v1, rsrc_t rs, u32 so) {
  asm volatile("s_mov_b32 m0, %0\n\ts_nop 0\n\t"
               "buffer_load_dwordx4 %1, %3, %4 offen lds\n\t"
               "buffer_load_dwordx4 %2, %3, %4 offen offset:1024 lds"
               :: "s"(m0v), "v"(v0), "v"(v1), "s"(rs), "s"(so) : "m0", "memory");
}
template <u32 off> DI void set_m0(u32 base) { asm volatile("s_add_u32 m0, %0, %1" :: "s"(base), "n"(off) : "m0", "scc"); }
template <int OFF> DI void dma1(u32 v, rsrc_t rs, u32 so) {
  asm volatile("buffer_load_dwordx4 %0, %1, %2 offen offset:%3 lds" :: "v"(v), "s"(rs), "s"(so), "n"(OFF) : "memory");
}
template <int OFF> DI void reld128_sc1(v4u& r, u32 vo, rsrc_t rs, u32 so) {
  asm volatile("buffer_load_dwordx4 %0, %1, %2, %3 offen offset:%4 sc1" : "+v"(r) : "v"(vo), "s"(rs), "s"(so), "n"(OFF) : "memory");
}
DI void ld32_sc1_x16(u32& r, u32 vo, rsrc_t rs, u32 so) {
  asm volatile("s_mov_b64 exec, 0xffff\n\tbuffer_load_dword %0, %1, %2, %3 offen sc1\n\ts_mov_b64 exec, -1" : "+v"(r) : "v"(vo), "s"(rs), "s"(so) : "memory");
}
template <int OFF> DI v4u ld128_sc1(u32 vo, rsrc_t rs, u32 so) {
  v4u r; asm volatile("buffer_load_dwordx4 %0, %1, %2, %3 offen offset:%4 sc1" : "=&v"(r) : "v"(vo), "s"(rs), "s"(so), "n"(OFF) : "memory"); return r;
}
DI v2u ld64(u32 vo, rsrc_t rs) {
  v2u r; asm volatile("buffer_load_dwordx2 %0, %1, %2, 0 offen" : "=&v"(r) : "v"(vo), "s"(rs) : "memory"); return r;
}
DI v2u ld64_sc1(u32 vo, rsrc_t rs) {
  v2u r; asm volatile("buffer_load_dwordx2 %0, %1, %2, 0 offen sc1" : "=&v"(r) : "v"(vo), "s"(rs) : "memory"); return r;
}
template <int OFF> DI u32 ld16(u32 vo, rsrc_t rs) {
  u32 r; asm volatile("buffer_load_ushort %0, %1, %2, 0 offen offset:%3" : "=&v"(r) : "v"(vo), "s"(rs), "n"(OFF) : "memory"); return r;
}
template <int OFF> DI void st128_sc1(v4u d, u32 vo, rsrc_t rs, u32 so) {
  asm volatile("buffer_store_dwordx4 %0, %1, %2, %3 offen offset:%4 sc1\n\ts_nop 1" :: "v"(d), "v"(vo), "s"(rs), "s"(so), "n"(OFF) : "memory");
}
DI void st64_sc1(v2u d, u32 vo, rsrc_t rs) {
  asm volatile("buffer_store_dwordx2 %0, %1, %2, 0 offen sc1" :: "v"(d), "v"(vo), "s"(rs) : "memory");
}
DI void flag_store(u32 off, rsrc_t rs, u64 tag) {
  const v2u d = {(u32)tag, (u32)(tag >> 32)};
  asm volatile("s_mov_b64 exec, 1\n\tbuffer_store_dwordx2 %0, off, %1, %2 sc1\n\ts_mov_b64 exec, -1" :: "v"(d), "s"(rs), "s"(off) : "memory");
}
DI u64 epoch_tag(v2u e) {
  const u64 v = (u64)(u32)__builtin_amdgcn_readfirstlane(e.x) | ((u64)(u32)__builtin_amdgcn_readfirstlane(e.y) << 32);
  return ((v * 0x9E3779B97F4A7C15ull + 0x632BE59BD9B4E019ull) & ~0xffffull) | (~v & 0xffffull);
}
constexpr u32 SGN = 0x80008000u;
DI u32 sign_of(u64 tag, int t) { return ((((u32)tag >> (t >> 1)) ^ t) & 1) ? SGN : 0u; }
DI void strip(v4u& d, u64 ns) {
  u64 a = (u64)d.x | ((u64)d.y << 32), b = (u64)d.z | ((u64)d.w << 32);
  a += ns; b += ns;
  d = (v4u){(u32)a, (u32)(a >> 32), (u32)b, (u32)(b >> 32)};
}
DI u32 ne3(u32 a, u32 b, u32 c) { return __builtin_amdgcn_bitop3_b32(a, b, c, 0x7e); }
DI u32 fold(v4u d, u32 m) { return (m | d.x | d.y) | (d.z | d.w); }
DI bool any(u32 m) { return __builtin_amdgcn_ballot_w64(m != 0) != 0; }
DI bool flag_bad(v2u v, u64 tag) { return __builtin_amdgcn_ballot_w64((v.x != (u32)tag) | (v.y != (u32)(tag >> 32))) != 0; }
template <int SL = 1> DI void flag_wait(u32 vo, rsrc_t rs, u64 tag) {
#pragma unroll 1
  for (int it = 0; it < SPIN_MAX; ++it) {
    v2u v; asm volatile("buffer_load_dwordx2 %0, %1, %2, 0 offen sc1\n\ts_waitcnt vmcnt(0)" : "=&v"(v) : "v"(vo), "s"(rs) : "memory");
    if (!flag_bad(v, tag)) break;
    __builtin_amdgcn_s_sleep(SL);
  }
}

DI float opaque(float v) { asm("" : "+v"(v)); return v; }

template <int OFF> DI void lds_put_tid(u32 v) { asm volatile("ds_write_addtid_b32 %0 offset:%1" :: "v"(v), "n"(OFF) : "memory"); }
DI bf8 lds_rd128(LAS char* p) { return *(LAS bf8*)p; }
DI v2u lds_tr(LAS char* p) { return __builtin_bit_cast(v2u, __builtin_amdgcn_ds_read_tr16_b64_v4i16((LAS s4*)p)); }
DI bf8 join(v2u x, v2u y) { return __builtin_bit_cast(bf8, (v4u){x.x, x.y, y.x, y.y}); }
DI bf8 lds_tr2(LAS char* p0, LAS char* p1) {
  s4 a = __builtin_amdgcn_ds_read_tr16_b64_v4i16((LAS s4*)p0);
  s4 b = __builtin_amdgcn_ds_read_tr16_b64_v4i16((LAS s4*)p1);
  v2u x = __builtin_bit_cast(v2u, a), y = __builtin_bit_cast(v2u, b);
  return __builtin_bit_cast(bf8, (v4u){x.x, x.y, y.x, y.y});
}
DI f16v mfma32(bf8 a, bf8 b, f16v c) { return __builtin_amdgcn_mfma_f32_32x32x16_bf16(a, b, c, 0, 0, 0); }
DI f4 mfma16(bf8 a, bf8 b, f4 c) { return __builtin_amdgcn_mfma_f32_16x16x32_bf16(a, b, c, 0, 0, 0); }

template <int I, int N, class F> DI void static_for(F&& f) {
  if constexpr (I < N) { f(std::integral_constant<int, I>{}); static_for<I + 1, N>(f); }
}

constexpr u32 REG = 20480;
constexpr int wblk(int t) { constexpr int b[16] = {6, 7, 9, 8, 6, 7, 9, 8, 6, 4, 5, 7, 0, 1, 9, 2}; return b[t]; }
constexpr int rblk(int k) { constexpr int b[10] = {3, 8, 6, 4, 5, 7, 0, 1, 9, 2}; return b[k % 10]; }
constexpr u32 w2row(int k) { return 64 * (k / 14) + 8 * (k & 1); }
constexpr int mn(int a, int b) { return a < b ? a : b; }

constexpr int TMN = TM2 ? 112 : 120;
constexpr int g1st(int n) { return TM2 && n >= 112 ? 14 + ((n >> 2) & 1) : n >> 3; }
constexpr int g1ms(int n) { return n >= TMN || (n >> 3) == 7 ? n & 3 : (n & 7) >> 1; }
constexpr int g1mt(int n) { return TM2 && n >= 112 ? (n >> 3) & 1 : (n & 63) >= 56 ? (n >> 2) & 1 : n & 1; }
constexpr int rdpos(int m, int e) {
  if (m >= 64) return m - LAM;
  const int h = 2 * (m & 7) + e;
  return 8 * (m >> 3) - 4 + (h < 12 ? h / 3 : 4 + (h - 12) / 2);
}
constexpr bool g1bar(int T) { return T <= 8 || (BMASK >> T & 1); }
constexpr int g1need(int T) {
  int nb = T + 1, s = T;
  while (nb <= 16 && !g1bar(nb)) ++nb;
  for (int m = 0; m < 128; ++m) if (rdpos(m, 0) < 8 * nb - 4 && g1st(m) > s) s = g1st(m);
  return mn(s, 15);
}
constexpr int upos(int u) { return u < 16 ? EP0 + u : TM2 ? (u < 24 ? mn(EPB + u - 16, 127) : 128) : mn(125 + (u - 16) / 2, 128); }
constexpr int hstep(int i) { return i < 2 * (28 - NT) ? i >> 1 : 28 - NT + (i - 2 * (28 - NT)) % NT; }
constexpr int hsub(int i) { return i < 2 * (28 - NT) ? i & 1 : (i - 2 * (28 - NT)) / NT; }

enum { OX, OW, OR, OS, OE, OH, OP, OA };
struct Op { int pos, kind, idx, sub; };
constexpr int G2P = 200, NA = 7;
constexpr bool own_m0(int pos) { return pos < 0 || (pos >= 128 && pos < G2P); }
struct OpTab {
  Op op[256]; int n;
  constexpr void add(int pos, int kind, int idx, int sub) { op[n] = Op{pos, kind, idx, sub}; ++n; }
  constexpr int at(int kind, int idx, int sub) const {
    for (int i = 0; i < n; ++i) if (op[i].kind == kind && op[i].idx == idx && op[i].sub == sub) return i;
    return -1;
  }
  constexpr int from(int pos) const { int i = 0; while (i < n && op[i].pos < pos) ++i; return i; }
  constexpr int younger(int kind, int idx, int sub, int pos) const {
    int c = 0, a = at(kind, idx, sub);
    if (a < 0 || op[a].pos >= pos) return -1000;
    for (int i = a + 1; i < n; ++i) if (op[i].pos < pos) ++c;
    return c > 63 ? 63 : c;
  }
  constexpr bool before(int kind, int idx, int sub, int pos) const { return op[at(kind, idx, sub)].pos < pos; }
  constexpr int lds(int i) const {
    const Op o = op[i];
    return o.kind == OX ? (o.idx % 3) * 4096 + (o.sub >> 1) * 2048 : o.kind == OW ? wblk(o.idx) * 2048 : o.kind == OR ? rblk(o.idx) * 2048 : -1;
  }
  constexpr int m0_at(int pos) const {
    int cur = -1;
    for (int i = 0; i < n; ++i) if (lds(i) >= 0) { if (op[i].pos == pos) return lds(i) != cur ? lds(i) : -1; cur = lds(i); }
    return -1;
  }
  constexpr bool m0_ok() const {
    for (int i = 0; i < n; ++i) for (int j = i + 1; j < n; ++j)
      if (op[i].pos == op[j].pos && !own_m0(op[i].pos) && lds(i) >= 0 && lds(j) >= 0 && lds(i) != lds(j)) return false;
    return true;
  }
};
template <bool AUX> constexpr OpTab optab() {
  OpTab t{};
  int nh = 0;
  if (!AUX) {
    for (int c = 0; c < 3; ++c) { for (int s = 0; s < 4; ++s) t.add(-1, OX, c, s); t.add(-1, OW, c, 0); t.add(-1, OW, c, 1); }
    t.add(-1, OW, 3, 0); t.add(-1, OW, 3, 1);
    int nr = 4, done = 7;
    for (int n = 0; n < 128; ++n) {
      const int T = (n + 4) / 8, i = (n + 4) % 8, s = i - 3;
      if (T == 0) t.add(n, OX, 3, i - 4);
      if (T >= 1 && T <= 8 && i < 2) { if (T == 8) t.add(n, OR, 1, i); else t.add(n, OW, T <= 5 ? T + 3 : 3 * T - 7, i); }
      if (T >= 1 && T <= 7 && s >= 0 && s < 4) {
        if (T <= 4) t.add(n, OX, T + 3, s);
        else if (T == 7 && s >= 2) t.add(n, OR, 0, s & 1);
        else t.add(n, OW, 3 * T - 6 + s / 2, s & 1);
      }
      if (T > 8 && i == 0 && g1bar(T)) done = mn(T - 2, TM2 ? 13 : 15);
      const bool ring = nr / 2 + 6 <= done;
      if (ring) { t.add(n, OR, nr >> 1, nr & 1); ++nr; }
      for (int q = 0; q < 8; ++q) if (upos(4 * q + 3) == n) t.add(n, OS, q, 0);
      if (n == upos(15) + 1) t.add(n, OE, 0, 0);
      if (n == PA) t.add(n, OP, 0, 0);
      if (SPEC0 && n >= PC && !ring && nh < 2 * PD) { t.add(n, OH, hstep(nh), hsub(nh)); ++nh; }
    }
    for (int q = 0; q < 8; ++q) if (upos(4 * q + 3) == 128) t.add(128 + q, OS, q, 0);
    if (nr & 1) { t.add(140, OR, nr >> 1, 1); ++nr; }
    for (; nh < 2 * PD; ++nh) t.add(141, OH, hstep(nh), hsub(nh));
    for (; nr < 20; ++nr) t.add(142, OR, nr >> 1, nr & 1);
  } else {
    for (; nh < 2 * PD; ++nh) t.add(-1, OH, hstep(nh), hsub(nh));
  }
  int na = 0;
  for (int k = 0; k < 28 - NT; ++k)
    for (int g = 0; g < 4; ++g) {
      const int pos = G2P + 4 * k + g;
      const bool ring = (g == 1 || g == 2) && k + 10 < 28;
      if (ring) t.add(pos, OR, k + 10, g - 1);
      if (SPEC1 ? k == KC - 1 && g == 3 : k == KP && g == 0) t.add(pos, OP, 1, 0);
      const int lim = mn(k < KC ? 28 : 56, 2 * (k + PD) + (g == 3 ? 2 : 1));
      int room = ring ? 0 : 1;
      if (k >= KC && nh + 2 < lim) ++room;
      for (; room > 0 && nh < lim; --room, ++nh) t.add(pos, OH, hstep(nh), hsub(nh));
      if (k >= KA && na < NA) { t.add(pos, OA, 0, na); ++na; }
    }
  return t;
}
constexpr OpTab GT[2] = {optab<false>(), optab<true>()};
constexpr int g1wait(int T) { return mn(GT[0].younger(OW, g1need(T), 1, T ? 8 * T - 4 : 0), T ? 63 : GT[0].younger(OX, 0, 3, 0)); }
template <bool AUX> constexpr int g2wait(int k) {
  const int pos = G2P + 4 * k;
  const bool last = k == 27 - NT;
  return mn(last ? GT[AUX].younger(OH, 27, 0, pos) : GT[AUX].younger(OH, k + 1, 1, pos),
            (AUX && k + 1 < 10) ? 63 : GT[AUX].younger(OR, last ? 27 : k + 1, 1, pos));
}
constexpr int G2E = G2P + 4 * (28 - NT), KG = SPEC1 ? 13 : KC;
template <bool AUX> constexpr bool sched_ok() {
  for (int k = 0; k < 28 - NT; ++k) if (g2wait<AUX>(k) < 0) return false;
  for (int T = 0; T < 16; ++T) if (g1bar(T) && g1wait(T) < 0) return false;
  for (int T = 0; T < 7; ++T) if (GT[0].younger(OX, T + 1, 3, 8 * T + 2) < 0) return false;
  return GT[AUX].m0_ok() && GT[AUX].younger(OA, 0, NA - 1, G2P + 4 * KB) >= 0 && GT[AUX].younger(OP, 1, 0, G2P + 4 * KG) >= 0 &&
         GT[AUX].younger(OH, 27, 1, G2E) >= 0;
}
static_assert(sched_ok<false>() && sched_ok<true>() && GT[0].at(OW, 15, 1) < GT[0].at(OS, 0, 0), "schedule");
static_assert((SPEC1 || KP < KC) && KC < 14 && KB >= 18 && KB < 27 - NT && NT >= 2 && NT <= 4 && PD >= NT && PD <= 14 && LAM == 4, "schedule");
static_assert(!(BMASK >> 16) && (!SPEC0 || PA < PC), "schedule");

template <int N> DI void wait_pin(v4u& a, v4u& b) { asm volatile("s_waitcnt vmcnt(%2)" : "+v"(a), "+v"(b) : "n"(N) : "memory"); }
template <int N> DI void wait_pin(v2u& a) { asm volatile("s_waitcnt vmcnt(%1)" : "+v"(a) : "n"(N) : "memory"); }
template <int N> DI void wait_pin(u32& a) { asm volatile("s_waitcnt vmcnt(%1)" : "+v"(a) : "n"(N) : "memory"); }
template <int N> DI void wait_pin(v4u (&a)[6], v2u& b) {
  asm volatile("s_waitcnt vmcnt(%7)" : "+v"(a[0]), "+v"(a[1]), "+v"(a[2]), "+v"(a[3]), "+v"(a[4]), "+v"(a[5]), "+v"(b) : "n"(N) : "memory");
}
DI void pin(v4u& a) { asm volatile("" : "+v"(a)); }
DI void pin(u32& a) { asm volatile("" : "+v"(a)); }
template <int I> using IC = std::integral_constant<int, I>;

DI void touch(u32& d, u32 vo, rsrc_t rs) { asm volatile("buffer_load_dword %0, %1, %2, 0 offen" : "+v"(d) : "v"(vo), "s"(rs) : "memory"); }

template <bool AUX>
DI void gemm_role(int b, LAS char* sm) {
  v16u ka, kb;
  constexpr const OpTab& TB = GT[AUX];
  [[maybe_unused]] constexpr int role_ = AUX;
  u32 lane = threadIdx.x & 63;
  const int wave = __builtin_amdgcn_readfirstlane(threadIdx.x >> 6);
  STAMP_DECL;
  STAMP(0);
  const int xcd = b & 7, r = b >> 3;
  const u32 mreg = (u32)(uintptr_t)sm + wave * REG;
  const int t2 = 4 * xcd + (r >> 3), c2 = r & 7;
  const int tau = 4 * (xcd >> 1) + r / 7, j = 7 * (xcd & 1) + r % 7, rot = (r % 7) + 2 * (r / 7);

  u32 wch = ((lane & 7) ^ ((lane >> 2) & 4)) << 4;
  u32 vx0 = (u32)(128 * tau + 32 * wave + (lane >> 3)) * 1024 + (((lane & 7) ^ (lane >> 4)) << 4), vx1 = (vx0 ^ 64) + 7168;
  u32 vw0 = (u32)(16 * wave + (lane >> 3)) * 3584 + j * 256 + wch, vw1 = vw0 + 8 * 3584 - 1024;
  u32 vb = (u32)(128 * j + (lane & 32) + 16 * ((lane >> 2) & 1) + 4 * ((lane >> 3) & 3) + (lane & 3)) * 2, zero = 0;
  if constexpr (!AUX) asm volatile("s_waitcnt lgkmcnt(0)" : KARG_OUT, "+v"(vx0), "+v"(vx1), "+v"(vw0), "+v"(vw1), "+v"(vb), "+v"(zero));
  else asm volatile("s_waitcnt lgkmcnt(0)" : KARG_OUT, "+v"(zero));
  const u64 ws = karg(ka, AWS);
  const rsrc_t rws = mkrsrc(ws, WS_END), rx = mkrsrc(karg(ka, AX), 2048 * 512 * 2), rw1 = mkrsrc(karg(ka, AW1), 512 * 1792 * 2);
  const rsrc_t rw2 = mkrsrc(karg(ka, AW2), 1792 * 512 * 2), rb2 = mkrsrc(karg(ka, AB2), 512 * 2);

  u64 tag;
  u32 sg, pf, vv0, vv1, vh, vhh[2], vp, va, vg;
  v4u hd[8], hb[28][2], av[6];
  v2u bv;
  const int rot2 = ROT2 ? (c2 + 2 * (r >> 3)) % 7 : 0;
  u32 ho[7][2], wo[7];
  auto hload = [&](auto K, auto S, auto AGAIN) {
    constexpr int k = decltype(K)::value, s = decltype(S)::value;
    const u32 so = ho[HK ? 0 : (k % 14) >> 1][s];
    if constexpr (decltype(AGAIN)::value) reld128_sc1<(k & 1) * 1024>(hb[k][s], vhh[k / 14], rws, so);
    else hb[k][s] = ld128_sc1<(k & 1) * 1024>(vhh[HK ? 0 : k / 14], rws, so);
  };
  auto aload = [&](auto S) {
    constexpr int s = decltype(S)::value;
    if constexpr (s < 4) av[s] = ld128_sc1<s * 512>(va, rws, 0);
    else if constexpr (s < 6) av[s] = ld128_sc1<(s - 4) * 16>(vg, rws, 0);
    else bv = ld64((u32)(64 * c2 + 4 * (lane & 15)) * 2, rb2);
  };
  auto issue = [&](auto I) {
    constexpr Op o = TB.op[decltype(I)::value];
    constexpr int lo = (o.sub & 1) * 1024;
    if constexpr (own_m0(o.pos) && TB.lds(decltype(I)::value) >= 0 && lo == 0) { set_m0<TB.lds(decltype(I)::value)>(mreg); asm volatile("s_nop 0"); }
    if constexpr (o.kind == OX) dma1<lo>(lo ? vx1 : vx0, rx, (u32)((o.idx + rot) & 7) * 128 + (o.sub >> 1) * 16384);
    if constexpr (o.kind == OW) dma1<lo>(lo ? vw1 : vw0, rw1, (u32)((o.idx + rot) & 7) * (64 * 3584) + (o.idx >> 3) * 128);
    if constexpr (o.kind == OR) dma1<lo>(lo ? vv1 : vv0, rw2, RK ? wo[0] : wo[(o.idx % 14) >> 1] + w2row(o.idx) * 1024);
    if constexpr (o.kind == OS) st128_sc1<(o.idx & 3) * 1024>(hd[o.idx], vh, rws, (o.idx >> 2) * 4096);
    if constexpr (o.kind == OE) flag_store(WS_EFLAG + (u32)b * 8, rws, tag);
    if constexpr (o.kind == OP) ld32_sc1_x16(pf, vp, rws, o.idx * 4096);
    if constexpr (o.kind == OH) hload(IC<o.idx>{}, IC<o.sub>{}, IC<0>{});
    if constexpr (o.kind == OA) aload(IC<o.sub>{});
    if constexpr (o.pos < 0 && o.kind == OW && o.sub == 1 && o.idx < 2) asm volatile("s_barrier");
  };
  auto issue_at = [&](auto N) {
    static_for<TB.from(decltype(N)::value), TB.from(decltype(N)::value + 1)>(issue);
  };
  auto m0_for = [&](auto N) { if constexpr (TB.m0_at(decltype(N)::value) >= 0) set_m0<TB.m0_at(decltype(N)::value)>(mreg); };
  auto gate = [&](auto PH, auto K0, auto POS) {
    if (__builtin_expect(any((pf ^ sg) & SGN), 0)) {
#pragma unroll 1
      for (int it = 0; it < SPIN_MAX; ++it) {
        __builtin_amdgcn_s_sleep(1);
        ld32_sc1_x16(pf, vp, rws, decltype(PH)::value * 4096);
        wait_pin<0>(pf);
        if (!any((pf ^ sg) & SGN)) break;
      }
      static_for<2 * decltype(K0)::value, 56>([&](auto I) {
        constexpr int k = decltype(I)::value >> 1, s = decltype(I)::value & 1;
        if constexpr (TB.before(OH, k, s, decltype(POS)::value)) hload(IC<k>{}, IC<s>{}, IC<1>{});
      });
      if constexpr (TB.before(OH, decltype(K0)::value, 0, decltype(POS)::value)) wait_vm<0>();
    }
  };

  v2u ep;
  u32 bq0, bq1;
  if constexpr (!AUX) {
    const rsrc_t rb1 = mkrsrc(karg(ka, AB1), 1792 * 2);
    ep = ld64_sc1(zero, rws);
    bq0 = ld16<0>(vb, rb1); bq1 = ld16<128>(vb, rb1);
    issue_at(IC<-1>{});
    __builtin_amdgcn_s_setprio(PRG1);
  } else {
    ep = ld64_sc1(zero, rws);
  }
  pin(lane);
  fence_sched();
  const int g = lane >> 4, q = (lane >> 2) & 3, p = lane & 3, l31 = lane & 31, hh = lane >> 5, l3 = lane >> 3, l7 = lane & 7;
  u32 aoff[2];
#pragma unroll
  for (int mi = 0; mi < 2; ++mi) aoff[mi] = (8 * (g >> 1) + q) * 128 + (((4 * mi + 2 * (p & 1) + (g & 1)) ^ ((q >> 1) << 2)) << 4) + (p >> 1) * 8;
  vv0 = (u32)(896 * (wave >> 1) + 32 * (wave & 1) + l3) * 1024 + c2 * 128 + wch; vv1 = vv0 + 15360;
  vh = WS_H + (u32)((4 * tau + wave) * 112 + 8 * j) * 1024 + lane * 16;
  vhh[0] = WS_H + (u32)(2 * t2 * 112 + 56 * (wave >> 1) + 2 * (wave & 1)) * 1024 + lane * 16; vhh[1] = vhh[0] + 4096;
  vp = vhh[0] + (u32)((l3 & 1) * 112 + 8 * min(l7, 6) + 1) * 1024;
  va = WS_A + (u32)t2 * 131072 + (u32)(32 * c2 + 4 * l3) * 512 + (u32)(16 * wave + 2 * l7) * 8;
  vg = WS_G + (u32)(64 * t2 + 16 * wave + 4 * g) * 8;
  u32 sgn = SGN;
  asm("" : "+s"(sgn));
  pin(vv0); pin(vv1); pin(vh); pin(vhh[0]); pin(vhh[1]); pin(vp); pin(va); pin(vg);
#pragma unroll
  for (int i = 0; i < 7; ++i) {
    const u32 pr = i + rot2 >= 7 ? i + rot2 - 7 : i + rot2;
    u32 h0 = pr * 8192, h1 = h0 + 114688, w0 = pr * 131072;
    asm volatile("" : "+s"(h0), "+s"(h1), "+s"(w0));
    ho[i][0] = h0; ho[i][1] = h1; wo[i] = w0;
  }

  if constexpr (!AUX) {
    f16v acc[4];
    LAS char* pa[2][2]; LAS char* pb[4];
#pragma unroll
    for (int mi = 0; mi < 2; ++mi) { u32 hi = aoff[mi] + 2 * REG; pin(hi); pa[mi][0] = sm + aoff[mi]; pa[mi][1] = sm + hi; }
#pragma unroll
    for (int u = 0; u < 4; ++u) pb[u] = sm + wave * REG + l31 * 128 + (((2 * u + hh) ^ ((l31 >> 1) & 7)) << 4);

    asm volatile("s_waitcnt vmcnt(20)" : "+v"(ep), "+v"(bq0), "+v"(bq1) :: "memory");
    tag = epoch_tag(ep);
    pf = sg = sign_of(tag, l31);
    {
      const bf8 o0 = __builtin_bit_cast(bf8, (v4u){hh ? 0u : 0x3f80u, 0, 0, 0}), o1 = __builtin_bit_cast(bf8, (v4u){hh ? 0x3f80u : 0u, 0, 0, 0});
      const bf8 a0 = __builtin_bit_cast(bf8, (v4u){bq0, 0, 0, 0}), a1 = __builtin_bit_cast(bf8, (v4u){bq1, 0, 0, 0});
      acc[0] = mfma32(a0, o0, (f16v)(0.f)); acc[1] = mfma32(a0, o1, (f16v)(0.f));
      acc[2] = mfma32(a1, o0, (f16v)(0.f)); acc[3] = mfma32(a1, o1, (f16v)(0.f));
    }
    v2u af[128][2];
    bf8 xf[32];
    auto rda_at = [&](auto N) {
      constexpr int n = decltype(N)::value, m0_ = n < 0 ? 0 : n;
      static_for<2 * m0_, mn(2 * m0_ + 24, 256)>([&](auto H) {
        constexpr int m = decltype(H)::value >> 1, e = decltype(H)::value & 1, u = g1ms(m);
        if constexpr (n < 0 ? rdpos(m, e) < 0 : rdpos(m, e) == n)
          af[m][e] = lds_tr(pa[g1mt(m)][u >> 1] + (u & 1) * REG + wblk(g1st(m)) * 2048 + e * 512);
      });
    };
    auto rdx = [&](auto S) { constexpr int s = decltype(S)::value; xf[s] = lds_rd128(pb[s & 3] + ((s >> 2) % 3) * 4096); };
    auto unit = [&](auto U) {
      constexpr int un = decltype(U)::value, tl = un >> 3, e = (un & 7) * 2;
      const float v0 = acc[tl][e], v1 = acc[tl][e + 1];
      hd[un >> 2][un & 3] = relu2(v0, v1);
    };
    auto mark = [&](auto U) { constexpr int un = decltype(U)::value; hd[un >> 2][un & 3] |= sg; };
    STAMP(1);
    fence_sched();
    wait_bar<g1wait(0), 15>();
    fence_sched();
    STAMP(2); STAMPC(60);
    static_for<0, 4>(rdx);
    fence_sched();
    rda_at(IC<-1>{});
    static_for<0, 128>([&](auto N) {
      constexpr int n = decltype(N)::value, T = (n + 4) / 8, i = (n + 4) % 8;
      fence_sched();
      if constexpr (i == 0 && g1bar(T)) wait_bar<g1wait(T), T < 8 ? 4 : T == 8 ? 0 : 15>();
      if constexpr (i == 0) { STAMPQ(15 + T); }
      if constexpr (i == 6 && T < 7) wait_vm<TB.younger(OX, T + 1, 3, n)>();
      m0_for(N);
      fence_sched();
      constexpr int tl = 2 * (n >> 6) + g1mt(n);
      acc[tl] = mfma32(join(af[n][0], af[n][1]), xf[4 * (g1st(n) & 7) + g1ms(n)], acc[tl]);
      fence_sched();
      static_for<0, 32>([&](auto U) { if constexpr (upos(decltype(U)::value) == n) unit(U); });
      fence_sched();
      rda_at(N);
      if constexpr (i >= 6 && T < 7) static_for<4 * (T + 1) + 2 * (i - 6), 4 * (T + 1) + 2 * (i - 5)>(rdx);
      fence_sched();
      static_for<0, 32>([&](auto U) { if constexpr (upos(decltype(U)::value) == n) mark(U); });
      fence_sched();
      if constexpr (T == 0 && i == 4) wait_lds<14>();
      if constexpr (T >= 1 && T <= 7 && i == 3) wait_lds<12>();
      issue_at(N);
    });
    fence_sched();
    STAMP(5); STAMPC(61);
    static_for<16, 32>([&](auto U) {
      constexpr int un = decltype(U)::value;
      if constexpr (upos(un) == 128) { unit(U); mark(U); }
      if constexpr ((un & 3) == 3) { fence_sched(); issue_at(IC<128 + (un >> 2)>{}); fence_sched(); }
    });
    STAMP(6);
    issue_at(IC<140>{});
    if constexpr (!SPEC0) { wait_pin<TB.younger(OP, 0, 0, 141)>(pf); gate(IC<0>{}, IC<0>{}, IC<141>{}); }
    STAMP(3);
    issue_at(IC<141>{});
    STAMP(4);
    asm volatile("s_barrier" ::: "memory");
    issue_at(IC<142>{});
    if constexpr (SPEC0) { wait_pin<TB.younger(OP, 0, 0, G2P)>(pf); gate(IC<0>{}, IC<0>{}, IC<G2P>{}); }
  } else {
    const int tg = xcd * 4 + (r - 28);
    const v4u gv = *(const GAS v4u*)(karg(ka, AGW) + lane * 16);
    const GAS char* xr = (const GAS char*)(karg(ka, AX) + (u32)((tg * 64 + wave * 16) * 1024 + lane * 16));
    float s[16];
#pragma unroll
    for (int i = 0; i < 16; ++i) {
      const v4u xv = *(const GAS v4u*)(xr + i * 1024);
      s[i] = 0.f;
#pragma unroll
      for (int k = 0; k < 4; ++k) s[i] = dot2(xv[k], gv[k], s[i]);
    }
#pragma unroll
    for (int i = 0; i < 8; ++i) {
      const v2u t = __builtin_amdgcn_permlane32_swap(__float_as_uint(s[i]), __float_as_uint(s[i + 8]), false, false);
      s[i] = opaque(__uint_as_float(t.x) + __uint_as_float(t.y));
    }
#pragma unroll
    for (int i = 0; i < 4; ++i) {
      const v2u t = __builtin_amdgcn_permlane16_swap(__float_as_uint(s[i]), __float_as_uint(s[i + 4]), false, false);
      float v = __uint_as_float(t.x) + __uint_as_float(t.y);
      v += __int_as_float(__builtin_amdgcn_update_dpp(0, __float_as_int(v), 0x128, 0xf, 0xf, false));
      v += __int_as_float(__builtin_amdgcn_update_dpp(0, __float_as_int(v), 0x124, 0xf, 0xf, false));
      v += __int_as_float(__builtin_amdgcn_update_dpp(0, __float_as_int(v), 0x122, 0xf, 0xf, false));
      v += __int_as_float(__builtin_amdgcn_update_dpp(0, __float_as_int(v), 0x121, 0xf, 0xf, false));
      s[i] = opaque(v);
    }
    const float m01 = opaque(p & 1 ? s[1] : s[0]), m23 = opaque(p & 1 ? s[3] : s[2]), mine = p & 2 ? m23 : m01;
    const float sl = bflo(pk2(mine, 0.f));
    const float gt = bflo(pk2(1.f / (1.f + __expf(-sl)), 0.f));
    fence_sched();
    asm volatile("s_waitcnt vmcnt(0)\n\ts_barrier" : "+v"(ep) :: "memory");
    tag = epoch_tag(ep);
    pf = sg = sign_of(tag, l31);
    if (q == 0) st64_sc1((v2u){__float_as_uint(gt), (u32)(tag >> 32)}, WS_G + (u32)(tg * 64 + wave * 16 + 4 * g + p) * 8, rws);
    STAMP(1);
    static_for<0, 10>([&](auto I) { constexpr int i = decltype(I)::value; dma2(mreg + rblk(i) * 2048, vv0, vv1, rw2, wo[i >> 1] + w2row(i) * 1024); });
    flag_store(WS_EFLAG + (u32)b * 8, rws, tag);
    STAMP(2);
    ld32_sc1_x16(pf, vp, rws, 0);
    wait_pin<0>(pf);
    gate(IC<0>{}, IC<0>{}, IC<-1>{});
    STAMP(4);
    issue_at(IC<-1>{});
  }
  __builtin_amdgcn_s_setprio(PRG2);

  const u32 tg32 = (u32)(tag >> 32);
  const u64 ns = sg ? 0x7fff7fff7fff8000ull : 0;
  LAS char* pv[2] = {sm + wave * REG + aoff[0], sm + wave * REG + aoff[1]};
  auto vfrag = [&](int k, int mi) { LAS char* a = pv[mi] + rblk(k) * 2048; return lds_tr2(a, a + 512); };
  auto fresh = [&](auto K0, auto K1, auto SM, u32 m) {
    constexpr int k0 = decltype(K0)::value, k1 = decltype(K1)::value, sm_ = decltype(SM)::value, ke = sm_ == 3 ? mn(k1 | 1, 27 - NT) : k1;
    if (__builtin_expect(any(m), 0)) {
#pragma unroll 1
      for (int it = 0; it < SPIN_MAX; ++it) {
        static_for<k0, ke + 1>([&](auto K) {
          if constexpr (sm_ & 1) hload(K, IC<0>{}, IC<1>{});
          if constexpr (sm_ & 2) hload(K, IC<1>{}, IC<1>{});
        });
        wait_vm<0>();
        u32 z = 0;
        static_for<2 * k0, 2 * ke + 2>([&](auto I) {
          constexpr int k = decltype(I)::value >> 1, s = decltype(I)::value & 1;
          if constexpr (sm_ >> s & 1) { pin(hb[k][s]); if constexpr (k <= k1) { strip(hb[k][s], ns); z = fold(hb[k][s], z); } }
        });
        if (!any(z & sgn)) break;
        __builtin_amdgcn_s_sleep(1);
      }
    }
  };
  auto abad = [&](int i0, int i1) {
    u32 m = 0;
#pragma unroll
    for (int i = i0 < A0 ? A0 : i0; i < i1; ++i) m |= ne3(av[i].y, av[i].w, tg32);
    return m;
  };
  const int cq = lane & 15;
  LAS char* ap = sm + wave * REG + 9 * 2048 + (cq & 1) * 1024 + (cq >> 1) * 32 + g * 8;
  v2u a2[2][2];
  float ga[4][4], gn[4], bb[4];
  auto aput = [&]() {
    static_for<0, 8>([&](auto I) { constexpr int i = decltype(I)::value; lds_put_tid<9 * 2048 + i * 256>(i & 1 ? av[i >> 1].z : av[i >> 1].x); });
  };
  auto aget = [&]() {
#pragma unroll
    for (int i = 0; i < 4; ++i) a2[i >> 1][i & 1] = *(volatile LAS v2u*)(ap + i * 256);
  };
  auto gate_of = [&](int e) { return __uint_as_float(e & 1 ? av[4 + (e >> 1)].z : av[4 + (e >> 1)].x); };
  auto amix = [&](int u) {
    if (u == 0) { bb[0] = opaque(bflo(bv.x)); bb[1] = opaque(bfhi(bv.x)); bb[2] = opaque(bflo(bv.y)); bb[3] = opaque(bfhi(bv.y)); }
    if (u == 1) for (int e = 0; e < 4; ++e) gn[e] = opaque(1.f - gate_of(e));
    if (u >= 2) {
      const int e = (u - 2) >> 1, h = u & 1;
      const u32 a = e & 2 ? a2[h][e & 1].y : a2[h][e & 1].x;
      ga[e][2 * h] = opaque(__builtin_fmaf(gate_of(e), bflo(a), opaque(gn[e] * bb[2 * h])));
      ga[e][2 * h + 1] = opaque(__builtin_fmaf(gate_of(e), bfhi(a), opaque(gn[e] * bb[2 * h + 1])));
    }
  };
  constexpr int AM0 = 4 * KB + 3, K0 = 28 - NT, TS = 17 * 256;
  u32 pro = ((cq >> 3) + 2 * (wave >> 1)) * TS + (cq & 3) * (1024 + 32) + ((cq >> 2) & 1) * 128 + (wave & 1) * 64 + g * 16;
  u32 vo = (u32)(64 * t2 + 16 * wave + 4 * g) * 1024 + (u32)(64 * c2 + 4 * cq) * 2;
  pin(pro); pin(vo);
  LAS char* pr = sm + pro;
  rsrc_t ro = mkrsrc(karg(ka, AOUT), 2048 * 512 * 2);
  asm volatile("" : "+s"(ro));
  f16v o2[2][2];
  bf8 wb[28][2];
  u32 lm = 0;
  fence_sched();
  wait_pin<mn(TB.younger(OH, 0, 1, G2P), AUX ? 63 : TB.younger(OR, 0, 1, G2P))>(hb[0][0], hb[0][1]);
  wb[0][0] = vfrag(0, 0); wb[0][1] = vfrag(0, 1);
  strip(hb[0][0], ns); strip(hb[0][1], ns);
  fresh(IC<0>{}, IC<0>{}, IC<3>{}, fold(hb[0][1], fold(hb[0][0], 0)) & sgn);
  STAMP(7);
  static_for<0, K0>([&](auto K) {
    constexpr int k = decltype(K)::value;
    constexpr bool last = k == K0 - 1;
    fence_sched();
    if constexpr (k == KG) { wait_pin<TB.younger(OP, 1, 0, G2P + 4 * k)>(pf); gate(IC<1>{}, IC<14>{}, IC<G2P + 4 * k>{}); }
    wait_vm<g2wait<AUX>(k)>();
    if constexpr (last) static_for<K0, 28>([&](auto J) { pin(hb[decltype(J)::value][0]); });
    else { pin(hb[k + 1][0]); pin(hb[k + 1][1]); }
    if constexpr (k == KB) { asm volatile("s_mov_b32 m0, %0" :: "s"(mreg) : "m0"); wait_pin<TB.younger(OA, 0, NA - 1, G2P + 4 * k)>(av, bv); }
    STAMPQ(32 + k);
    u32 m = 0;
    static_for<0, 4>([&](auto G) {
      constexpr int gi = decltype(G)::value, mi = gi & 1, ni = gi >> 1;
      m0_for(IC<G2P + 4 * k + gi>{});
      fence_sched();
      if constexpr (k == 0) o2[mi][ni] = mfma32(wb[k][mi], __builtin_bit_cast(bf8, hb[k][ni]), (f16v)(0.f));
      else o2[mi][ni] = mfma32(wb[k][mi], __builtin_bit_cast(bf8, hb[k][ni]), o2[mi][ni]);
      fence_sched();
      if constexpr (last) {
        if constexpr (gi < NT) { wb[K0 + gi][0] = vfrag(K0 + gi, 0); strip(hb[K0 + gi][0], ns); m = fold(hb[K0 + gi][0], m); }
        if constexpr (gi == 3) m &= sgn;
      } else {
        if constexpr (gi == 0) { wb[k + 1][0] = vfrag(k + 1, 0); wb[k + 1][1] = vfrag(k + 1, 1); }
        if constexpr (gi == 1) { strip(hb[k + 1][0], ns); const v4u d = hb[k + 1][0]; m = d.x | d.y | d.z; pin(m); }
        if constexpr (gi == 2) { strip(hb[k + 1][1], ns); m = m | hb[k + 1][0].w | hb[k + 1][1].x; pin(m); }
        if constexpr (gi == 3) { const v4u d = hb[k + 1][1]; m = m | d.y | d.z; pin(m); m = (m | d.w) & sgn; }
      }
      if constexpr (k == KB && gi == 0) aput();
      if constexpr (k == KB && gi == 1) { aget(); lm = abad(0, 3); pin(lm); }
      if constexpr (k == KB && gi == 2) { lm |= abad(3, 6); pin(lm); }
      if constexpr (constexpr int u = 4 * k + gi - AM0; u >= 0 && u < 10) amix(u);
      fence_sched();
      issue_at(IC<G2P + 4 * k + gi>{});
      if constexpr (gi == 3) { if constexpr (last) fresh(IC<K0>{}, IC<27>{}, IC<1>{}, m); else fresh(IC<k + 1>{}, IC<k + 1>{}, IC<3>{}, m); }
    });
  });
  auto pput = [&](auto I) {
    constexpr int tl = decltype(I)::value >> 4, i = decltype(I)::value & 15;
    lds_put_tid<tl * TS + i * 256 + 32 * (i >> 2)>(__float_as_uint(o2[tl & 1][tl >> 1][i]));
  };
  constexpr auto ppos = [](int i) constexpr { return mn(((i >> 4) + 1) * NT + 1 + (i & 15) * NT / 16, 4 * NT); };
  u32 m = 0;
  static_for<0, 4 * NT>([&](auto N) {
    constexpr int n = decltype(N)::value, tl = n / NT, v = n % NT, k = K0 + v;
    fence_sched();
    if constexpr (n == NT) { wait_vm<TB.younger(OH, 27, 1, G2E)>(); static_for<K0, 28>([&](auto J) { pin(hb[decltype(J)::value][1]); }); }
    o2[tl & 1][tl >> 1] = mfma32(wb[k][tl & 1], __builtin_bit_cast(bf8, hb[k][tl >> 1]), o2[tl & 1][tl >> 1]);
    fence_sched();
    if constexpr (tl == 0) wb[k][1] = vfrag(k, 1);
    if constexpr (tl == 1) { strip(hb[k][1], ns); m = fold(hb[k][1], m); }
    if constexpr (constexpr int u = 4 * K0 + n - AM0; u >= 0 && u < 10) amix(u);
    fence_sched();
    if constexpr (v == 1 && tl >= 1) asm volatile("s_nop %0" :: "n"(tl == 1 ? PN1 : PN2));
    static_for<0, 48>([&](auto I) { if constexpr (ppos(decltype(I)::value) == n) pput(I); });
    if constexpr (n == 2 * NT - 1) fresh(IC<K0>{}, IC<27>{}, IC<2>{}, m & sgn);
  });
  fence_sched();
  STAMP(10); STAMPC(62);
  static_for<0, 48>([&](auto I) { if constexpr (ppos(decltype(I)::value) == 4 * NT) pput(I); });
  asm volatile("s_nop %0" :: "n"(PN3));
  static_for<48, 64>(pput);
#pragma unroll
  for (int i = 0; i < 9; ++i) __builtin_amdgcn_s_setprio(PRG2);
  lds_bar();
  STAMP(11);
  if (__builtin_expect(any(lm), 0)) {
#pragma unroll 1
    for (int it = 0; it < SPIN_MAX; ++it) {
      static_for<0, 6>(aload);
      wait_pin<0>(av, bv);
      if (!any(abad(0, 6))) break;
      __builtin_amdgcn_s_sleep(1);
    }
    aput(); aget();
#pragma unroll
    for (int u = 0; u < 10; ++u) amix(u);
  }
  if constexpr (CODEPF) asm volatile(".Lpf_end%0:" :: "n"((int)AUX));
  {
    f4 t[4][4], s[4];
#pragma unroll
    for (int i = 0; i < 16; ++i) t[i >> 2][i & 3] = *(LAS f4*)(pr + (i >> 2) * 256 + (i & 3) * REG);
    fence_sched();
#pragma unroll
    for (int c = 0; c < 4; ++c) s[c] = (t[c][0] + t[c][1]) + (t[c][2] + t[c][3]);
#pragma unroll
    for (int e = 0; e < 4; ++e) {
      auto mix = [&](int c) { const float t = s[c][e]; return opaque(__builtin_fmaf(gn[e], t, ga[e][c])); };
      __builtin_amdgcn_raw_buffer_store_b64((v2u){pk2(mix(0), mix(1)), pk2(mix(2), mix(3))}, ro, vo + e * 1024, 0, OUTA);
    }
  }
  STAMP(12);
  STAMP_OUT;
}

struct cf { float x, y; };
DI cf cmul(cf a, cf m) { return cf{opaque(__builtin_fmaf(a.x, m.x, -opaque(a.y * m.y))), opaque(__builtin_fmaf(a.x, m.y, opaque(a.y * m.x)))}; }
DI cf cexp_s(float lr, float rv, float m) {
  const float e = __builtin_amdgcn_exp2f(lr * m), f = __builtin_amdgcn_fractf(rv * m);
  return cf{opaque(e * __builtin_amdgcn_cosf(f)), opaque(e * __builtin_amdgcn_sinf(f))};
}

constexpr u32 TROW = 544, TCH = 63 * TROW, XT = 69632;
constexpr u32 KCH = 9472, KOO = 4656, UA0 = 18944, UCH = 8768, UBO = 4224, EX = 40960;

constexpr int NPF = !!(PF & 1) + !!(PF & 2);

DI void conv_role(int ci, LAS char* sm) {
  v16u ka, kb;
  const int tid = threadIdx.x, lane = tid & 63, wave = __builtin_amdgcn_readfirstlane(tid >> 6);
  [[maybe_unused]] constexpr int role_ = 2;
  STAMP_DECL;
  STAMP(0);
  u32 tch = 0;
  if constexpr (CODEPF) {
    u64 pc; u32 len;
    asm volatile("s_getpc_b64 s[2:3]\n.Lpf_here:\n\ts_sub_u32 s2, s2, .Lpf_here-.Lpf_start\n\ts_subb_u32 s3, s3, 0\n\ts_mov_b64 %0, s[2:3]\n\t"
                 "s_max_i32 %1, 0, .Lpf_end0-.Lpf_start\n\ts_max_i32 %1, %1, .Lpf_end1-.Lpf_start\n\ts_max_i32 %1, %1, .Lpf_end2-.Lpf_start"
                 : "=s"(pc), "=&s"(len) :: "scc", "s2", "s3");
    touch(tch, lane < 4 ? ((ci >> 3) * 16 + wave * 4 + lane) * 128 : 1u << 30, mkrsrc(pc, len));
  }
  if (CDLY) __builtin_amdgcn_s_sleep(CDLY);
  asm volatile("s_waitcnt lgkmcnt(0)" : KARG_OUT);
  const u64 ws = karg(ka, AWS);
  const rsrc_t rws = mkrsrc(ws, WS_END), rx = mkrsrc(karg(ka, AX), 2048 * 512 * 2);
  v2u ep = ld64_sc1(0, rws);
  __builtin_amdgcn_s_setprio(PRC0);
  const u32 smb = (u32)(uintptr_t)sm;
  const int pr = (ci & 7) * 32 + (ci >> 3), cg = (ci >> 3) & 7, tt = 4 * (ci & 7) + (ci >> 6);
  const int g = lane >> 4, q = (lane >> 2) & 3, p = lane & 3, n = lane & 15;
  const int ch = wave >> 1, half = wave & 1;

  {
    const u32 v0 = (u32)(64 * tt + 16 * wave + (lane >> 3)) * 1024 + cg * 128 + (((lane & 7) ^ ((g & 1) << 1)) << 4);
    const u32 v1 = (v0 ^ 64) + 7168;
    dma2(smb + XT + wave * 2048, v0, v1, rx, 0);
  }
  const int pi = pr * 256 + tid;
  auto par = [&](int a, int i) { return (u32)((const GAS u16*)karg(kb, a))[i]; };
  u32 r_are = par(AARE, pi), r_aim = par(AAIM, pi), r_bre = par(ABRE, pi), r_bim = par(ABIM, pi), r_cre = par(ACRE, pi), r_cim = par(ACIM, pi);
  u32 r_ldt = par(ALDT, 2 * pr + ch);
  u32 r_dsk = ((const GAS u32*)karg(kb, ADSK))[pr];
  fence_sched();
  asm volatile("s_waitcnt vmcnt(0)" : "+v"(r_are), "+v"(r_aim), "+v"(r_bre), "+v"(r_bim), "+v"(r_cre), "+v"(r_cim), "+v"(r_ldt), "+v"(r_dsk), "+v"(ep) :: "memory");
  const u64 tag = epoch_tag(ep);
  const u32 tg32 = (u32)(tag >> 32);
  fence_sched();
  STAMP(1);
  {
    LAS char* tb = sm + XT + wave * 2048 + (8 * (p & 1) + q) * 128 + (p >> 1) * 8;
    const int sw = (q & 2) | ((p & 1) << 2);
    const u32 vo = WS_UT + (u32)((16 * cg + 2 * g + (q >> 1)) * 256 + 8 * tt + 2 * wave + (q & 1)) * 64 + p * 16;
    const bf8 d0 = lds_tr2(tb + ((g ^ sw) << 4), tb + ((g ^ sw) << 4) + 512);
    const bf8 d1 = lds_tr2(tb + (((4 + g) ^ sw) << 4), tb + (((4 + g) ^ sw) << 4) + 512);
    st128_sc1<0>(__builtin_bit_cast(v4u, d0), vo, rws, 0);
    st128_sc1<0>(__builtin_bit_cast(v4u, d1), vo, rws, 8 * 256 * 64);
  }
  fence_sched();
  {
    const u32 wv = 4 * ci + wave, big = 1u << 30;
    if (PF & 1) touch(tch, lane < 7 ? (wv >> 1) * 3584 + ((wv & 1) * 7 + lane) * 256 + 128 : big, mkrsrc(karg(ka, AW1), 512 * 1792 * 2));
    if (PF & 2) touch(tch, lane < 14 ? (wv * 14 + lane) * 128 : big, mkrsrc(karg(ka, AW2), 512 * 1792 * 2));
  }
  fence_sched();

  __builtin_amdgcn_s_setprio(PRC1);
  const u32 uf = WS_UFLAG + (u32)(cg * 128 + tt * 4 + wave) * FSP;
  {
    const float dt = bflo(pk2(expf(bflo(r_ldt)), 0.f));
    const float lr = dt * bflo(r_are) * 1.44269504088896341f, rv = dt * bflo(r_aim) * 0.15915494309189535f;
    const cf db = {opaque(dt * bflo(r_bre)), opaque(-dt * bflo(r_bim))};
    const cf cb = cmul(cf{bflo(r_cre), -bflo(r_cim)}, db);
    const cf e16 = cexp_s(lr, rv, 16.f), e1 = cexp_s(lr, -rv, 1.f), e256 = cexp_s(lr, -rv, 256.f);
    const cf e2 = cexp_s(lr, -rv, 2.f), e4 = cexp_s(lr, -rv, 4.f), e8 = cexp_s(lr, -rv, 8.f);
    u32 tv[63];
    auto put = [&](int row, cf v) { tv[row] = (KO & 1) ? r_are + row : pk2(v.x, v.y); };
    cf a = e16, t0 = cb, tb[8], v[8];
    put(0, cf{1.f, 0.f}); put(16, t0);
    tb[0] = cb;
#pragma unroll
    for (int i = 1; i < 16; ++i) {
      if (i == UFP) { wait_vm<NPF>(); flag_store(uf, rws, tag); }
      put(i, a); if (i < 15) a = cmul(a, e16);
      t0 = cmul(t0, e1); put(16 + i, t0);
      if (i < 8) { tb[i] = cmul(tb[i - 1], e256); v[i] = tb[i]; }
#pragma unroll
      for (int tl = 1; tl < 8; ++tl) {
        const int base = tl == 1 ? 32 : (tl < 4 ? 41 + 5 * (tl - 2) : 51 + 3 * (tl - 4));
        const int cnt = tl == 1 ? 9 : (tl < 4 ? 5 : 3), k = i - tl;
        if (k >= 0 && k < cnt) {
          if (k > 0) v[tl] = cmul(v[tl], tl == 1 ? e2 : (tl < 4 ? e4 : e8));
          put(base + k, v[tl]);
        }
      }
    }
#pragma unroll
    for (int i = 0; i < 63; ++i) asm volatile("" : "+v"(tv[i]));
    fence_sched();
    __builtin_amdgcn_s_setprio(PRCB);
    asm volatile("s_mov_b32 m0, %0\n\ts_nop 0" :: "s"(smb + ch * TCH + half * 256) : "m0");
    if (!(KO & 2)) static_for<0, 63>([&](auto R) { lds_put_tid<decltype(R)::value * TROW>(tv[decltype(R)::value]); });
    __builtin_amdgcn_s_setprio(PRC1);
  }
  fence_sched();
  if (UFP >= 16) { wait_vm<NPF>(); flag_store(uf, rws, tag); }
  const u32 vf = WS_UFLAG + (u32)((ci & 7) * 128 + wave * 32 + (lane & 31)) * FSP;
  v2u pf = ld64_sc1(vf, rws);
  STAMP(2);
  lds_bar();

  f4 kk[4];
  v4u uv[2];
  {
    LAS char* tb = sm + ch * TCH + g * 16;
    const int m2 = (n + ((n >> 1) & 1)) >> 1, m4 = (n + 1 + ((n >> 2) & 1)) >> 2, m8 = (n + 3 + ((n >> 3) & 1)) >> 3;
    LAS char* pr_[4] = {tb + (half ? 51 + m8 : 16 + n) * TROW, tb + (half ? 54 + m8 : 32 + m2) * TROW,
                        tb + (half ? 57 + m8 : 41 + m4) * TROW, tb + (half ? 60 + m8 : 46 + m4) * TROW};
    LAS char* pe = tb + n * TROW;
#pragma unroll
    for (int i = 0; i < 4; ++i) kk[i] = (f4)(0.f);
#pragma unroll
    for (int ks = 0; ks < 8; ++ks) {
      if (ks == 4) {
        fence_sched();
        wait_pin<0>(pf);
        asm volatile("" :: "v"(tch));
        if (__builtin_expect(flag_bad(pf, tag), 0)) flag_wait(vf, rws, tag);
        STAMP(3);
        const u32 vo = WS_UT + (u32)((pr >> 1) * 256 + 64 * wave + (lane >> 1)) * 64 + ((pr & 1) * 2 + (lane & 1)) * 16;
        uv[0] = ld128_sc1<0>(vo, rws, 0); uv[1] = ld128_sc1<2048>(vo, rws, 0);
        fence_sched();
      }
      if (KO & 4) continue;
      const bf8 e = lds_rd128(pe + ks * 64);
#pragma unroll
      for (int i = 0; i < 4; ++i) kk[i] = mfma16(lds_rd128(pr_[i] + ks * 64), e, kk[i]);
    }
  }
  fence_sched();
  STAMP(4);
  lds_bar();
  if (KO & 8) asm volatile("" :: "v"(kk[0]), "v"(kk[1]), "v"(kk[2]), "v"(kk[3]));
  else {
    LAS char* kr = sm + ch * KCH;
#pragma unroll
    for (int i = 0; i < 4; ++i) {
      const int l0 = 256 * (4 * half + i) + 16 * n + 4 * g;
      const float k0 = opaque(kk[i][0]), k1 = opaque(kk[i][1]), k2 = opaque(kk[i][2]), k3 = opaque(kk[i][3]);
      const u32 a = pk2(k3, k2), bq = pk2(k1, k0), c = pk2(k2, k1);
      *(LAS v2u*)(kr + KOO + 2 * (2044 - l0)) = (v2u){a, bq};
      *(LAS u16*)(kr + 2 * (2051 - l0)) = (u16)a;
      *(LAS u32*)(kr + 2 * (2052 - l0)) = c;
      *(LAS u16*)(kr + 2 * (2054 - l0)) = (u16)(bq >> 16);
    }
    const v4u z = {0, 0, 0, 0};
    if (lane < 29) *(LAS v4u*)(kr + (half ? KOO + 4096 : 4112) + lane * 16) = z;
    else if (lane < 33) *(LAS v4u*)(sm + UA0 + ch * UCH + (half ? UBO : 0) + (lane - 29) * 16 - (half ? 0 : 16)) = z;
    else if (lane == 33 && !half) *(LAS u16*)(kr + 2 * 2055) = 0;
    else if (lane == 34 && !half) *(LAS v3u*)(sm + UA0 + ch * UCH + 48) = (v3u){0, 0, 0};
  }
  STAMP(8);
  asm volatile("s_waitcnt vmcnt(0)" : "+v"(uv[0]), "+v"(uv[1]) :: "memory");
  STAMP(9);
#pragma unroll
  for (int k = 0; k < 2; ++k) {
    if (KO & 8) continue;
    LAS char* ub = sm + UA0 + (lane & 1) * UCH + 64 + (64 * wave + 32 * k + (lane >> 1)) * 16;
    *(LAS v4u*)(ub + UBO) = uv[k];
    *(LAS u32*)(ub - 4) = uv[k].x;
    *(LAS v3u*)(ub) = (v3u){uv[k].y, uv[k].z, uv[k].w};
  }
  lds_bar();
  STAMP(5);
  flag_store(WS_EFLAG + (u32)(256 + ci) * 8, rws, tag);
  __builtin_amdgcn_s_setprio(PRCV);

  f4 y[8];
  if (KO & 16) { for (int i = 0; i < 8; ++i) y[i] = kk[i & 3]; }
  else {
    LAS char* pk = sm + ch * KCH + ((n & 1) ? KOO + 4032 : 4048) - 64 * (n >> 1) + 16 * g - 3584 + 256 * half;
    LAS char* pu = sm + UA0 + ch * UCH + (n < 8 ? 8 * n : UBO + 8 * (n - 8) + 8) + 16 * g + 256 * half;
    bf8 kf[4][8], uf[4][8];
    auto rd = [&](int rr, int i) {
      kf[rr][i] = lds_rd128(pk + 512 * (7 - i) + 64 * rr);
      const v2u lo = *(volatile LAS v2u*)(pu + 512 * i + 64 * rr), hi = *(volatile LAS v2u*)(pu + 512 * i + 64 * rr + 8);
      uf[rr][i] = __builtin_bit_cast(bf8, (v4u){lo.x, lo.y, hi.x, hi.y});
    };
#pragma unroll
    for (int i = 0; i < 8; ++i) { rd(0, i); fence_sched(); }
    static_for<0, 144>([&](auto N) {
      constexpr int nn = decltype(N)::value, rr = nn / 36, v = nn % 36;
      constexpr int s = v < 1 ? 0 : v < 3 ? 1 : v < 6 ? 2 : v < 10 ? 3 : v < 15 ? 4 : v < 21 ? 5 : v < 28 ? 6 : 7, m = v - s * (s + 1) / 2;
      if constexpr (rr == 0 && m == 0) y[s] = mfma16(kf[rr][s - m], uf[rr][m], (f4)(0.f));
      else y[s] = mfma16(kf[rr][s - m], uf[rr][m], y[s]);
      fence_sched();
      if constexpr (rr < 3 && v % 4 == 2 && v / 4 < 8) rd(rr + 1, v / 4);
      fence_sched();
    });
  }
  STAMP(6);
#pragma unroll
  for (int a = 0; a < 8; ++a) { if (KO & 32) asm volatile("" :: "v"(y[a])); else *(LAS f4*)(sm + EX + (wave * 8 + a) * 1024 + lane * 16) = y[a]; }
  lds_bar();
  {
    const float d0 = bflo(r_dsk), d1 = bfhi(r_dsk);
    const int rn = n < 8 ? 2 * n : 2 * (n - 8) + 1;
#pragma unroll
    for (int i = 0; i < 2; ++i) {
      const int al = 2 * wave + i;
      LAS char* ex = sm + EX + al * 1024 + lane * 16;
      const f4 y0 = *(LAS f4*)(ex) + *(LAS f4*)(ex + 8192), y1 = *(LAS f4*)(ex + 16384) + *(LAS f4*)(ex + 24576);
      LAS char* up = sm + UA0 + UBO + 64 + (256 * al + 2 * rn + 64 * g) * 2;
      const u32 u00 = *(LAS u32*)(up), u01 = *(LAS u32*)(up + 64), u10 = *(LAS u32*)(up + UCH), u11 = *(LAS u32*)(up + UCH + 64);
      auto mix = [&](float y, float u, float d) { return opaque(__builtin_fmaf(u, d, opaque(y))); };
      const u32 x0 = pk2(mix(y0[0], bflo(u00), d0), mix(y1[0], bflo(u10), d1)), x1 = pk2(mix(y0[1], bfhi(u00), d0), mix(y1[1], bfhi(u10), d1));
      const u32 z0 = pk2(mix(y0[2], bflo(u01), d0), mix(y1[2], bflo(u11), d1)), z1 = pk2(mix(y0[3], bfhi(u01), d0), mix(y1[3], bfhi(u11), d1));
      const u32 vo = WS_A + (u32)(4 * al + g) * 131072 + pr * 512 + rn * 16;
      st128_sc1<0>((v4u){x0, tg32, x1, tg32}, vo, rws, 0);
      st128_sc1<256>((v4u){z0, tg32, z1, tg32}, vo, rws, 0);
    }
  }
  STAMP(7);
  if constexpr (CODEPF) asm volatile(".Lpf_end2:");
  if (ci == 0 && wave == 0) {
#pragma unroll 1
    for (int it = 0; it < SPIN_MAX; ++it) {
      v4u f[4];
      static_for<0, 4>([&](auto I) { f[decltype(I)::value] = ld128_sc1<decltype(I)::value * 1024>(WS_EFLAG + lane * 16, rws, 0); });
      wait_pin<0>(f[0], f[1]); pin(f[2]); pin(f[3]);
      u32 m = 0;
#pragma unroll
      for (int i = E0; i < 4; ++i) m |= (f[i].x ^ (u32)tag) | (f[i].y ^ tg32) | (f[i].z ^ (u32)tag) | (f[i].w ^ tg32);
      if (!any(m)) break;
      __builtin_amdgcn_s_sleep(8);
    }
    flag_store(0, rws, tag);
  }
  STAMP_OUT;
}

__global__ __launch_bounds__(256, 2) void fused_kernel(const u16* x, const u16* w1, const u16* b1, const u16* w2, const u16* b2, const u16* gw,
                                                       u16* out, char* ws, const u16* are, const u16* aim, const u16* bre, const u16* bim,
                                                       const u16* cre, const u16* cim, const u16* logdt, const u16* dskip) {
  __shared__ __attribute__((aligned(256))) char smem[81920];
  if constexpr (CODEPF) asm volatile(".Lpf_start:");
  karg_load();
  LAS char* sm = (LAS char*)smem;
  const int b = blockIdx.x;
  if (__builtin_expect(b < 224, 1)) gemm_role<false>(b, sm);
  else if (b < 256) gemm_role<true>(b, sm);
  else conv_role(b - 256, sm);
}

extern "C" void kernel_launch(void* const* d_in, const int* in_sizes, int n_in, void* d_out, int out_size, void* d_ws, size_t ws_size,
                              hipStream_t stream) {
  const u16* const* in = (const u16* const*)d_in;
  fused_kernel<<<512, 256, 0, stream>>>(in[0], in[10], in[11], in[12], in[13], in[14], (u16*)d_out, (char*)d_ws, in[1], in[2], in[3], in[4],
                                        in[5], in[6], in[7], in[8]);
}
```

```cpp
#include <hip/hip_runtime.h>
#include <stdint.h>
#include <type_traits>
#pragma clang diagnostic ignored "-Winline-asm"

typedef unsigned short u16;
typedef unsigned int u32;
typedef unsigned long long u64;
typedef __attribute__((ext_vector_type(2))) u32 v2u;
typedef __attribute__((ext_vector_type(3))) u32 v3u;
typedef __attribute__((ext_vector_type(4))) u32 v4u;
typedef __attribute__((ext_vector_type(2))) float f2;
typedef __attribute__((ext_vector_type(4))) float f4;
typedef __attribute__((ext_vector_type(16))) float f16v;
typedef __attribute__((ext_vector_type(16))) u32 v16u;
typedef __attribute__((ext_vector_type(2))) __bf16 bf2;
typedef __attribute__((ext_vector_type(8))) __bf16 bf8;
typedef __attribute__((ext_vector_type(2))) short s2;
typedef __attribute__((ext_vector_type(4))) short s4;
typedef __amdgpu_buffer_rsrc_t rsrc_t;
#define DI __device__ __forceinline__
#define LAS __attribute__((address_space(3)))
#define GAS __attribute__((address_space(1)))
#define CAS __attribute__((address_space(4)))

constexpr int LAM = 4, EP0 = 61, EPB = 121, PA = 120, BMASK = 0xAA00;
constexpr int PD = 8, KP = 3, KC = 6, KA = 16, KB = 22, NT = 4;
constexpr int PRG1 = 2, PRG2 = 1, PRC0 = 3, PRC1 = 1, PRCB = 3, PRCV = 2;

constexpr u32 FSP = 256;
constexpr u32 WS_G = 4096, WS_EFLAG = 65536, WS_UFLAG = WS_EFLAG + 1024 * FSP;
constexpr u32 WS_UT = 1u << 20, WS_A = 3u << 20, WS_H = 7u << 20, WS_END = 15u << 20;
constexpr int SPIN_MAX = 1 << 16;

DI rsrc_t mkrsrc(u64 p, u32 bytes) {
  rsrc_t r = __builtin_amdgcn_make_buffer_rsrc((void*)p, (short)0, (int)bytes, 0x00020000);
  asm("" : "+s"(r));
  return r;
}
enum { AX, AW1, AB1, AW2, AB2, AGW, AOUT, AWS };
enum { AARE, AAIM, ABRE, ABIM, ACRE, ACIM, ALDT, ADSK };
DI void karg_load() {
  asm volatile("s_load_dwordx16 s[64:79], %0, 0\n\ts_load_dwordx16 s[80:95], %0, 64" :: "s"(__builtin_amdgcn_kernarg_segment_ptr())
               : "s64", "s65", "s66", "s67", "s68", "s69", "s70", "s71", "s72", "s73", "s74", "s75", "s76", "s77", "s78", "s79",
                 "s80", "s81", "s82", "s83", "s84", "s85", "s86", "s87", "s88", "s89", "s90", "s91", "s92", "s93", "s94", "s95");
}
#define KARG_OUT "={s[64:79]}"(ka), "={s[80:95]}"(kb)
DI u64 karg(const v16u& k, int i) { return (u64)k[2 * i] | ((u64)k[2 * i + 1] << 32); }
DI float bflo(u32 v) { return __uint_as_float(v << 16); }
DI float bfhi(u32 v) { return __uint_as_float(v & 0xffff0000u); }
DI u32 pk2(float a, float b) { bf2 r = __builtin_convertvector((f2){a, b}, bf2); return __builtin_bit_cast(u32, r); }
DI float dot2(u32 a, u32 b, float c) { return __builtin_amdgcn_fdot2_f32_bf16(__builtin_bit_cast(bf2, a), __builtin_bit_cast(bf2, b), c, false); }
DI u32 relu2(float a, float b) {
  const u32 q = pk2(a * __builtin_fabsf(a), __builtin_fmaf(b, __builtin_fabsf(b), 0.f));
  return __builtin_bit_cast(u32, __builtin_elementwise_max(__builtin_bit_cast(s2, q), (s2){0, 0}));
}
DI void fence_sched() { asm volatile("" ::: "memory"); __builtin_amdgcn_sched_barrier(0); }

template <int N> DI void wait_vm() { asm volatile("s_waitcnt vmcnt(%0)" :: "n"(N) : "memory"); }
template <int N, int L> DI void wait_bar() { asm volatile("s_waitcnt vmcnt(%0) lgkmcnt(%1)\n\ts_barrier" :: "n"(N), "n"(L) : "memory"); }
template <int L> DI void wait_lds() { asm volatile("s_waitcnt lgkmcnt(%0)" :: "n"(L) : "memory"); }
DI void lds_bar() { asm volatile("s_waitcnt lgkmcnt(0)\n\ts_barrier" ::: "memory"); }

DI void dma2(u32 m0v, u32 v0, u32 v1, rsrc_t rs, u32 so) {
  asm volatile("s_mov_b32 m0, %0\n\ts_nop 0\n\t"
               "buffer_load_dwordx4 %1, %3, %4 offen lds\n\t"
               "buffer_load_dwordx4 %2, %3, %4 offen offset:1024 lds"
               :: "s"(m0v), "v"(v0), "v"(v1), "s"(rs), "s"(so) : "m0", "memory");
}
template <u32 off> DI void set_m0(u32 base) { asm volatile("s_add_u32 m0, %0, %1" :: "s"(base), "n"(off) : "m0", "scc"); }
template <int OFF> DI void dma1(u32 v, rsrc_t rs, u32 so) {
  asm volatile("buffer_load_dwordx4 %0, %1, %2 offen offset:%3 lds" :: "v"(v), "s"(rs), "s"(so), "n"(OFF) : "memory");
}
template <int OFF> DI void reld128_sc1(v4u& r, u32 vo, rsrc_t rs, u32 so) {
  asm volatile("buffer_load_dwordx4 %0, %1, %2, %3 offen offset:%4 sc1" : "+v"(r) : "v"(vo), "s"(rs), "s"(so), "n"(OFF) : "memory");
}
DI void ld32_sc1_x16(u32& r, u32 vo, rsrc_t rs, u32 so) {
  asm volatile("s_mov_b64 exec, 0xffff\n\tbuffer_load_dword %0, %1, %2, %3 offen sc1\n\ts_mov_b64 exec, -1" : "+v"(r) : "v"(vo), "s"(rs), "s"(so) : "memory");
}
template <int OFF> DI v4u ld128_sc1(u32 vo, rsrc_t rs, u32 so) {
  v4u r; asm volatile("buffer_load_dwordx4 %0, %1, %2, %3 offen offset:%4 sc1" : "=&v"(r) : "v"(vo), "s"(rs), "s"(so), "n"(OFF) : "memory"); return r;
}
DI v2u ld64(u32 vo, rsrc_t rs) {
  v2u r; asm volatile("buffer_load_dwordx2 %0, %1, %2, 0 offen" : "=&v"(r) : "v"(vo), "s"(rs) : "memory"); return r;
}
DI v2u ld64_sc1(u32 vo, rsrc_t rs) {
  v2u r; asm volatile("buffer_load_dwordx2 %0, %1, %2, 0 offen sc1" : "=&v"(r) : "v"(vo), "s"(rs) : "memory"); return r;
}
template <int OFF> DI u32 ld16(u32 vo, rsrc_t rs) {
  u32 r; asm volatile("buffer_load_ushort %0, %1, %2, 0 offen offset:%3" : "=&v"(r) : "v"(vo), "s"(rs), "n"(OFF) : "memory"); return r;
}
template <int OFF> DI void st128_sc1(v4u d, u32 vo, rsrc_t rs, u32 so) {
  asm volatile("buffer_store_dwordx4 %0, %1, %2, %3 offen offset:%4 sc1\n\ts_nop 1" :: "v"(d), "v"(vo), "s"(rs), "s"(so), "n"(OFF) : "memory");
}
DI void st64_sc1(v2u d, u32 vo, rsrc_t rs) {
  asm volatile("buffer_store_dwordx2 %0, %1, %2, 0 offen sc1" :: "v"(d), "v"(vo), "s"(rs) : "memory");
}
DI void flag_store(u32 off, rsrc_t rs, u64 tag) {
  const v2u d = {(u32)tag, (u32)(tag >> 32)};
  asm volatile("s_mov_b64 exec, 1\n\tbuffer_store_dwordx2 %0, off, %1, %2 sc1\n\ts_mov_b64 exec, -1" :: "v"(d), "s"(rs), "s"(off) : "memory");
}
DI u64 epoch_tag(v2u e) {
  const u64 v = (u64)(u32)__builtin_amdgcn_readfirstlane(e.x) | ((u64)(u32)__builtin_amdgcn_readfirstlane(e.y) << 32);
  return ((v * 0x9E3779B97F4A7C15ull + 0x632BE59BD9B4E019ull) & ~0xffffull) | (~v & 0xffffull);
}
constexpr u32 SGN = 0x80008000u;
DI u32 sign_of(u64 tag, int t) { return ((((u32)tag >> (t >> 1)) ^ t) & 1) ? SGN : 0u; }
DI void strip(v4u& d, u64 ns) {
  u64 a = (u64)d.x | ((u64)d.y << 32), b = (u64)d.z | ((u64)d.w << 32);
  a += ns; b += ns;
  d = (v4u){(u32)a, (u32)(a >> 32), (u32)b, (u32)(b >> 32)};
}
DI u32 ne3(u32 a, u32 b, u32 c) { return __builtin_amdgcn_bitop3_b32(a, b, c, 0x7e); }
DI u32 fold(v4u d, u32 m) { return (m | d.x | d.y) | (d.z | d.w); }
DI bool any(u32 m) { return __builtin_amdgcn_ballot_w64(m != 0) != 0; }
DI bool flag_bad(v2u v, u64 tag) { return __builtin_amdgcn_ballot_w64((v.x != (u32)tag) | (v.y != (u32)(tag >> 32))) != 0; }
DI void flag_wait(u32 vo, rsrc_t rs, u64 tag) {
#pragma unroll 1
  for (int it = 0; it < SPIN_MAX; ++it) {
    v2u v; asm volatile("buffer_load_dwordx2 %0, %1, %2, 0 offen sc1\n\ts_waitcnt vmcnt(0)" : "=&v"(v) : "v"(vo), "s"(rs) : "memory");
    if (!flag_bad(v, tag)) break;
    __builtin_amdgcn_s_sleep(1);
  }
}

DI float opaque(float v) { asm("" : "+v"(v)); return v; }

template <int OFF> DI void lds_put_tid(u32 v) { asm volatile("ds_write_addtid_b32 %0 offset:%1" :: "v"(v), "n"(OFF) : "memory"); }
DI bf8 lds_rd128(LAS char* p) { return *(LAS bf8*)p; }
DI v2u lds_tr(LAS char* p) { return __builtin_bit_cast(v2u, __builtin_amdgcn_ds_read_tr16_b64_v4i16((LAS s4*)p)); }
DI bf8 join(v2u x, v2u y) { return __builtin_bit_cast(bf8, (v4u){x.x, x.y, y.x, y.y}); }
DI bf8 lds_tr2(LAS char* p0, LAS char* p1) {
  s4 a = __builtin_amdgcn_ds_read_tr16_b64_v4i16((LAS s4*)p0);
  s4 b = __builtin_amdgcn_ds_read_tr16_b64_v4i16((LAS s4*)p1);
  v2u x = __builtin_bit_cast(v2u, a), y = __builtin_bit_cast(v2u, b);
  return __builtin_bit_cast(bf8, (v4u){x.x, x.y, y.x, y.y});
}
DI f16v mfma32(bf8 a, bf8 b, f16v c) { return __builtin_amdgcn_mfma_f32_32x32x16_bf16(a, b, c, 0, 0, 0); }
DI f4 mfma16(bf8 a, bf8 b, f4 c) { return __builtin_amdgcn_mfma_f32_16x16x32_bf16(a, b, c, 0, 0, 0); }

template <int I, int N, class F> DI void static_for(F&& f) {
  if constexpr (I < N) { f(std::integral_constant<int, I>{}); static_for<I + 1, N>(f); }
}

constexpr u32 REG = 20480;
constexpr int LP[10] = {36, 39, 41, 44, 47, 49, 52, 55, 57, 60};
constexpr int LB[10] = {6, 4, 5, 7, 0, 1, 9, 2, 3, 8};
constexpr int wblk(int t) { constexpr int b[4] = {6, 7, 9, 8}; return t < 8 ? b[t & 3] : LB[t - 8]; }
constexpr int rblk(int k) { return LB[(k + 8) % 10]; }
constexpr u32 w2row(int k) { return 64 * (k / 14) + 8 * (k & 1); }
constexpr int mn(int a, int b) { return a < b ? a : b; }

constexpr int g1st(int n) { return n >= 112 ? 14 + ((n >> 2) & 1) : n >> 3; }
constexpr int g1ms(int n) { return n >= 112 || (n >> 3) == 7 ? n & 3 : (n & 7) >> 1; }
constexpr int g1mt(int n) { return n >= 112 ? (n >> 3) & 1 : (n & 63) >= 56 ? (n >> 2) & 1 : n & 1; }
constexpr int rdpos(int m, int e) {
  if (m >= 64) return m - LAM;
  const int h = 2 * (m & 7) + e;
  return 8 * (m >> 3) - 4 + (h < 12 ? h / 3 : 4 + (h - 12) / 2);
}
constexpr bool g1bar(int T) { return T <= 8 || (BMASK >> T & 1); }
constexpr int g1need(int T) {
  int nb = T + 1, s = T;
  while (nb <= 16 && !g1bar(nb)) ++nb;
  for (int m = 0; m < 128; ++m) if (rdpos(m, 0) < 8 * nb - 4 && g1st(m) > s) s = g1st(m);
  return mn(s, 15);
}
constexpr int upos(int u) { return u < 16 ? EP0 + u : u < 24 ? mn(EPB + u - 16, 127) : 128; }
constexpr int hstep(int i) { return i < 2 * (28 - NT) ? i >> 1 : 28 - NT + (i - 2 * (28 - NT)) % NT; }
constexpr int hsub(int i) { return i < 2 * (28 - NT) ? i & 1 : (i - 2 * (28 - NT)) / NT; }

enum { OX, OW, OR, OS, OE, OH, OP, OA };
struct Op { int pos, kind, idx, sub; };
constexpr int G2P = 200, NA = 7;
constexpr bool own_m0(int pos) { return pos < 0 || (pos >= 128 && pos < G2P); }
struct OpTab {
  Op op[256]; int n;
  constexpr void add(int pos, int kind, int idx, int sub) { op[n] = Op{pos, kind, idx, sub}; ++n; }
  constexpr int at(int kind, int idx, int sub) const {
    for (int i = 0; i < n; ++i) if (op[i].kind == kind && op[i].idx == idx && op[i].sub == sub) return i;
    return -1;
  }
  constexpr int from(int pos) const { int i = 0; while (i < n && op[i].pos < pos) ++i; return i; }
  constexpr int younger(int kind, int idx, int sub, int pos) const {
    int c = 0, a = at(kind, idx, sub);
    if (a < 0 || op[a].pos >= pos) return -1000;
    for (int i = a + 1; i < n; ++i) if (op[i].pos < pos) ++c;
    return c > 63 ? 63 : c;
  }
  constexpr bool before(int kind, int idx, int sub, int pos) const { return op[at(kind, idx, sub)].pos < pos; }
  constexpr int lds(int i) const {
    const Op o = op[i];
    return o.kind == OX ? (o.idx % 3) * 4096 + (o.sub >> 1) * 2048 : o.kind == OW ? wblk(o.idx) * 2048 : o.kind == OR ? rblk(o.idx) * 2048 : -1;
  }
  constexpr int m0_at(int pos) const {
    int cur = -1;
    for (int i = 0; i < n; ++i) if (lds(i) >= 0) { if (op[i].pos == pos) return lds(i) != cur ? lds(i) : -1; cur = lds(i); }
    return -1;
  }
  constexpr bool m0_ok() const {
    for (int i = 0; i < n; ++i) for (int j = i + 1; j < n; ++j)
      if (op[i].pos == op[j].pos && !own_m0(op[i].pos) && lds(i) >= 0 && lds(j) >= 0 && lds(i) != lds(j)) return false;
    return true;
  }
};
template <bool AUX> constexpr OpTab optab() {
  OpTab t{};
  int nh = 0;
  if (!AUX) {
    for (int c = 0; c < 3; ++c) { for (int s = 0; s < 4; ++s) t.add(-1, OX, c, s); t.add(-1, OW, c, 0); t.add(-1, OW, c, 1); }
    t.add(-1, OW, 3, 0); t.add(-1, OW, 3, 1);
    int nr = 4, done = 7;
    for (int n = 0; n < 128; ++n) {
      const int T = (n + 4) / 8, i = (n + 4) % 8, s = i - 3;
      if (T == 0) t.add(n, OX, 3, i - 4);
      if (T >= 1 && T <= 4 && i < 2) t.add(n, OW, T + 3, i);
      if (T >= 1 && T <= 4 && s >= 0 && s < 4) t.add(n, OX, T + 3, s);
      for (int e = 0; e < 20; ++e) if (LP[e >> 1] + (e & 1) == n) t.add(n, e < 16 ? OW : OR, e < 16 ? 8 + (e >> 1) : (e >> 1) - 8, e & 1);
      if (T > 8 && i == 0 && g1bar(T)) done = mn(T - 2, 13);
      if (nr / 2 + 6 <= done) { t.add(n, OR, nr >> 1, nr & 1); ++nr; }
      for (int q = 0; q < 8; ++q) if (upos(4 * q + 3) == n) t.add(n, OS, q, 0);
      if (n == upos(15) + 1) t.add(n, OE, 0, 0);
      if (n == PA) t.add(n, OP, 0, 0);
    }
    for (int q = 0; q < 8; ++q) if (upos(4 * q + 3) == 128) t.add(128 + q, OS, q, 0);
    if (nr & 1) { t.add(140, OR, nr >> 1, 1); ++nr; }
    for (; nh < 2 * PD; ++nh) t.add(141, OH, hstep(nh), hsub(nh));
    for (; nr < 20; ++nr) t.add(142, OR, nr >> 1, nr & 1);
  } else {
    for (; nh < 2 * PD; ++nh) t.add(-1, OH, hstep(nh), hsub(nh));
  }
  int na = 0;
  for (int k = 0; k < 28 - NT; ++k)
    for (int g = 0; g < 4; ++g) {
      const int pos = G2P + 4 * k + g;
      const bool ring = (g == 1 || g == 2) && k + 10 < 28;
      if (ring) t.add(pos, OR, k + 10, g - 1);
      if (k == KP && g == 0) t.add(pos, OP, 1, 0);
      const int lim = mn(k < KC ? 28 : 56, 2 * (k + PD) + (g == 3 ? 2 : 1));
      int room = ring ? 0 : 1;
      if (k >= KC && nh + 2 < lim) ++room;
      for (; room > 0 && nh < lim; --room, ++nh) t.add(pos, OH, hstep(nh), hsub(nh));
      if (k >= KA && na < NA) { t.add(pos, OA, 0, na); ++na; }
    }
  return t;
}
constexpr OpTab GT[2] = {optab<false>(), optab<true>()};
constexpr int g1wait(int T) { return mn(GT[0].younger(OW, g1need(T), 1, T ? 8 * T - 4 : 0), T ? 63 : GT[0].younger(OX, 0, 3, 0)); }
template <bool AUX> constexpr int g2wait(int k) {
  const int pos = G2P + 4 * k;
  const bool last = k == 27 - NT;
  return mn(last ? GT[AUX].younger(OH, 27, 0, pos) : GT[AUX].younger(OH, k + 1, 1, pos),
            AUX && k + 1 < 10 ? 63 : GT[AUX].younger(OR, last ? 27 : k + 1, 1, pos));
}
constexpr int G2E = G2P + 4 * (28 - NT);
template <bool AUX> constexpr bool sched_ok() {
  for (int k = 0; k < 28 - NT; ++k) if (g2wait<AUX>(k) < 0) return false;
  for (int T = 0; T < 16; ++T) if (g1bar(T) && g1wait(T) < 0) return false;
  for (int T = 0; T < 7; ++T) if (GT[0].younger(OX, T + 1, 3, 8 * T + 2) < 0) return false;
  return GT[AUX].m0_ok() && GT[AUX].younger(OA, 0, NA - 1, G2P + 4 * KB) >= 0 && GT[AUX].younger(OP, 1, 0, G2P + 4 * KC) >= 0 &&
         GT[AUX].younger(OH, 27, 1, G2E) >= 0;
}
static_assert(sched_ok<false>() && sched_ok<true>() && GT[0].at(OW, 15, 1) < GT[0].at(OS, 0, 0), "schedule");
static_assert(KP < KC && KC < 14 && KB >= 18 && KB < 27 - NT && NT >= 2 && NT <= 4 && PD >= NT && PD <= 14 && LAM == 4, "schedule");
static_assert(!(BMASK >> 16), "schedule");

template <int N> DI void wait_pin(v4u& a, v4u& b) { asm volatile("s_waitcnt vmcnt(%2)" : "+v"(a), "+v"(b) : "n"(N) : "memory"); }
template <int N> DI void wait_pin(v2u& a) { asm volatile("s_waitcnt vmcnt(%1)" : "+v"(a) : "n"(N) : "memory"); }
template <int N> DI void wait_pin(u32& a) { asm volatile("s_waitcnt vmcnt(%1)" : "+v"(a) : "n"(N) : "memory"); }
template <int N> DI void wait_pin(v4u (&a)[6], v2u& b) {
  asm volatile("s_waitcnt vmcnt(%7)" : "+v"(a[0]), "+v"(a[1]), "+v"(a[2]), "+v"(a[3]), "+v"(a[4]), "+v"(a[5]), "+v"(b) : "n"(N) : "memory");
}
DI void pin(v4u& a) { asm volatile("" : "+v"(a)); }
DI void pin(u32& a) { asm volatile("" : "+v"(a)); }
template <int I> using IC = std::integral_constant<int, I>;

DI void touch(u32& d, u32 vo, rsrc_t rs) { asm volatile("buffer_load_dword %0, %1, %2, 0 offen" : "+v"(d) : "v"(vo), "s"(rs) : "memory"); }
DI void touch_so(u32& d, u32 vo, rsrc_t rs, u32 so) { asm volatile("buffer_load_dword %0, %1, %2, %3 offen" : "+v"(d) : "v"(vo), "s"(rs), "s"(so) : "memory"); }

template <bool AUX>
DI void gemm_role(int b, LAS char* sm) {
  v16u ka, kb;
  constexpr const OpTab& TB = GT[AUX];
  u32 lane = threadIdx.x & 63;
  const int wave = __builtin_amdgcn_readfirstlane(threadIdx.x >> 6);
  const int xcd = b & 7, r = b >> 3;
  const u32 mreg = (u32)(uintptr_t)sm + wave * REG;
  const int t2 = 4 * xcd + (r >> 3), c2 = r & 7;
  const int tau = 4 * (xcd >> 1) + r / 7, j = 7 * (xcd & 1) + r % 7, rot = 4 * (r / 7 & 1) + (r % 7 & 3);

  u32 wch = ((lane & 7) ^ ((lane >> 2) & 4)) << 4;
  u32 vx0 = (u32)(128 * tau + 32 * wave + (lane >> 3)) * 1024 + (((lane & 7) ^ (lane >> 4)) << 4), vx1 = (vx0 ^ 64) + 7168;
  u32 vw0 = (u32)(16 * wave + (lane >> 3)) * 3584 + j * 256 + wch, vw1 = vw0 + 8 * 3584 - 1024;
  u32 vb = (u32)(128 * j + (lane & 32) + 16 * ((lane >> 2) & 1) + 4 * ((lane >> 3) & 3) + (lane & 3)) * 2, zero = 0;
  if constexpr (!AUX) asm volatile("s_waitcnt lgkmcnt(0)" : KARG_OUT, "+v"(vx0), "+v"(vx1), "+v"(vw0), "+v"(vw1), "+v"(vb), "+v"(zero));
  else asm volatile("s_waitcnt lgkmcnt(0)" : KARG_OUT, "+v"(zero));
  const u64 ws = karg(ka, AWS);
  const rsrc_t rws = mkrsrc(ws, WS_END), rx = mkrsrc(karg(ka, AX), 2048 * 512 * 2), rw1 = mkrsrc(karg(ka, AW1), 512 * 1792 * 2);
  const rsrc_t rw2 = mkrsrc(karg(ka, AW2), 1792 * 512 * 2), rb2 = mkrsrc(karg(ka, AB2), 512 * 2);

  u64 tag;
  u32 sg, pf, vv0, vv1, vh, vhh[2], vp, va, vg;
  v4u hd[8], hb[28][2], av[6];
  v2u bv;
  const int rot2 = (c2 + 2 * (r >> 3)) % 7;
  u32 ho[7][2], wo[7];
  auto hload = [&](auto K, auto S, auto AGAIN) {
    constexpr int k = decltype(K)::value, s = decltype(S)::value;
    const u32 so = ho[(k % 14) >> 1][s];
    if constexpr (decltype(AGAIN)::value) reld128_sc1<(k & 1) * 1024>(hb[k][s], vhh[k / 14], rws, so);
    else hb[k][s] = ld128_sc1<(k & 1) * 1024>(vhh[k / 14], rws, so);
  };
  auto aload = [&](auto S) {
    constexpr int s = decltype(S)::value;
    if constexpr (s < 4) av[s] = ld128_sc1<s * 512>(va, rws, 0);
    else if constexpr (s < 6) av[s] = ld128_sc1<(s - 4) * 16>(vg, rws, 0);
    else bv = ld64((u32)(64 * c2 + 4 * (lane & 15)) * 2, rb2);
  };
  auto issue = [&](auto I) {
    constexpr Op o = TB.op[decltype(I)::value];
    constexpr int lo = (o.sub & 1) * 1024;
    if constexpr (own_m0(o.pos) && TB.lds(decltype(I)::value) >= 0 && lo == 0) { set_m0<TB.lds(decltype(I)::value)>(mreg); asm volatile("s_nop 0"); }
    if constexpr (o.kind == OX) dma1<lo>(lo ? vx1 : vx0, rx, (u32)((o.idx + rot) & 7) * 128 + (o.sub >> 1) * 16384);
    if constexpr (o.kind == OW) dma1<lo>(lo ? vw1 : vw0, rw1, (u32)((o.idx + rot) & 7) * (64 * 3584) + (o.idx >> 3) * 128);
    if constexpr (o.kind == OR) dma1<lo>(lo ? vv1 : vv0, rw2, wo[(o.idx % 14) >> 1] + w2row(o.idx) * 1024);
    if constexpr (o.kind == OS) st128_sc1<(o.idx & 3) * 1024>(hd[o.idx], vh, rws, (o.idx >> 2) * 4096);
    if constexpr (o.kind == OE) flag_store(WS_EFLAG + (u32)b * 8, rws, tag);
    if constexpr (o.kind == OP) ld32_sc1_x16(pf, vp, rws, o.idx * 4096);
    if constexpr (o.kind == OH) hload(IC<o.idx>{}, IC<o.sub>{}, IC<0>{});
    if constexpr (o.kind == OA) aload(IC<o.sub>{});
    if constexpr (o.pos < 0 && o.kind == OW && o.sub == 1 && o.idx < 2) asm volatile("s_barrier");
  };
  auto issue_at = [&](auto N) {
    static_for<TB.from(decltype(N)::value), TB.from(decltype(N)::value + 1)>(issue);
  };
  auto m0_for = [&](auto N) { if constexpr (TB.m0_at(decltype(N)::value) >= 0) set_m0<TB.m0_at(decltype(N)::value)>(mreg); };
  auto gate = [&](auto PH, auto K0, auto POS) {
    if (__builtin_expect(any((pf ^ sg) & SGN), 0)) {
#pragma unroll 1
      for (int it = 0; it < SPIN_MAX; ++it) {
        __builtin_amdgcn_s_sleep(1);
        ld32_sc1_x16(pf, vp, rws, decltype(PH)::value * 4096);
        wait_pin<0>(pf);
        if (!any((pf ^ sg) & SGN)) break;
      }
      static_for<2 * decltype(K0)::value, 56>([&](auto I) {
        constexpr int k = decltype(I)::value >> 1, s = decltype(I)::value & 1;
        if constexpr (TB.before(OH, k, s, decltype(POS)::value)) hload(IC<k>{}, IC<s>{}, IC<1>{});
      });
      if constexpr (TB.before(OH, decltype(K0)::value, 0, decltype(POS)::value)) wait_vm<0>();
    }
  };

  v2u ep;
  u32 bq0, bq1;
  if constexpr (!AUX) {
    const rsrc_t rb1 = mkrsrc(karg(ka, AB1), 1792 * 2);
    ep = ld64_sc1(zero, rws);
    bq0 = ld16<0>(vb, rb1); bq1 = ld16<128>(vb, rb1);
    issue_at(IC<-1>{});
    __builtin_amdgcn_s_setprio(PRG1);
  } else {
    ep = ld64_sc1(zero, rws);
  }
  pin(lane);
  fence_sched();
  const int g = lane >> 4, q = (lane >> 2) & 3, p = lane & 3, l31 = lane & 31, hh = lane >> 5, l3 = lane >> 3, l7 = lane & 7;
  u32 aoff[2];
#pragma unroll
  for (int mi = 0; mi < 2; ++mi) aoff[mi] = (8 * (g >> 1) + q) * 128 + (((4 * mi + 2 * (p & 1) + (g & 1)) ^ ((q >> 1) << 2)) << 4) + (p >> 1) * 8;
  vv0 = (u32)(896 * (wave >> 1) + 32 * (wave & 1) + l3) * 1024 + c2 * 128 + wch; vv1 = vv0 + 15360;
  vh = WS_H + (u32)((4 * tau + wave) * 112 + 8 * j) * 1024 + lane * 16;
  vhh[0] = WS_H + (u32)(2 * t2 * 112 + 56 * (wave >> 1) + 2 * (wave & 1)) * 1024 + lane * 16; vhh[1] = vhh[0] + 4096;
  vp = vhh[0] + (u32)((l3 & 1) * 112 + 8 * min(l7, 6) + 1) * 1024;
  va = WS_A + (u32)t2 * 131072 + (u32)(32 * c2 + 4 * l3) * 512 + (u32)(16 * wave + 2 * l7) * 8;
  vg = WS_G + (u32)(64 * t2 + 16 * wave + 4 * g) * 8;
  u32 sgn = SGN;
  asm("" : "+s"(sgn));
  pin(vv0); pin(vv1); pin(vh); pin(vhh[0]); pin(vhh[1]); pin(vp); pin(va); pin(vg);
#pragma unroll
  for (int i = 0; i < 7; ++i) {
    const u32 pr = i + rot2 >= 7 ? i + rot2 - 7 : i + rot2;
    u32 h0 = pr * 8192, h1 = h0 + 114688, w0 = pr * 131072;
    asm volatile("" : "+s"(h0), "+s"(h1), "+s"(w0));
    ho[i][0] = h0; ho[i][1] = h1; wo[i] = w0;
  }

  if constexpr (!AUX) {
    f16v acc[4];
    LAS char* pa[2][2]; LAS char* pb[4];
#pragma unroll
    for (int mi = 0; mi < 2; ++mi) { u32 hi = aoff[mi] + 2 * REG; pin(hi); pa[mi][0] = sm + aoff[mi]; pa[mi][1] = sm + hi; }
#pragma unroll
    for (int u = 0; u < 4; ++u) pb[u] = sm + wave * REG + l31 * 128 + (((2 * u + hh) ^ ((l31 >> 1) & 7)) << 4);

    asm volatile("s_waitcnt vmcnt(20)" : "+v"(ep), "+v"(bq0), "+v"(bq1) :: "memory");
    tag = epoch_tag(ep);
    pf = sg = sign_of(tag, l31);
    {
      const bf8 o0 = __builtin_bit_cast(bf8, (v4u){hh ? 0u : 0x3f80u, 0, 0, 0}), o1 = __builtin_bit_cast(bf8, (v4u){hh ? 0x3f80u : 0u, 0, 0, 0});
      const bf8 a0 = __builtin_bit_cast(bf8, (v4u){bq0, 0, 0, 0}), a1 = __builtin_bit_cast(bf8, (v4u){bq1, 0, 0, 0});
      acc[0] = mfma32(a0, o0, (f16v)(0.f)); acc[1] = mfma32(a0, o1, (f16v)(0.f));
      acc[2] = mfma32(a1, o0, (f16v)(0.f)); acc[3] = mfma32(a1, o1, (f16v)(0.f));
    }
    v2u af[128][2];
    bf8 xf[32];
    auto rda_at = [&](auto N) {
      constexpr int n = decltype(N)::value, m0_ = n < 0 ? 0 : n;
      static_for<2 * m0_, mn(2 * m0_ + 24, 256)>([&](auto H) {
        constexpr int m = decltype(H)::value >> 1, e = decltype(H)::value & 1, u = g1ms(m);
        if constexpr (n < 0 ? rdpos(m, e) < 0 : rdpos(m, e) == n)
          af[m][e] = lds_tr(pa[g1mt(m)][u >> 1] + (u & 1) * REG + wblk(g1st(m)) * 2048 + e * 512);
      });
    };
    auto rdx = [&](auto S) { constexpr int s = decltype(S)::value; xf[s] = lds_rd128(pb[s & 3] + ((s >> 2) % 3) * 4096); };
    auto unit = [&](auto U) {
      constexpr int un = decltype(U)::value, tl = un >> 3, e = (un & 7) * 2;
      const float v0 = acc[tl][e], v1 = acc[tl][e + 1];
      hd[un >> 2][un & 3] = relu2(v0, v1);
    };
    auto mark = [&](auto U) { constexpr int un = decltype(U)::value; hd[un >> 2][un & 3] |= sg; };
    fence_sched();
    wait_bar<g1wait(0), 15>();
    fence_sched();
    static_for<0, 4>(rdx);
    fence_sched();
    rda_at(IC<-1>{});
    static_for<0, 128>([&](auto N) {
      constexpr int n = decltype(N)::value, T = (n + 4) / 8, i = (n + 4) % 8;
      fence_sched();
      if constexpr (i == 0 && g1bar(T)) wait_bar<g1wait(T), T < 8 ? 4 : T == 8 ? 0 : 15>();
      if constexpr (i == 6 && T < 7) wait_vm<TB.younger(OX, T + 1, 3, n)>();
      m0_for(N);
      fence_sched();
      constexpr int tl = 2 * (n >> 6) + g1mt(n);
      acc[tl] = mfma32(join(af[n][0], af[n][1]), xf[4 * (g1st(n) & 7) + g1ms(n)], acc[tl]);
      fence_sched();
      static_for<0, 32>([&](auto U) { if constexpr (upos(decltype(U)::value) == n) unit(U); });
      fence_sched();
      rda_at(N);
      if constexpr (i >= 6 && T < 7) static_for<4 * (T + 1) + 2 * (i - 6), 4 * (T + 1) + 2 * (i - 5)>(rdx);
      fence_sched();
      static_for<0, 32>([&](auto U) { if constexpr (upos(decltype(U)::value) == n) mark(U); });
      fence_sched();
      if constexpr (T == 0 && i == 4) wait_lds<14>();
      if constexpr (T >= 1 && T <= 7 && i == 3) wait_lds<12>();
      issue_at(N);
    });
    fence_sched();
    static_for<16, 32>([&](auto U) {
      constexpr int un = decltype(U)::value;
      if constexpr (upos(un) == 128) { unit(U); mark(U); }
      if constexpr ((un & 3) == 3) { fence_sched(); issue_at(IC<128 + (un >> 2)>{}); fence_sched(); }
    });
    issue_at(IC<140>{});
    wait_pin<TB.younger(OP, 0, 0, 141)>(pf);
    gate(IC<0>{}, IC<0>{}, IC<141>{});
    issue_at(IC<141>{});
    asm volatile("s_barrier" ::: "memory");
    issue_at(IC<142>{});
  } else {
    const u32 wv = (r - 28) * 4 + wave;
    const u32 vta = ((wv >> 2) * 128 + (wv & 3) * 16 + (lane >> 2)) * 1024 + (lane & 3) * 128;
    const u32 vm = l3 < 7 ? (u32)((xcd * 16 + wv) * 14 + l3) * 1024 + l7 * 128 : 1u << 30;
    __builtin_amdgcn_s_sleep(8);
    fence_sched();
    touch(zero, vm, rw2); touch_so(zero, vm, rw2, 7 * 1024);
    fence_sched();
    __builtin_amdgcn_s_sleep(30);
    fence_sched();
    const int tg = xcd * 4 + (r - 28);
    const v4u gv = *(const GAS v4u*)(karg(ka, AGW) + lane * 16);
    const GAS char* xr = (const GAS char*)(karg(ka, AX) + (u32)((tg * 64 + wave * 16) * 1024 + lane * 16));
    float s[16];
#pragma unroll
    for (int i = 0; i < 16; ++i) {
      const v4u xv = *(const GAS v4u*)(xr + i * 1024);
      s[i] = 0.f;
#pragma unroll
      for (int k = 0; k < 4; ++k) s[i] = dot2(xv[k], gv[k], s[i]);
    }
#pragma unroll
    for (int i = 0; i < 8; ++i) {
      const v2u t = __builtin_amdgcn_permlane32_swap(__float_as_uint(s[i]), __float_as_uint(s[i + 8]), false, false);
      s[i] = opaque(__uint_as_float(t.x) + __uint_as_float(t.y));
    }
#pragma unroll
    for (int i = 0; i < 4; ++i) {
      const v2u t = __builtin_amdgcn_permlane16_swap(__float_as_uint(s[i]), __float_as_uint(s[i + 4]), false, false);
      float v = __uint_as_float(t.x) + __uint_as_float(t.y);
      v += __int_as_float(__builtin_amdgcn_update_dpp(0, __float_as_int(v), 0x128, 0xf, 0xf, false));
      v += __int_as_float(__builtin_amdgcn_update_dpp(0, __float_as_int(v), 0x124, 0xf, 0xf, false));
      v += __int_as_float(__builtin_amdgcn_update_dpp(0, __float_as_int(v), 0x122, 0xf, 0xf, false));
      v += __int_as_float(__builtin_amdgcn_update_dpp(0, __float_as_int(v), 0x121, 0xf, 0xf, false));
      s[i] = opaque(v);
    }
    const float m01 = opaque(p & 1 ? s[1] : s[0]), m23 = opaque(p & 1 ? s[3] : s[2]), mine = p & 2 ? m23 : m01;
    const float sl = bflo(pk2(mine, 0.f));
    const float gt = bflo(pk2(1.f / (1.f + __expf(-sl)), 0.f));
    fence_sched();
    asm volatile("s_waitcnt vmcnt(0)\n\ts_barrier" : "+v"(ep) :: "memory");
    tag = epoch_tag(ep);
    pf = sg = sign_of(tag, l31);
    flag_store(WS_EFLAG + (u32)b * 8, rws, tag);
    if (q == 0) st64_sc1((v2u){__float_as_uint(gt), (u32)(tag >> 32)}, WS_G + (u32)(tg * 64 + wave * 16 + 4 * g + p) * 8, rws);
    fence_sched();
    static_for<0, 10>([&](auto J) { constexpr int i = decltype(J)::value; dma2(mreg + rblk(i) * 2048, vv0, vv1, rw2, wo[i >> 1] + w2row(i) * 1024); });
    fence_sched();
#pragma unroll
    for (int i = 0; i < 3; ++i) touch_so(zero, vta, rw2, (i * 512 + 64) * 1024);
    if (wv < 8) touch_so(zero, vta, rw2, (3 * 512 + 64) * 1024);
    fence_sched();
    ld32_sc1_x16(pf, vp, rws, 0);
    wait_pin<0>(pf);
    pin(zero);
    gate(IC<0>{}, IC<0>{}, IC<-1>{});
    issue_at(IC<-1>{});
  }
  __builtin_amdgcn_s_setprio(PRG2);

  const u32 tg32 = (u32)(tag >> 32);
  const u64 ns = sg ? 0x7fff7fff7fff8000ull : 0;
  LAS char* pv[2] = {sm + wave * REG + aoff[0], sm + wave * REG + aoff[1]};
  auto vfrag = [&](int k, int mi) { LAS char* a = pv[mi] + rblk(k) * 2048; return lds_tr2(a, a + 512); };
  auto fresh = [&](auto K0, auto K1, auto SM, u32 m) {
    constexpr int k0 = decltype(K0)::value, k1 = decltype(K1)::value, sm_ = decltype(SM)::value, ke = sm_ == 3 ? mn(k1 | 1, 27 - NT) : k1;
    if (__builtin_expect(any(m), 0)) {
#pragma unroll 1
      for (int it = 0; it < SPIN_MAX; ++it) {
        static_for<k0, ke + 1>([&](auto K) {
          if constexpr (sm_ & 1) hload(K, IC<0>{}, IC<1>{});
          if constexpr (sm_ & 2) hload(K, IC<1>{}, IC<1>{});
        });
        wait_vm<0>();
        u32 z = 0;
        static_for<2 * k0, 2 * ke + 2>([&](auto I) {
          constexpr int k = decltype(I)::value >> 1, s = decltype(I)::value & 1;
          if constexpr (sm_ >> s & 1) { pin(hb[k][s]); if constexpr (k <= k1) { strip(hb[k][s], ns); z = fold(hb[k][s], z); } }
        });
        if (!any(z & sgn)) break;
        __builtin_amdgcn_s_sleep(1);
      }
    }
  };
  auto abad = [&](int i0, int i1) {
    u32 m = 0;
#pragma unroll
    for (int i = i0; i < i1; ++i) m |= ne3(av[i].y, av[i].w, tg32);
    return m;
  };
  const int cq = lane & 15;
  LAS char* ap = sm + wave * REG + rblk(8) * 2048 + (cq & 1) * 1024 + (cq >> 1) * 32 + g * 8;
  v2u a2[2][2];
  float ga[4][4], gn[4], bb[4];
  auto aput = [&]() {
    static_for<0, 8>([&](auto I) { constexpr int i = decltype(I)::value; lds_put_tid<rblk(8) * 2048 + i * 256>(i & 1 ? av[i >> 1].z : av[i >> 1].x); });
  };
  auto aget = [&]() {
#pragma unroll
    for (int i = 0; i < 4; ++i) a2[i >> 1][i & 1] = *(volatile LAS v2u*)(ap + i * 256);
  };
  auto gate_of = [&](int e) { return __uint_as_float(e & 1 ? av[4 + (e >> 1)].z : av[4 + (e >> 1)].x); };
  auto amix = [&](int u) {
    if (u == 0) { bb[0] = opaque(bflo(bv.x)); bb[1] = opaque(bfhi(bv.x)); bb[2] = opaque(bflo(bv.y)); bb[3] = opaque(bfhi(bv.y)); }
    if (u == 1) for (int e = 0; e < 4; ++e) gn[e] = opaque(1.f - gate_of(e));
    if (u >= 2) {
      const int e = (u - 2) >> 1, h = u & 1;
      const u32 a = e & 2 ? a2[h][e & 1].y : a2[h][e & 1].x;
      ga[e][2 * h] = opaque(__builtin_fmaf(gate_of(e), bflo(a), opaque(gn[e] * bb[2 * h])));
      ga[e][2 * h + 1] = opaque(__builtin_fmaf(gate_of(e), bfhi(a), opaque(gn[e] * bb[2 * h + 1])));
    }
  };
  constexpr int AM0 = 4 * KB + 3, K0 = 28 - NT, TS = 17 * 256;
  u32 pro = ((cq >> 3) + 2 * (wave >> 1)) * TS + (cq & 3) * (1024 + 32) + ((cq >> 2) & 1) * 128 + (wave & 1) * 64 + g * 16;
  u32 vo = (u32)(64 * t2 + 16 * wave + 4 * g) * 1024 + (u32)(64 * c2 + 4 * cq) * 2;
  pin(pro); pin(vo);
  LAS char* pr = sm + pro;
  rsrc_t ro = mkrsrc(karg(ka, AOUT), 2048 * 512 * 2);
  asm volatile("" : "+s"(ro));
  f16v o2[2][2];
  bf8 wb[28][2];
  u32 lm = 0;
  fence_sched();
  wait_pin<mn(TB.younger(OH, 0, 1, G2P), AUX ? 63 : TB.younger(OR, 0, 1, G2P))>(hb[0][0], hb[0][1]);
  wb[0][0] = vfrag(0, 0); wb[0][1] = vfrag(0, 1);
  strip(hb[0][0], ns); strip(hb[0][1], ns);
  fresh(IC<0>{}, IC<0>{}, IC<3>{}, fold(hb[0][1], fold(hb[0][0], 0)) & sgn);
  static_for<0, K0>([&](auto K) {
    constexpr int k = decltype(K)::value;
    constexpr bool last = k == K0 - 1;
    fence_sched();
    if constexpr (k == KC) { wait_pin<TB.younger(OP, 1, 0, G2P + 4 * k)>(pf); gate(IC<1>{}, IC<14>{}, IC<G2P + 4 * k>{}); }
    wait_vm<g2wait<AUX>(k)>();
    if constexpr (last) static_for<K0, 28>([&](auto J) { pin(hb[decltype(J)::value][0]); });
    else { pin(hb[k + 1][0]); pin(hb[k + 1][1]); }
    if constexpr (k == KB) { asm volatile("s_mov_b32 m0, %0" :: "s"(mreg) : "m0"); wait_pin<TB.younger(OA, 0, NA - 1, G2P + 4 * k)>(av, bv); }
    u32 m = 0;
    static_for<0, 4>([&](auto G) {
      constexpr int gi = decltype(G)::value, mi = gi & 1, ni = gi >> 1;
      m0_for(IC<G2P + 4 * k + gi>{});
      fence_sched();
      if constexpr (k == 0) o2[mi][ni] = mfma32(wb[k][mi], __builtin_bit_cast(bf8, hb[k][ni]), (f16v)(0.f));
      else o2[mi][ni] = mfma32(wb[k][mi], __builtin_bit_cast(bf8, hb[k][ni]), o2[mi][ni]);
      fence_sched();
      if constexpr (last) {
        if constexpr (gi < NT) { wb[K0 + gi][0] = vfrag(K0 + gi, 0); strip(hb[K0 + gi][0], ns); m = fold(hb[K0 + gi][0], m); }
        if constexpr (gi == 3) m &= sgn;
      } else {
        if constexpr (gi == 0) { wb[k + 1][0] = vfrag(k + 1, 0); wb[k + 1][1] = vfrag(k + 1, 1); }
        if constexpr (gi == 1) { strip(hb[k + 1][0], ns); const v4u d = hb[k + 1][0]; m = d.x | d.y | d.z; pin(m); }
        if constexpr (gi == 2) { strip(hb[k + 1][1], ns); m = m | hb[k + 1][0].w | hb[k + 1][1].x; pin(m); }
        if constexpr (gi == 3) { const v4u d = hb[k + 1][1]; m = m | d.y | d.z; pin(m); m = (m | d.w) & sgn; }
      }
      if constexpr (k == KB && gi == 0) aput();
      if constexpr (k == KB && gi == 1) { aget(); lm = abad(0, 3); pin(lm); }
      if constexpr (k == KB && gi == 2) { lm |= abad(3, 6); pin(lm); }
      if constexpr (constexpr int u = 4 * k + gi - AM0; u >= 0 && u < 10) amix(u);
      fence_sched();
      issue_at(IC<G2P + 4 * k + gi>{});
      if constexpr (gi == 3) { if constexpr (last) fresh(IC<K0>{}, IC<27>{}, IC<1>{}, m); else fresh(IC<k + 1>{}, IC<k + 1>{}, IC<3>{}, m); }
    });
  });
  auto pput = [&](auto I) {
    constexpr int tl = decltype(I)::value >> 4, i = decltype(I)::value & 15;
    lds_put_tid<tl * TS + i * 256 + 32 * (i >> 2)>(__float_as_uint(o2[tl & 1][tl >> 1][i]));
  };
  constexpr auto ppos = [](int i) constexpr { return mn(((i >> 4) + 1) * NT + 1 + (i & 15) * NT / 16, 4 * NT); };
  u32 m = 0;
  static_for<0, 4 * NT>([&](auto N) {
    constexpr int n = decltype(N)::value, tl = n / NT, v = n % NT, k = K0 + v;
    fence_sched();
    if constexpr (n == NT) { wait_vm<TB.younger(OH, 27, 1, G2E)>(); static_for<K0, 28>([&](auto J) { pin(hb[decltype(J)::value][1]); }); }
    o2[tl & 1][tl >> 1] = mfma32(wb[k][tl & 1], __builtin_bit_cast(bf8, hb[k][tl >> 1]), o2[tl & 1][tl >> 1]);
    fence_sched();
    if constexpr (tl == 0) wb[k][1] = vfrag(k, 1);
    if constexpr (tl == 1) { strip(hb[k][1], ns); m = fold(hb[k][1], m); }
    if constexpr (constexpr int u = 4 * K0 + n - AM0; u >= 0 && u < 10) amix(u);
    fence_sched();
    if constexpr (v == 1 && tl >= 1) asm volatile("s_nop %0" :: "n"(tl == 1 ? 4 : 1));
    static_for<0, 48>([&](auto I) { if constexpr (ppos(decltype(I)::value) == n) pput(I); });
    if constexpr (n == 2 * NT - 1) fresh(IC<K0>{}, IC<27>{}, IC<2>{}, m & sgn);
  });
  fence_sched();
  static_for<0, 48>([&](auto I) { if constexpr (ppos(decltype(I)::value) == 4 * NT) pput(I); });
  asm volatile("s_nop 3");
  static_for<48, 64>(pput);
#pragma unroll
  for (int i = 0; i < 9; ++i) __builtin_amdgcn_s_setprio(PRG2);
  lds_bar();
  if (__builtin_expect(any(lm), 0)) {
#pragma unroll 1
    for (int it = 0; it < SPIN_MAX; ++it) {
      static_for<0, 6>(aload);
      wait_pin<0>(av, bv);
      if (!any(abad(0, 6))) break;
      __builtin_amdgcn_s_sleep(1);
    }
    aput(); aget();
#pragma unroll
    for (int u = 0; u < 10; ++u) amix(u);
  }
  asm volatile(".Lpf_end%0:" :: "n"((int)AUX));
  {
    f4 t[4][4], s[4];
#pragma unroll
    for (int i = 0; i < 16; ++i) t[i >> 2][i & 3] = *(LAS f4*)(pr + (i >> 2) * 256 + (i & 3) * REG);
    fence_sched();
#pragma unroll
    for (int c = 0; c < 4; ++c) s[c] = (t[c][0] + t[c][1]) + (t[c][2] + t[c][3]);
#pragma unroll
    for (int e = 0; e < 4; ++e) {
      auto mix = [&](int c) { const float t = s[c][e]; return opaque(__builtin_fmaf(gn[e], t, ga[e][c])); };
      __builtin_amdgcn_raw_buffer_store_b64((v2u){pk2(mix(0), mix(1)), pk2(mix(2), mix(3))}, ro, vo + e * 1024, 0, 16);
    }
  }
}

struct cf { float x, y; };
DI cf cmul(cf a, cf m) { return cf{opaque(__builtin_fmaf(a.x, m.x, -opaque(a.y * m.y))), opaque(__builtin_fmaf(a.x, m.y, opaque(a.y * m.x)))}; }
DI cf cexp_s(float lr, float rv, float m) {
  const float e = __builtin_amdgcn_exp2f(lr * m), f = __builtin_amdgcn_fractf(rv * m);
  return cf{opaque(e * __builtin_amdgcn_cosf(f)), opaque(e * __builtin_amdgcn_sinf(f))};
}

constexpr u32 TROW = 544, TCH = 63 * TROW, XT = 69632;
constexpr u32 KCH = 9472, KOO = 4656, UA0 = 18944, UCH = 8768, UBO = 4224, EX = 40960;

template <int OFF> DI void stouch(u32& d, u64 base) { asm volatile("s_load_dword %0, %1, %2" : "+s"(d) : "s"(base), "n"(OFF)); }

DI void conv_role(int ci, LAS char* sm) {
  v16u ka, kb;
  const int tid = threadIdx.x, lane = tid & 63, wave = __builtin_amdgcn_readfirstlane(tid >> 6);
  u32 tch = 0;
  {
    u64 pc; u32 len;
    asm volatile("s_getpc_b64 s[2:3]\n.Lpf_here:\n\ts_sub_u32 s2, s2, .Lpf_here-.Lpf_start\n\ts_subb_u32 s3, s3, 0\n\ts_mov_b64 %0, s[2:3]\n\t"
                 "s_max_i32 %1, 0, .Lpf_end0-.Lpf_start\n\ts_max_i32 %1, %1, .Lpf_end1-.Lpf_start\n\ts_max_i32 %1, %1, .Lpf_end2-.Lpf_start"
                 : "=s"(pc), "=&s"(len) :: "scc", "s2", "s3");
    touch(tch, lane < 4 ? ((ci >> 3) * 16 + wave * 4 + lane) * 128 : 1u << 30, mkrsrc(pc, len));
  }
  __builtin_amdgcn_s_sleep(4);
  const u32 smb = (u32)(uintptr_t)sm;
  const int pr = (ci & 7) * 32 + (ci >> 3), cg = (ci >> 3) & 7, tt = 4 * (ci & 7) + (ci >> 6);
  const int g = lane >> 4, q = (lane >> 2) & 3, p = lane & 3, n = lane & 15;
  const int ch = wave >> 1, half = wave & 1;
  u32 vpi = (u32)(pr * 256 + tid) * 2, zero = 0;
  u32 v0 = (u32)(64 * tt + 16 * wave + (lane >> 3)) * 1024 + cg * 128 + (((lane & 7) ^ ((g & 1) << 1)) << 4), v1 = (v0 ^ 64) + 7168;
  __builtin_amdgcn_s_setprio(PRC0);
  asm volatile("s_waitcnt lgkmcnt(0)" : KARG_OUT, "+v"(vpi), "+v"(v0), "+v"(v1), "+v"(zero));
  auto par = [&](int a) { u32 r; asm volatile("global_load_ushort %0, %1, %2" : "=&v"(r) : "v"(vpi), "s"(karg(kb, a)) : "memory"); return r; };
  u32 r_are = par(AARE), r_aim = par(AAIM), r_bre = par(ABRE), r_bim = par(ABIM), r_cre = par(ACRE), r_cim = par(ACIM);
  const u32 r_ldt = ((const CAS u32*)karg(kb, ALDT))[pr], r_dsk = ((const CAS u32*)karg(kb, ADSK))[pr];
  const u64 ws = karg(ka, AWS);
  const rsrc_t rws = mkrsrc(ws, WS_END), rx = mkrsrc(karg(ka, AX), 2048 * 512 * 2);
  v2u ep = ld64_sc1(zero, rws);
  dma2(smb + XT + wave * 2048, v0, v1, rx, 0);
  fence_sched();
  const float dt = bflo(pk2(expf(ch ? bfhi(r_ldt) : bflo(r_ldt)), 0.f));
  fence_sched();
  asm volatile("s_waitcnt vmcnt(3)" : "+v"(r_are), "+v"(r_aim), "+v"(r_bre), "+v"(r_bim), "+v"(r_cre), "+v"(r_cim) :: "memory");
  fence_sched();
  __builtin_amdgcn_s_setprio(PRC1);
  u64 tag;
  u32 tg32, sd = 0;
  const u32 uf = WS_UFLAG + (u32)(cg * 128 + tt * 4 + wave) * FSP;
  const u32 vf = WS_UFLAG + (u32)((ci & 7) * 128 + wave * 32 + (lane & 31)) * FSP;
  v2u pf;
  auto handoff = [&](int step) {
    if (step == 0) {
      fence_sched();
      wait_pin<0>(ep);
      __builtin_amdgcn_s_setprio(PRC0);
      tag = epoch_tag(ep);
      tg32 = (u32)(tag >> 32);
      LAS char* tb = sm + XT + wave * 2048 + (8 * (p & 1) + q) * 128 + (p >> 1) * 8;
      const int sw = (q & 2) | ((p & 1) << 2);
      const u32 vo = WS_UT + (u32)((16 * cg + 2 * g + (q >> 1)) * 256 + 8 * tt + 2 * wave + (q & 1)) * 64 + p * 16;
      const bf8 d0 = lds_tr2(tb + ((g ^ sw) << 4), tb + ((g ^ sw) << 4) + 512);
      const bf8 d1 = lds_tr2(tb + (((4 + g) ^ sw) << 4), tb + (((4 + g) ^ sw) << 4) + 512);
      st128_sc1<0>(__builtin_bit_cast(v4u, d0), vo, rws, 0);
      st128_sc1<0>(__builtin_bit_cast(v4u, d1), vo, rws, 8 * 256 * 64);
      fence_sched();
      __builtin_amdgcn_s_setprio(PRC1);
    }
    if (step == 3) { fence_sched(); wait_vm<0>(); flag_store(uf, rws, tag); fence_sched(); }
    if (step == 16) { fence_sched(); pf = ld64_sc1(vf, rws); fence_sched(); }
  };
  {
    const float lr = dt * bflo(r_are) * 1.44269504088896341f, rv = dt * bflo(r_aim) * 0.15915494309189535f;
    const cf db = {opaque(dt * bflo(r_bre)), opaque(-dt * bflo(r_bim))};
    const cf cb = cmul(cf{bflo(r_cre), -bflo(r_cim)}, db);
    const cf e16 = cexp_s(lr, rv, 16.f), e1 = cexp_s(lr, -rv, 1.f), e256 = cexp_s(lr, -rv, 256.f);
    const cf e2 = cexp_s(lr, -rv, 2.f), e4 = cexp_s(lr, -rv, 4.f), e8 = cexp_s(lr, -rv, 8.f);
    u32 tv[63];
    auto put = [&](int row, cf v) { tv[row] = pk2(v.x, v.y); };
    const u32 sw_ = (ci >> 3) * 4 + wave;
    const u64 sb = karg(ka, AW2) + (u64)((sw_ >> 6) * 896 + 64 + (sw_ & 63)) * 1024;
    auto pull = [&](auto I) {
      constexpr int e0 = (decltype(I)::value - 3) * 3;
      if constexpr (e0 >= 0 && e0 < 28) {
        fence_sched();
        static_for<e0, mn(e0 + 3, 28)>([&](auto E) { constexpr int e = decltype(E)::value; stouch<(e / 4) * 131072 + 512 + (e % 4) * 128>(sd, sb); });
        fence_sched();
      }
    };
    handoff(0);
    cf a = e16, t0 = cb, tb[8], v[8];
    put(0, cf{1.f, 0.f}); put(16, t0);
    tb[0] = cb;
    static_for<1, 16>([&](auto I) {
      constexpr int i = decltype(I)::value;
      handoff(i);
      pull(I);
      put(i, a); if (i < 15) a = cmul(a, e16);
      t0 = cmul(t0, e1); put(16 + i, t0);
      if constexpr (i < 8) { tb[i] = cmul(tb[i - 1], e256); v[i] = tb[i]; }
#pragma unroll
      for (int tl = 1; tl < 8; ++tl) {
        const int base = tl == 1 ? 32 : (tl < 4 ? 41 + 5 * (tl - 2) : 51 + 3 * (tl - 4));
        const int cnt = tl == 1 ? 9 : (tl < 4 ? 5 : 3), k = i - tl;
        if (k >= 0 && k < cnt) {
          if (k > 0) v[tl] = cmul(v[tl], tl == 1 ? e2 : (tl < 4 ? e4 : e8));
          put(base + k, v[tl]);
        }
      }
    });
#pragma unroll
    for (int i = 0; i < 63; ++i) asm volatile("" : "+v"(tv[i]));
    fence_sched();
    __builtin_amdgcn_s_setprio(PRCB);
    asm volatile("s_mov_b32 m0, %0\n\ts_nop 0" :: "s"(smb + ch * TCH + half * 256) : "m0");
    static_for<0, 63>([&](auto R) { lds_put_tid<decltype(R)::value * TROW>(tv[decltype(R)::value]); });
    __builtin_amdgcn_s_setprio(PRC1);
  }
  fence_sched();
  handoff(16);
  lds_bar();
  asm volatile("" :: "s"(sd));

  f4 kk[2];
  v4u uv[2];
  auto uload = [&]() {
    fence_sched();
    wait_pin<0>(pf);
    asm volatile("" :: "v"(tch));
    if (__builtin_expect(flag_bad(pf, tag), 0)) flag_wait(vf, rws, tag);
    const u32 vo = WS_UT + (u32)((pr >> 1) * 256 + 64 * wave + (lane >> 1)) * 64 + ((pr & 1) * 2 + (lane & 1)) * 16;
    uv[0] = ld128_sc1<0>(vo, rws, 0); uv[1] = ld128_sc1<2048>(vo, rws, 0);
    fence_sched();
  };
  auto grp = [](int v) { return 2 * ((v ^ (v >> 1)) & 1) + (v >> 1); };
  {
    LAS char* tb = sm + ch * TCH + g * 16;
    const int ga = grp(n >> 2), ni = min(n & 3, 2);
    LAS char* pr_[2] = {tb + (half ? 41 + 5 * (ga >> 1) + 2 * (ga & 1) + ni : 32 + 2 * ga + ni) * TROW, tb + (half ? 51 + 3 * ga + ni : 16 + n) * TROW};
    LAS char* pe = tb + n * TROW;
    kk[0] = kk[1] = (f4)(0.f);
#pragma unroll
    for (int ks = 0; ks < 8; ++ks) {
      if (ks == 7) uload();
      const bf8 e = lds_rd128(pe + ks * 64);
#pragma unroll
      for (int i = 0; i < 2; ++i) kk[i] = mfma16(lds_rd128(pr_[i] + ks * 64), e, kk[i]);
    }
  }
  LAS char* kr = sm + ch * KCH;
  const int gd = grp(g);
  LAS char* pz = wave == 0 && lane >= 32 ? sm + UA0 + ((lane >> 3) & 1) * UCH + ((lane >> 2) & 1) * UBO + p * 16
                                         : kr + (half ? KOO + 4096 : 4096) + min(lane & 31, 28) * 16;
  LAS char* pw0 = kr - 32 * n - (half ? 1024 + 512 * (gd >> 1) + 16 * (gd & 1) : 512 + 8 * gd);
  LAS char* pw1 = kr - 32 * n - (half ? 2048 + 512 * gd : 8 * g);
  fence_sched();
  lds_bar();
  {
    *(volatile LAS v4u*)pz = (v4u){0, 0, 0, 0};
    const float a = opaque(kk[0][0]), b = opaque(kk[0][1]), c = opaque(kk[0][2]);
    const float k0 = opaque(kk[1][0]), k1 = opaque(kk[1][1]), k2 = opaque(kk[1][2]), k3 = opaque(kk[1][3]);
    if (half) {
      const u32 aa = pk2(k0, k0), bb = pk2(k1, k1), cc = pk2(k2, k2);
      *(volatile LAS v4u*)(pw0 + KOO + 4080) = (v4u){pk2(c, c), pk2(b, b), pk2(b, a), pk2(a, a)};
      *(volatile LAS v4u*)(pw0 + 4096) = (v4u){pk2(c, b), pk2(b, b), pk2(a, a), pk2(a, a)};
      *(volatile LAS v4u*)(pw1 + KOO + 4064) = (v4u){cc, cc, bb, bb};
      *(volatile LAS v4u*)(pw1 + KOO + 4080) = (v4u){bb, pk2(k1, k0), aa, aa};
      *(volatile LAS v4u*)(pw1 + 4080) = (v4u){cc, pk2(k2, k1), bb, bb};
      *(volatile LAS v4u*)(pw1 + 4096) = (v4u){bb, aa, aa, aa};
    } else {
      const u32 hi = pk2(k3, k2), lo = pk2(k1, k0);
      *(volatile LAS v2u*)(pw0 + KOO + 4088) = (v2u){pk2(c, b), pk2(a, a)};
      *(volatile LAS v2u*)(pw0 + 4104) = (v2u){pk2(b, a), pk2(a, a)};
      *(volatile LAS v2u*)(pw1 + KOO + 4088) = (v2u){hi, lo};
      *(volatile LAS u16*)(pw1 + 4102) = (u16)hi;
      *(volatile LAS u32*)(pw1 + 4104) = pk2(k2, k1);
      *(volatile LAS u16*)(pw1 + 4108) = (u16)(lo >> 16);
    }
  }
  wait_pin<0>(uv[0], uv[1]);
#pragma unroll
  for (int k = 0; k < 2; ++k) {
    LAS char* ub = sm + UA0 + (lane & 1) * UCH + 64 + (64 * wave + 32 * k + (lane >> 1)) * 16;
    *(LAS v4u*)(ub + UBO) = uv[k];
    *(LAS u32*)(ub - 4) = uv[k].x;
    *(LAS v3u*)(ub) = (v3u){uv[k].y, uv[k].z, uv[k].w};
  }
  lds_bar();
  flag_store(WS_EFLAG + (u32)(256 + ci) * 8, rws, tag);
  __builtin_amdgcn_s_setprio(PRCV);

  f4 y[8];
  {
    LAS char* pk = sm + ch * KCH + ((n & 1) ? KOO + 4032 : 4048) - 64 * (n >> 1) + 16 * g - 3584 + 256 * half;
    LAS char* pu = sm + UA0 + ch * UCH + (n < 8 ? 8 * n : UBO + 8 * (n - 8) + 8) + 16 * g + 256 * half;
    bf8 kf[4][8], uf[4][8];
    auto rd = [&](int rr, int i) {
      kf[rr][i] = lds_rd128(pk + 512 * (7 - i) + 64 * rr);
      const v2u lo = *(volatile LAS v2u*)(pu + 512 * i + 64 * rr), hi = *(volatile LAS v2u*)(pu + 512 * i + 64 * rr + 8);
      uf[rr][i] = __builtin_bit_cast(bf8, (v4u){lo.x, lo.y, hi.x, hi.y});
    };
#pragma unroll
    for (int i = 0; i < 8; ++i) { rd(0, i); fence_sched(); }
    static_for<0, 144>([&](auto N) {
      constexpr int nn = decltype(N)::value, rr = nn / 36, v = nn % 36;
      constexpr int s = v < 1 ? 0 : v < 3 ? 1 : v < 6 ? 2 : v < 10 ? 3 : v < 15 ? 4 : v < 21 ? 5 : v < 28 ? 6 : 7, m = v - s * (s + 1) / 2;
      if constexpr (rr == 0 && m == 0) y[s] = mfma16(kf[rr][s - m], uf[rr][m], (f4)(0.f));
      else y[s] = mfma16(kf[rr][s - m], uf[rr][m], y[s]);
      fence_sched();
      if constexpr (rr < 3 && v % 4 == 2 && v / 4 < 8) rd(rr + 1, v / 4);
      fence_sched();
    });
  }
  __builtin_amdgcn_s_setprio(PRCV);
#pragma unroll
  for (int a = 0; a < 8; ++a) *(LAS f4*)(sm + EX + (wave * 8 + a) * 1024 + lane * 16) = y[a];
  lds_bar();
  {
    const float d0 = bflo(r_dsk), d1 = bfhi(r_dsk);
    const int rn = n < 8 ? 2 * n : 2 * (n - 8) + 1;
#pragma unroll
    for (int i = 0; i < 2; ++i) {
      const int al = 2 * wave + i;
      LAS char* ex = sm + EX + al * 1024 + lane * 16;
      const f4 y0 = *(LAS f4*)(ex) + *(LAS f4*)(ex + 8192), y1 = *(LAS f4*)(ex + 16384) + *(LAS f4*)(ex + 24576);
      LAS char* up = sm + UA0 + UBO + 64 + (256 * al + 2 * rn + 64 * g) * 2;
      const u32 u00 = *(LAS u32*)(up), u01 = *(LAS u32*)(up + 64), u10 = *(LAS u32*)(up + UCH), u11 = *(LAS u32*)(up + UCH + 64);
      auto mix = [&](float y, float u, float d) { return opaque(__builtin_fmaf(u, d, opaque(y))); };
      const u32 x0 = pk2(mix(y0[0], bflo(u00), d0), mix(y1[0], bflo(u10), d1)), x1 = pk2(mix(y0[1], bfhi(u00), d0), mix(y1[1], bfhi(u10), d1));
      const u32 z0 = pk2(mix(y0[2], bflo(u01), d0), mix(y1[2], bflo(u11), d1)), z1 = pk2(mix(y0[3], bfhi(u01), d0), mix(y1[3], bfhi(u11), d1));
      const u32 vo = WS_A + (u32)(4 * al + g) * 131072 + pr * 512 + rn * 16;
      st128_sc1<0>((v4u){x0, tg32, x1, tg32}, vo, rws, 0);
      st128_sc1<256>((v4u){z0, tg32, z1, tg32}, vo, rws, 0);
    }
  }
  asm volatile(".Lpf_end2:");
  if (ci == 0 && wave == 0) {
#pragma unroll 1
    for (int it = 0; it < SPIN_MAX; ++it) {
      v4u f[4];
      static_for<0, 4>([&](auto I) { f[decltype(I)::value] = ld128_sc1<decltype(I)::value * 1024>(WS_EFLAG + lane * 16, rws, 0); });
      wait_pin<0>(f[0], f[1]); pin(f[2]); pin(f[3]);
      u32 m = 0;
#pragma unroll
      for (int i = 0; i < 4; ++i) m |= (f[i].x ^ (u32)tag) | (f[i].y ^ tg32) | (f[i].z ^ (u32)tag) | (f[i].w ^ tg32);
      if (!any(m)) break;
      __builtin_amdgcn_s_sleep(8);
    }
    flag_store(0, rws, tag);
  }
}

__global__ __launch_bounds__(256, 2) void fused_kernel(const u16* x, const u16* w1, const u16* b1, const u16* w2, const u16* b2, const u16* gw,
                                                       u16* out, char* ws, const u16* are, const u16* aim, const u16* bre, const u16* bim,
                                                       const u16* cre, const u16* cim, const u16* logdt, const u16* dskip) {
  __shared__ __attribute__((aligned(256))) char smem[81920];
  asm volatile(".Lpf_start:");
  karg_load();
  LAS char* sm = (LAS char*)smem;
  const int b = blockIdx.x;
  if (__builtin_expect(b < 224, 1)) gemm_role<false>(b, sm);
  else if (b < 256) gemm_role<true>(b, sm);
  else conv_role(b - 256, sm);
}

extern "C" void kernel_launch(void* const* d_in, const int* in_sizes, int n_in, void* d_out, int out_size, void* d_ws, size_t ws_size,
                              hipStream_t stream) {
  const u16* const* in = (const u16* const*)d_in;
  fused_kernel<<<512, 256, 0, stream>>>(in[0], in[10], in[11], in[12], in[13], in[14], (u16*)d_out, (char*)d_ws, in[1], in[2], in[3], in[4],
                                        in[5], in[6], in[7], in[8]);
}
```
